```python
import torch

def _softmax(x, axis=-1):
    return torch.softmax(x, dim=axis)
def _gelu(x):
    return torch.nn.functional.gelu(x, approximate="tanh")
def _silu(x):
    return torch.nn.functional.silu(x)
def _sigmoid(x):
    return torch.sigmoid(x)
def _layernorm(x, axis=-1, eps=1e-5):
    m = x.mean(dim=axis, keepdim=True)
    v = x.var(dim=axis, keepdim=True, unbiased=False)
    return (x - m) / torch.sqrt(v + eps)
def _scatter_add(tgt, idx, src, axis=0):
    return tgt.clone().index_add_(axis, idx, src)
def _segment_sum(src, seg, n):
    out = torch.zeros((n,) + src.shape[1:], dtype=src.dtype)
    return out.index_add_(0, seg, src)
def _pad(x, pw):
    flat = tuple(v for pair in reversed(pw) for v in pair)
    return torch.nn.functional.pad(x, flat)

kernel_name = 'recurrent_scan__gqa_block_cached__geglu_ffn__decode'

def setup_inputs():

    _w = (lambda a: (lambda t: t.to(torch.bfloat16) if t.is_floating_point() else t)(torch.as_tensor(a)))
    def _A_setup():
        B, Sq, D = 1, 1, 576
        Hq, Hkv, Sk, Dh = 32, 16, 65536, 128
        assert Hq % Hkv == 0, (Hq, Hkv)
        return {
            "x": _w((lambda *s: torch.randn(*s, dtype=torch.bfloat16) * 0.05)(B, Sq, D)),
            "k_cache": _w((lambda *s: torch.randn(*s, dtype=torch.bfloat16) * 1.6)(B, Hkv, Sk, Dh)),
            "v_cache": _w((lambda *s: torch.randn(*s, dtype=torch.bfloat16) * 0.2)(B, Hkv, Sk, Dh)),
            "wq": _w((lambda *s: torch.randn(*s, dtype=torch.bfloat16) * 1.6)(D, Hq * Dh) * (D ** -0.5)),
            "wo": _w((lambda *s: torch.randn(*s, dtype=torch.bfloat16) * 0.2)(Hq * Dh, D) * ((Hq * Dh) ** -0.5)),
        }
    _A_in = _A_setup()

    def _B_setup():
        B, S, D, F = 1, 1, 576, 4608
        return {
            "x": _w((lambda *s: torch.randn(*s, dtype=torch.bfloat16) * 0.05)(B, S, D)),
            "w_gate": _w((lambda *s: torch.randn(*s, dtype=torch.bfloat16) * 0.1)(D, F)),
            "w_up": _w((lambda *s: torch.randn(*s, dtype=torch.bfloat16) * 0.1)(D, F)),
            "w_down": _w((lambda *s: torch.randn(*s, dtype=torch.bfloat16) * 0.1)(F, D)),
        }
    _B_in = _B_setup()

    return {
        "A_x": _A_in["x"],
        "A_k_cache": _A_in["k_cache"],
        "A_v_cache": _A_in["v_cache"],
        "A_wq": _A_in["wq"],
        "A_wo": _A_in["wo"],
        "B_x": _B_in["x"],
        "B_w_gate": _B_in["w_gate"],
        "B_w_up": _B_in["w_up"],
        "B_w_down": _B_in["w_down"],
    }

def reference(A_x, A_k_cache, A_v_cache, A_wq, A_wo, B_x, B_w_gate, B_w_up, B_w_down):
    def _A_ref(x, k_cache, v_cache, wq, wo):
        B, Sq, D = x.shape
        Hq, Dh = 32, 128
        Hkv = k_cache.shape[1]
        rep = Hq // Hkv
        q = (lambda x, *d: x.permute(*d))((x @ wq).reshape(B, Sq, Hq, Dh), 0, 2, 1, 3)
        k = (lambda x, n, axis: torch.repeat_interleave(x, n, dim=axis))(k_cache, rep, axis=1)
        v = (lambda x, n, axis: torch.repeat_interleave(x, n, dim=axis))(v_cache, rep, axis=1)
        scores = torch.einsum("bhqd,bhkd->bhqk", q, k) * (Dh ** -0.5)
        p = _softmax(scores, axis=-1)
        o = torch.einsum("bhqk,bhkd->bhqd", p, v)
        return (lambda x, *d: x.permute(*d))(o, 0, 2, 1, 3).reshape(B, Sq, Hq * Dh) @ wo

    def _B_ref(x, w_gate, w_up, w_down):
        return (_gelu(x @ w_gate) * (x @ w_up)) @ w_down

    x = A_x
    xn = _layernorm(x, axis=-1)
    s = _A_ref(xn, A_k_cache, A_v_cache, A_wq, A_wo)
    g = _silu(_B_ref(xn, B_w_gate, B_w_up, B_w_down))
    return x + s * g
```

```cpp
#include <hip/hip_runtime.h>
#include <hip/hip_bf16.h>
#include <stdint.h>

namespace {

typedef unsigned v4u __attribute__((ext_vector_type(4)));
typedef unsigned v2u __attribute__((ext_vector_type(2)));
typedef float f32x4 __attribute__((ext_vector_type(4)));
typedef float f32x2 __attribute__((ext_vector_type(2)));
typedef __bf16 bf16x8 __attribute__((ext_vector_type(8)));
typedef __bf16 bf16x2 __attribute__((ext_vector_type(2)));
#define GAS __attribute__((address_space(1)))
#define RLX_AGENT __ATOMIC_RELAXED, __HIP_MEMORY_SCOPE_AGENT

#define KNB 1
#define MIMIC 1

constexpr int DM = 576, SK = 65536, FF = 4608, QD = 4096;
constexpr int NWG = 256, NW = 8, NTHR = NW * 64;
constexpr int NRED = 36;
constexpr int NFW = 192;
constexpr int TL = 4;
constexpr int NB = KNB;
constexpr int TILES_W = 4096 / 16 / NW;

constexpr int WS_EPOCH = 0;
constexpr int WS_QG = 4096;
constexpr int WS_P1M = WS_QG + 16 * 128 * 8;
constexpr int WS_P1O = WS_P1M + 16 * 2 * 16 * 16;
constexpr int WS_P2 = WS_P1O + 16 * 16 * 16 * 128;
constexpr int WS_PF = WS_P2 + NRED * NWG * 128;
constexpr int WS_END = WS_PF + NRED * NFW * 128;

constexpr int AUX_NT = 2, AUX_SC1 = 16;
constexpr int RSRC_W3 = 0x00020000;
constexpr unsigned SPIN_CAP = 1u << 18;

constexpr int L_XN = 0;
constexpr int L_RED = 576;
constexpr int L_H = L_RED + 512;
constexpr int L_O = L_H + 32;
constexpr int L_ML = L_O + 16;
constexpr int L_ACC = L_ML + 32;
constexpr int L_FIN = L_ACC + 2048;
constexpr int L_FLOATS = 24 * 1024;

template <int CTRL> __device__ __forceinline__ float dpp(float x) {
  return __builtin_bit_cast(float, __builtin_amdgcn_mov_dpp(__builtin_bit_cast(int, x), CTRL, 0xf, 0xf, true));
}
template <int CTRL> __device__ __forceinline__ unsigned dppu(unsigned x) {
  return (unsigned)__builtin_amdgcn_mov_dpp((int)x, CTRL, 0xf, 0xf, true);
}
constexpr int XOR1 = 0xB1, XOR2 = 0x4E, XOR7 = 0x141, XOR8 = 0x128, ROR4 = 0x124;
__device__ __forceinline__ float sum_x16(float x) {
  auto s = __builtin_amdgcn_permlane16_swap(__float_as_uint(x), __float_as_uint(x), false, false);
  return __uint_as_float(s[0]) + __uint_as_float(s[1]);
}
__device__ __forceinline__ float sum_x32(float x) {
  auto s = __builtin_amdgcn_permlane32_swap(__float_as_uint(x), __float_as_uint(x), false, false);
  return __uint_as_float(s[0]) + __uint_as_float(s[1]);
}
__device__ __forceinline__ float max_x16(float x) {
  auto s = __builtin_amdgcn_permlane16_swap(__float_as_uint(x), __float_as_uint(x), false, false);
  return fmaxf(__uint_as_float(s[0]), __uint_as_float(s[1]));
}
__device__ __forceinline__ float max_x32(float x) {
  auto s = __builtin_amdgcn_permlane32_swap(__float_as_uint(x), __float_as_uint(x), false, false);
  return fmaxf(__uint_as_float(s[0]), __uint_as_float(s[1]));
}
__device__ __forceinline__ float wave_sum(float x) {
  x += dpp<XOR1>(x); x += dpp<XOR2>(x); x += dpp<ROR4>(x); x += dpp<XOR8>(x);
  return sum_x32(sum_x16(x));
}
__device__ __forceinline__ unsigned pk_bf16(float lo, float hi) {
  bf16x2 v = {(__bf16)lo, (__bf16)hi};
  return __builtin_bit_cast(unsigned, v);
}
__device__ __forceinline__ float bfr(float x) { return (float)(__bf16)x; }
__device__ __forceinline__ float rnd(float x) { return MIMIC ? bfr(x) : x; }
__device__ __forceinline__ float lo_f(unsigned u) { return __uint_as_float(u << 16); }
__device__ __forceinline__ float hi_f(unsigned u) { return __uint_as_float(u & 0xffff0000u); }
__device__ __forceinline__ float dot2(unsigned a, unsigned b, float c) {
  return __builtin_amdgcn_fdot2_f32_bf16(__builtin_bit_cast(bf16x2, a), __builtin_bit_cast(bf16x2, b), c, false);
}
__device__ __forceinline__ __amdgpu_buffer_rsrc_t mk_rsrc(const void* p, int bytes) {
  return __builtin_amdgcn_make_buffer_rsrc((void*)p, 0, bytes, RSRC_W3);
}
__device__ __forceinline__ float rdlane(float x, int l) { return __uint_as_float(__builtin_amdgcn_readlane(__float_as_uint(x), l)); }

__global__ void __launch_bounds__(NTHR, 2)
fused_decode(const unsigned short* __restrict__ xg, const unsigned short* __restrict__ kg, const unsigned short* __restrict__ vg,
             const unsigned short* __restrict__ wq, const unsigned short* __restrict__ wo, const unsigned short* __restrict__ wgate,
             const unsigned short* __restrict__ wup, const unsigned short* __restrict__ wdown, unsigned short* __restrict__ out,
             unsigned char* __restrict__ ws) {
  __shared__ __attribute__((aligned(16))) float lds[L_FLOATS];
  const int tid = threadIdx.x, lane = tid & 63, w = __builtin_amdgcn_readfirstlane(tid >> 6);
  const int b = blockIdx.x, g = b >> 4, s = b & 15;
  const int x8 = b & 7, k32 = b >> 3, qpos = (x8 >> 2) * 2;
  const bool isq = (k32 & 3) == qpos;
  const int fidx = x8 * 24 + (k32 >> 2) * 3 + (k32 & 3) - ((k32 & 3) > qpos ? 1 : 0);
  const int c = lane & 15;

  const __amdgpu_buffer_rsrc_t rws = mk_rsrc(ws, WS_END);
  const __amdgpu_buffer_rsrc_t rx = mk_rsrc(xg, DM * 2);
  const __amdgpu_buffer_rsrc_t rk = mk_rsrc(kg + (size_t)g * SK * 128, SK * 256);
  const __amdgpu_buffer_rsrc_t rv = mk_rsrc(vg + (size_t)g * SK * 128, SK * 256);

  const unsigned tag = __builtin_amdgcn_raw_buffer_load_b32(rws, WS_EPOCH, 0, AUX_SC1) + 1u;
  unsigned xd[5];
#pragma unroll
  for (int k = 0; k < 5; ++k) xd[k] = __builtin_amdgcn_raw_buffer_load_b32(rx, lane * 4 + k * 256, 0, 0);
  int row0;
  const GAS unsigned char* wp;
  unsigned wstride;
  if (isq) {
    row0 = 72 * w + (lane >> 3);
    wp = (const GAS unsigned char*)(wq + (size_t)row0 * QD + g * 256 + (s & 3) * 64 + (lane & 7) * 8);
    wstride = 8 * QD * 2;
  } else {
    row0 = 72 * w + ((lane & 31) >> 2);
    const int ch = (lane & 3) < 3 ? (lane & 3) : 2;
    wp = (const GAS unsigned char*)((lane < 32 ? wgate : wup) + (size_t)row0 * FF + fidx * 24 + ch * 8);
    wstride = 8 * FF * 2;
  }
  v4u wv[9];
#pragma unroll
  for (int i = 0; i < 9; ++i) wv[i] = __builtin_nontemporal_load((const GAS v4u*)(wp + (size_t)i * wstride));
  const int voff = lane * 16;
  const int tbase = (s * 256 + w) * 4096;
  constexpr int TSTEP = NW * 4096;
  v4u kb[NB][TL], vb[NB][TL];
#pragma unroll
  for (int n = 0; n < NB; ++n) {
#pragma unroll
    for (int i = 0; i < TL; ++i) kb[n][i] = __builtin_amdgcn_raw_buffer_load_b128(rk, voff + i * 1024, tbase + n * TSTEP, AUX_NT);
#pragma unroll
    for (int i = 0; i < TL; ++i) vb[n][i] = __builtin_amdgcn_raw_buffer_load_b128(rv, voff + i * 1024, tbase + n * TSTEP, AUX_NT);
  }
  __builtin_amdgcn_sched_barrier(0);

  {
    float xv[10];
#pragma unroll
    for (int k = 0; k < 5; ++k) { xv[2 * k] = lo_f(xd[k]); xv[2 * k + 1] = hi_f(xd[k]); }
    float sm = 0.f;
#pragma unroll
    for (int k = 0; k < 10; ++k) sm += xv[k];
    const float mean = wave_sum(sm) * (1.0f / DM);
    float sq = 0.f;
#pragma unroll
    for (int k = 0; k < 8; ++k) { const float d = xv[k] - mean; sq = fmaf(d, d, sq); }
    if (lane < 32) {
#pragma unroll
      for (int k = 8; k < 10; ++k) { const float d = xv[k] - mean; sq = fmaf(d, d, sq); }
    }
    const float var = wave_sum(sq) * (1.0f / DM);
    const float mb = rnd(mean);
    const float den = rnd(sqrtf(rnd(rnd(var) + 1e-5f)));
#pragma unroll
    for (int k = 0; k < 5; ++k) {
      if (k < 4 || lane < 32) {
        f32x2 v = {rnd(rnd(xv[2 * k] - mb) / den), rnd(rnd(xv[2 * k + 1] - mb) / den)};
        *reinterpret_cast<f32x2*>(&lds[L_XN + 2 * (lane + 64 * k)]) = v;
      }
    }
  }
  __builtin_amdgcn_fence(__ATOMIC_RELEASE, "wavefront");
  __builtin_amdgcn_wave_barrier();
  {
    float a8[8];
#pragma unroll
    for (int j = 0; j < 8; ++j) a8[j] = 0.f;
#pragma unroll
    for (int i = 0; i < 9; ++i) {
      const float xr = lds[L_XN + row0 + 8 * i];
#pragma unroll
      for (int j = 0; j < 4; ++j) { a8[2 * j] = fmaf(xr, lo_f(wv[i][j]), a8[2 * j]); a8[2 * j + 1] = fmaf(xr, hi_f(wv[i][j]), a8[2 * j + 1]); }
    }
    if (isq) {
#pragma unroll
      for (int j = 0; j < 8; ++j) { float t = a8[j]; t += dpp<XOR8>(t); a8[j] = sum_x32(sum_x16(t)); }
      if (lane < 8) {
        *reinterpret_cast<f32x4*>(&lds[L_RED + w * 64 + lane * 8]) = f32x4{a8[0], a8[1], a8[2], a8[3]};
        *reinterpret_cast<f32x4*>(&lds[L_RED + w * 64 + lane * 8 + 4]) = f32x4{a8[4], a8[5], a8[6], a8[7]};
      }
    } else {
#pragma unroll
      for (int j = 0; j < 8; ++j) { float t = a8[j]; t += dpp<ROR4>(t); t += dpp<XOR8>(t); a8[j] = sum_x16(t); }
      if ((lane & 31) < 3) {
        const int o = L_RED + w * 64 + (lane >> 5) * 32 + (lane & 31) * 8;
        *reinterpret_cast<f32x4*>(&lds[o]) = f32x4{a8[0], a8[1], a8[2], a8[3]};
        *reinterpret_cast<f32x4*>(&lds[o + 4]) = f32x4{a8[4], a8[5], a8[6], a8[7]};
      }
    }
  }
  __syncthreads();
  if (isq) {
    if (tid < 32) {
      float q0 = 0.f, q1 = 0.f;
#pragma unroll
      for (int ww = 0; ww < NW; ++ww) { const f32x2 v = *reinterpret_cast<const f32x2*>(&lds[L_RED + ww * 64 + 2 * tid]); q0 += v.x; q1 += v.y; }
      __builtin_amdgcn_raw_buffer_store_b64(v2u{pk_bf16(q0, q1), tag}, rws, WS_QG + (g * 128 + (s & 3) * 32 + tid) * 8, 0, AUX_SC1);
    }
  } else if (tid < 24) {
    float gt = 0.f, up = 0.f;
#pragma unroll
    for (int ww = 0; ww < NW; ++ww) { gt += lds[L_RED + ww * 64 + tid]; up += lds[L_RED + ww * 64 + 32 + tid]; }
    gt = rnd(gt); up = rnd(up);
    const float inner = 0.7978845608028654f * (gt + 0.044715f * gt * gt * gt);
    const float ge = rnd(0.5f * gt * (1.0f + tanhf(inner)));
    lds[L_H + tid] = rnd(ge * up);
  }

  unsigned qf[4], qs[4];
  {
    const int hf = (c >> 2) & 1;
    const int of = WS_QG + (g * 128 + hf * 64 + 4 * c) * 8, os = WS_QG + (g * 128 + (hf ^ 1) * 64 + 4 * c) * 8;
    for (unsigned spins = 0;; ++spins) {
      asm volatile("" ::: "memory");
      const v4u a0 = __builtin_amdgcn_raw_buffer_load_b128(rws, of, 0, AUX_SC1);
      const v4u a1 = __builtin_amdgcn_raw_buffer_load_b128(rws, of + 16, 0, AUX_SC1);
      const v4u b0 = __builtin_amdgcn_raw_buffer_load_b128(rws, os, 0, AUX_SC1);
      const v4u b1 = __builtin_amdgcn_raw_buffer_load_b128(rws, os + 16, 0, AUX_SC1);
      qf[0] = a0.x; qf[1] = a0.z; qf[2] = a1.x; qf[3] = a1.z;
      qs[0] = b0.x; qs[1] = b0.z; qs[2] = b1.x; qs[3] = b1.z;
      const bool ok = a0.y == tag && a0.w == tag && a1.y == tag && a1.w == tag && b0.y == tag && b0.w == tag && b1.y == tag && b1.w == tag;
      if (__all(ok) || spins > SPIN_CAP) break;
      __builtin_amdgcn_s_sleep(8);
    }
  }

  constexpr float LOG2E = 1.4426950408889634f, SCALE = 0.08838834764831845f;
  float m = -INFINITY, lsum = 0.f;
  f32x4 acc = {0.f, 0.f, 0.f, 0.f};
  unsigned amask[4];
  {
    const int jp = (c & 3) + 4 * (c >> 3);
#pragma unroll
    for (int r = 0; r < 4; ++r) amask[r] = (jp >> 1) == r ? ((jp & 1) ? 0xffff0000u : 0x0000ffffu) : 0u;
  }
  const bool b0 = (c & 1) != 0, b1 = (c & 2) != 0;
  for (int t = 0; t < TILES_W; t += NB) {
#pragma unroll
    for (int n = 0; n < NB; ++n) {
      const int nxt = (t + n + NB < TILES_W) ? tbase + (t + n + NB) * TSTEP : 0x70000000;
      float y[4];
#pragma unroll
      for (int i = 0; i < TL; ++i) {
        float xf = 0.f, xs = 0.f;
#pragma unroll
        for (int j = 0; j < 4; ++j) { xf = dot2(kb[n][i][j], qf[j], xf); xs = dot2(kb[n][i][j], qs[j], xs); }
        y[i] = xf + dpp<XOR7>(xs);
      }
      __builtin_amdgcn_sched_barrier(0);
#pragma unroll
      for (int i = 0; i < TL; ++i) kb[n][i] = __builtin_amdgcn_raw_buffer_load_b128(rk, voff + i * 1024, nxt, AUX_NT);
      __builtin_amdgcn_sched_barrier(0);
      const float u0 = (b0 ? y[1] : y[0]) + dpp<XOR1>(b0 ? y[0] : y[1]);
      const float u1 = (b0 ? y[3] : y[2]) + dpp<XOR1>(b0 ? y[2] : y[3]);
      float sc = (b1 ? u1 : u0) + dpp<XOR2>(b1 ? u0 : u1);
      sc += dpp<XOR8>(sc);
      float tl2;
      if (MIMIC) tl2 = bfr(bfr(sc) * SCALE) * LOG2E; else tl2 = sc * (SCALE * LOG2E);
      if (__any(tl2 > m)) {
        float tm = tl2;
        tm = fmaxf(tm, dpp<XOR1>(tm)); tm = fmaxf(tm, dpp<XOR2>(tm));
        tm = max_x32(max_x16(tm));
        const float mn = fmaxf(m, tm);
        const float alpha = __builtin_amdgcn_exp2f(m - mn);
        m = mn;
        lsum *= alpha;
        const float a0 = rdlane(alpha, 0), a1 = rdlane(alpha, 4);
        const float ad = (lane & 16) ? a1 : a0;
        acc *= ad;
      }
      const float p = __builtin_amdgcn_exp2f(tl2 - m);
      lsum += p;
      const unsigned pp = pk_bf16(p, p);
#pragma unroll
      for (int i = 0; i < TL; ++i) {
        const unsigned pb = i == 0 ? dppu<0x00>(pp) : i == 1 ? dppu<0x55>(pp) : i == 2 ? dppu<0xAA>(pp) : dppu<0xFF>(pp);
        const v4u av = {pb & amask[0], pb & amask[1], pb & amask[2], pb & amask[3]};
        acc = __builtin_amdgcn_mfma_f32_16x16x32_bf16(__builtin_bit_cast(bf16x8, av), __builtin_bit_cast(bf16x8, vb[n][i]), acc, 0, 0, 0);
      }
      __builtin_amdgcn_sched_barrier(0);
#pragma unroll
      for (int i = 0; i < TL; ++i) vb[n][i] = __builtin_amdgcn_raw_buffer_load_b128(rv, voff + i * 1024, nxt, AUX_NT);
      __builtin_amdgcn_sched_barrier(0);
    }
  }

  unsigned wod[16], wdd[24];
  {
    const __amdgpu_buffer_rsrc_t rwo = mk_rsrc(wo + (size_t)b * 16 * DM, 16 * DM * 2);
    const __amdgpu_buffer_rsrc_t rwd = mk_rsrc(wdown + (size_t)(isq ? 0 : fidx) * 24 * DM, isq ? 0 : 24 * DM * 2);
    const int o = tid < 288 ? tid * 4 : 0x70000000;
    if (w < 5) {
#pragma unroll
      for (int j = 0; j < 24; ++j) wdd[j] = __builtin_amdgcn_raw_buffer_load_b32(rwd, o, j * DM * 2, AUX_NT);
#pragma unroll
      for (int j = 0; j < 16; ++j) wod[j] = __builtin_amdgcn_raw_buffer_load_b32(rwo, o, j * DM * 2, AUX_NT);
    }
  }
  __builtin_amdgcn_sched_barrier(0);
  {
    float l = lsum;
    l += dpp<XOR1>(l); l += dpp<XOR2>(l);
    l = sum_x32(sum_x16(l));
    if ((lane & 59) == 0) { *reinterpret_cast<f32x2*>(&lds[L_ML + w * 4 + (lane >> 2) * 2]) = f32x2{m, l}; }
    const int hh = (lane >> 4) & 1, dd = 8 * c + 4 * (lane >> 5);
    *reinterpret_cast<f32x4*>(&lds[L_ACC + w * 256 + hh * 128 + dd]) = acc;
  }
  __syncthreads();
  if (tid < 256) {
    const int hh = tid >> 7;
    float mw[NW], lw[NW];
#pragma unroll
    for (int ww = 0; ww < NW; ++ww) { const f32x2 v = *reinterpret_cast<const f32x2*>(&lds[L_ML + ww * 4 + hh * 2]); mw[ww] = v.x; lw[ww] = v.y; }
    float M = mw[0];
#pragma unroll
    for (int ww = 1; ww < NW; ++ww) M = fmaxf(M, mw[ww]);
    float L = 0.f, o = 0.f;
#pragma unroll
    for (int ww = 0; ww < NW; ++ww) { const float e = __builtin_amdgcn_exp2f(mw[ww] - M); L = fmaf(e, lw[ww], L); o = fmaf(e, lds[L_ACC + ww * 256 + tid], o); }
    __builtin_amdgcn_raw_buffer_store_b64(v2u{__float_as_uint(o), tag}, rws, WS_P1O + (((g * 16 + (tid >> 4)) * 16 + s) * 16 + (tid & 15)) * 8, 0, AUX_SC1);
    if ((tid & 127) == 0)
      __builtin_amdgcn_raw_buffer_store_b128(v4u{__float_as_uint(M), tag, __float_as_uint(L), tag}, rws, WS_P1M + ((g * 2 + hh) * 16 + s) * 16, 0, AUX_SC1);
  }
  if (!isq && tid < 288) {
    float y0 = 0.f, y1 = 0.f;
#pragma unroll
    for (int j = 0; j < 24; ++j) { const float hv = lds[L_H + j]; y0 = fmaf(hv, lo_f(wdd[j]), y0); y1 = fmaf(hv, hi_f(wdd[j]), y1); }
    __builtin_amdgcn_raw_buffer_store_b128(v4u{__float_as_uint(y0), tag, __float_as_uint(y1), tag}, rws, WS_PF + ((tid >> 3) * NFW + fidx) * 128 + (tid & 7) * 16, 0, AUX_SC1);
  }
  if (w == 7) {
    const int pr = lane >> 2, part = lane & 3;
    const int oo = WS_P1O + ((g * 16 + s) * 16 + pr) * 128 + part * 32, om = WS_P1M + ((g * 2 + (s >> 3)) * 16 + pr) * 16;
    v4u a0, a1, ml;
    for (unsigned spins = 0;; ++spins) {
      asm volatile("" ::: "memory");
      a0 = __builtin_amdgcn_raw_buffer_load_b128(rws, oo, 0, AUX_SC1);
      a1 = __builtin_amdgcn_raw_buffer_load_b128(rws, oo + 16, 0, AUX_SC1);
      ml = __builtin_amdgcn_raw_buffer_load_b128(rws, om, 0, AUX_SC1);
      const bool ok = a0.y == tag && a0.w == tag && a1.y == tag && a1.w == tag && ml.y == tag && ml.w == tag;
      if (__all(ok) || spins > SPIN_CAP) break;
      __builtin_amdgcn_s_sleep(2);
    }
    const float mp = __uint_as_float(ml.x);
    float M = mp;
    M = fmaxf(M, dpp<ROR4>(M)); M = fmaxf(M, dpp<XOR8>(M)); M = max_x32(max_x16(M));
    const float e = __builtin_amdgcn_exp2f(mp - M);
    float v5[5] = {e * __uint_as_float(a0.x), e * __uint_as_float(a0.z), e * __uint_as_float(a1.x), e * __uint_as_float(a1.z), e * __uint_as_float(ml.z)};
#pragma unroll
    for (int j = 0; j < 5; ++j) { float t = v5[j]; t += dpp<ROR4>(t); t += dpp<XOR8>(t); v5[j] = sum_x32(sum_x16(t)); }
    const float inv = 1.0f / v5[4];
    if (lane < 4) *reinterpret_cast<f32x4*>(&lds[L_O + lane * 4]) = f32x4{rnd(v5[0] * inv), rnd(v5[1] * inv), rnd(v5[2] * inv), rnd(v5[3] * inv)};
  }
  __syncthreads();
  if (tid < 288) {
    float y0 = 0.f, y1 = 0.f;
#pragma unroll
    for (int j = 0; j < 16; ++j) { const float ov = lds[L_O + j]; y0 = fmaf(ov, lo_f(wod[j]), y0); y1 = fmaf(ov, hi_f(wod[j]), y1); }
    __builtin_amdgcn_raw_buffer_store_b128(v4u{__float_as_uint(y0), tag, __float_as_uint(y1), tag}, rws, WS_P2 + ((tid >> 3) * NWG + b) * 128 + (tid & 7) * 16, 0, AUX_SC1);
  }
  if (b >= NRED) return;
  {
    const int r = b;
    float f0, f1, s0, s1;
    for (unsigned spins = 0;; ++spins) {
      asm volatile("" ::: "memory");
      v4u pf[3];
#pragma unroll
      for (int k = 0; k < 3; ++k) pf[k] = __builtin_amdgcn_raw_buffer_load_b128(rws, WS_PF + r * NFW * 128 + k * 8192 + tid * 16, 0, AUX_SC1);
      bool ok = true; f0 = 0.f; f1 = 0.f;
#pragma unroll
      for (int k = 0; k < 3; ++k) { ok = ok && pf[k].y == tag && pf[k].w == tag; f0 += __uint_as_float(pf[k].x); f1 += __uint_as_float(pf[k].z); }
      if (__all(ok) || spins > SPIN_CAP) break;
      __builtin_amdgcn_s_sleep(2);
    }
    for (unsigned spins = 0;; ++spins) {
      asm volatile("" ::: "memory");
      v4u p2[4];
#pragma unroll
      for (int k = 0; k < 4; ++k) p2[k] = __builtin_amdgcn_raw_buffer_load_b128(rws, WS_P2 + r * NWG * 128 + k * 8192 + tid * 16, 0, AUX_SC1);
      bool ok = true; s0 = 0.f; s1 = 0.f;
#pragma unroll
      for (int k = 0; k < 4; ++k) { ok = ok && p2[k].y == tag && p2[k].w == tag; s0 += __uint_as_float(p2[k].x); s1 += __uint_as_float(p2[k].z); }
      if (__all(ok) || spins > SPIN_CAP) break;
      __builtin_amdgcn_s_sleep(2);
    }
    float v4[4] = {s0, s1, f0, f1};
#pragma unroll
    for (int j = 0; j < 4; ++j) { float t = v4[j]; t += dpp<XOR8>(t); v4[j] = sum_x32(sum_x16(t)); }
    if (lane < 8) *reinterpret_cast<f32x4*>(&lds[L_FIN + w * 32 + lane * 4]) = f32x4{v4[0], v4[1], v4[2], v4[3]};
    __syncthreads();
    if (tid < 8) {
      f32x4 t = {0.f, 0.f, 0.f, 0.f};
#pragma unroll
      for (int ww = 0; ww < NW; ++ww) t += *reinterpret_cast<const f32x4*>(&lds[L_FIN + ww * 32 + tid * 4]);
      const unsigned xp = *reinterpret_cast<const unsigned*>(xg + r * 16 + tid * 2);
      float o2[2];
#pragma unroll
      for (int j = 0; j < 2; ++j) {
        const float sv = rnd(t[j]), yv = rnd(t[2 + j]);
        const float gv = rnd(yv / (1.0f + __expf(-yv)));
        o2[j] = (j ? hi_f(xp) : lo_f(xp)) + rnd(sv * gv);
      }
      *reinterpret_cast<unsigned*>(out + r * 16 + tid * 2) = pk_bf16(o2[0], o2[1]);
    }
    if (b == 0 && tid == 0) __builtin_amdgcn_raw_buffer_store_b32(tag, rws, WS_EPOCH, 0, AUX_SC1);
  }
}

}

extern "C" void kernel_launch(void* const* d_in, const int* in_sizes, int n_in, void* d_out, int out_size, void* d_ws, size_t ws_size,
                              hipStream_t stream) {
  (void)in_sizes; (void)n_in; (void)out_size; (void)ws_size;
  fused_decode<<<NWG, NTHR, 0, stream>>>((const unsigned short*)d_in[0], (const unsigned short*)d_in[1], (const unsigned short*)d_in[2],
                                         (const unsigned short*)d_in[3], (const unsigned short*)d_in[4], (const unsigned short*)d_in[6],
                                         (const unsigned short*)d_in[7], (const unsigned short*)d_in[8], (unsigned short*)d_out,
                                         (unsigned char*)d_ws);
}
```

```cpp
#include <hip/hip_runtime.h>
#include <hip/hip_bf16.h>
#include <stdint.h>

namespace {

typedef unsigned v4u __attribute__((ext_vector_type(4)));
typedef unsigned v2u __attribute__((ext_vector_type(2)));
typedef float f32x4 __attribute__((ext_vector_type(4)));
typedef float f32x2 __attribute__((ext_vector_type(2)));
typedef __bf16 bf16x8 __attribute__((ext_vector_type(8)));
typedef __bf16 bf16x2 __attribute__((ext_vector_type(2)));
#define GAS __attribute__((address_space(1)))
#define LAS __attribute__((address_space(3)))

#define KNSW 3
#define KDM 1
#define KWSEND 0
#define KKF (KNSW == 3 ? 38 : 29)
#define KKO (KNSW == 3 ? 41 : 31)
#define MIMIC 1

constexpr int DM = 576, SK = 65536, FF = 4608, QD = 4096;
constexpr int NWG = 256, NW = 8, NTHR = (NW + 1) * 64;
constexpr int NSW = KNSW;
constexpr int NHELP = NW - NSW;
constexpr int NRED = 36;
constexpr int NFW = 72;
constexpr int TL = 4;
constexpr int K_F = KKF, K_O = KKO;
constexpr int OOB = 0x70000000;

constexpr int WS_EPOCH = 0;
constexpr int WS_QG = 4096;
constexpr int WS_P1M = WS_QG + 16 * 4 * 256 * 8;
constexpr int WS_P1O = WS_P1M + 16 * 2 * 16 * 16;
constexpr int WS_P2 = WS_P1O + 16 * 16 * 16 * 128;
constexpr int WS_PF = WS_P2 + NRED * NWG * 128;
constexpr int WS_END = WS_PF + NRED * NFW * 128;

#define STAMP_DECL do {} while (0)
#define STAMP(i) do {} while (0)
#define STAMP_FLUSH() do {} while (0)
constexpr int AUX_NT = 2, AUX_SC1 = 16;
constexpr int RSRC_W3 = 0x00020000;
constexpr unsigned SPIN_CAP = 1u << 14;

constexpr int L_WO = 0;
constexpr int L_FR = L_WO + 4608;
constexpr int L_YF = L_FR + 1152;
constexpr int L_O = L_YF + 768;
constexpr int L_ML = L_O + 16;
constexpr int L_ACC = L_ML + 32;
constexpr int L_Q = L_ACC + 2048;
constexpr int L_TAG = L_Q + 128;
constexpr int L_CNT = L_TAG + 1, L_LN = L_TAG + 2;
constexpr int L_SC = L_TAG + 4;
constexpr int L_FLOATS = 24 * 1024;
static_assert(L_SC + 288 * 32 <= 22 * 1024, "LDS map");

template <int CTRL> __device__ __forceinline__ float dpp(float x) {
  return __builtin_bit_cast(float, __builtin_amdgcn_mov_dpp(__builtin_bit_cast(int, x), CTRL, 0xf, 0xf, true));
}
template <int CTRL> __device__ __forceinline__ unsigned dppu(unsigned x) {
  return (unsigned)__builtin_amdgcn_mov_dpp((int)x, CTRL, 0xf, 0xf, true);
}
constexpr int XOR1 = 0xB1, XOR2 = 0x4E, XOR7 = 0x141, XOR8 = 0x128, ROR4 = 0x124;
__device__ __forceinline__ float sum_x16(float x) {
  auto s = __builtin_amdgcn_permlane16_swap(__float_as_uint(x), __float_as_uint(x), false, false);
  return __uint_as_float(s[0]) + __uint_as_float(s[1]);
}
__device__ __forceinline__ float sum_x32(float x) {
  auto s = __builtin_amdgcn_permlane32_swap(__float_as_uint(x), __float_as_uint(x), false, false);
  return __uint_as_float(s[0]) + __uint_as_float(s[1]);
}
__device__ __forceinline__ float max_x16(float x) {
  auto s = __builtin_amdgcn_permlane16_swap(__float_as_uint(x), __float_as_uint(x), false, false);
  return fmaxf(__uint_as_float(s[0]), __uint_as_float(s[1]));
}
__device__ __forceinline__ float max_x32(float x) {
  auto s = __builtin_amdgcn_permlane32_swap(__float_as_uint(x), __float_as_uint(x), false, false);
  return fmaxf(__uint_as_float(s[0]), __uint_as_float(s[1]));
}
__device__ __forceinline__ float wave_sum(float x) {
  x += dpp<XOR1>(x); x += dpp<XOR2>(x); x += dpp<ROR4>(x); x += dpp<XOR8>(x);
  return sum_x32(sum_x16(x));
}
__device__ __forceinline__ unsigned pk_bf16(float lo, float hi) {
  bf16x2 v = {(__bf16)lo, (__bf16)hi};
  return __builtin_bit_cast(unsigned, v);
}
__device__ __forceinline__ float bfr(float x) { return (float)(__bf16)x; }
__device__ __forceinline__ float rnd(float x) { return MIMIC ? bfr(x) : x; }
__device__ __forceinline__ float lo_f(unsigned u) { return __uint_as_float(u << 16); }
__device__ __forceinline__ float hi_f(unsigned u) { return __uint_as_float(u & 0xffff0000u); }
__device__ __forceinline__ float dot2(unsigned a, unsigned b, float c) {
  return __builtin_amdgcn_fdot2_f32_bf16(__builtin_bit_cast(bf16x2, a), __builtin_bit_cast(bf16x2, b), c, false);
}
__device__ __forceinline__ __amdgpu_buffer_rsrc_t mk_rsrc(const void* p, int bytes) {
  return __builtin_amdgcn_make_buffer_rsrc((void*)p, 0, bytes, RSRC_W3);
}
__device__ __forceinline__ void glds16(const void* gsrc, unsigned lds_dst) {
  unsigned keep;
  asm volatile("s_mov_b32 %0, m0\n\ts_mov_b32 m0, %2\n\ts_nop 0\n\tglobal_load_lds_dwordx4 %1, off nt\n\ts_mov_b32 m0, %0"
               : "=&s"(keep) : "v"(gsrc), "s"(lds_dst) : "memory");
}
__device__ __forceinline__ float rdlane(float x, int l) { return __uint_as_float(__builtin_amdgcn_readlane(__float_as_uint(x), l)); }

__device__ __forceinline__ v4u gemv_a(unsigned xx, const unsigned (&gm)[4]) { return v4u{xx & gm[0], xx & gm[1], xx & gm[2], xx & gm[3]}; }
__device__ __forceinline__ f32x4 mfma16(v4u a, v4u bm, f32x4 cacc) {
  return __builtin_amdgcn_mfma_f32_16x16x32_bf16(__builtin_bit_cast(bf16x8, a), __builtin_bit_cast(bf16x8, bm), cacc, 0, 0, 0);
}
__device__ __forceinline__ f32x4 gemv_fin(f32x4 d, int lane) {
  const bool valid = (lane >> 5) == ((lane >> 3) & 1);
#pragma unroll
  for (int r = 0; r < 4; ++r) { float t = valid ? d[r] : 0.f; t += dpp<XOR8>(t); d[r] = sum_x32(t); }
  return d;
}
constexpr unsigned ffn_mask() { unsigned m = 0; for (int i = 0; i < 32; ++i) m |= (((i * 9) & 31) < 9 ? 1u : 0u) << i; return m; }

__global__ void __launch_bounds__(NTHR, 3)
fused_decode(const unsigned short* __restrict__ xg, const unsigned short* __restrict__ kg, const unsigned short* __restrict__ vg,
             const unsigned short* __restrict__ wq, const unsigned short* __restrict__ wo, const unsigned short* __restrict__ wgate,
             const unsigned short* __restrict__ wup, const unsigned short* __restrict__ wdown, unsigned short* __restrict__ out,
             unsigned char* __restrict__ ws) {
  __shared__ __attribute__((aligned(16))) float lds[L_FLOATS];
  asm volatile("" :: "s"(xg), "s"(kg), "s"(vg), "s"(wq), "s"(wo), "s"(ws));
  const int lane = threadIdx.x & 63, pw = __builtin_amdgcn_readfirstlane(threadIdx.x >> 6);
  const int w = pw == 0 ? NW : pw - 1, tid = w * 64 + lane;
  const int b = blockIdx.x, g = b >> 4, s = b & 15;
  const int c = lane & 15;
  const int b2 = (b + (b >> 5)) & 31;
  const bool isf = ((b2 * 9) & 31) < 9;
  const int fidx = (b >> 5) * 9 + ((b2 * 9) >> 5);
  unsigned* const ldu = reinterpret_cast<unsigned*>(lds);
  const unsigned lds0 = (unsigned)(uintptr_t)(LAS float*)lds;
  STAMP_DECL;

  const __amdgpu_buffer_rsrc_t rws = mk_rsrc(ws, WS_END);
  const __amdgpu_buffer_rsrc_t rx = mk_rsrc(xg, DM * 2);
  const __amdgpu_buffer_rsrc_t rk = mk_rsrc(kg + (size_t)g * SK * 128, SK * 256);
  const __amdgpu_buffer_rsrc_t rv = mk_rsrc(vg + (size_t)g * SK * 128, SK * 256);

  STAMP(0);
  const int voff = lane * 16;
  auto ln_stats = [&](const unsigned (&xd)[5], float& mb, float& den) {
    float xv[10];
#pragma unroll
    for (int k = 0; k < 5; ++k) { xv[2 * k] = lo_f(xd[k]); xv[2 * k + 1] = hi_f(xd[k]); }
    float sm = 0.f;
#pragma unroll
    for (int k = 0; k < 10; ++k) sm += xv[k];
    const float mean = wave_sum(sm) * (1.0f / DM);
    float sq = 0.f;
#pragma unroll
    for (int k = 0; k < 8; ++k) { const float d = xv[k] - mean; sq = fmaf(d, d, sq); }
    const float d8 = lane < 32 ? xv[8] - mean : 0.f, d9 = lane < 32 ? xv[9] - mean : 0.f;
    sq = fmaf(d8, d8, sq); sq = fmaf(d9, d9, sq);
    const float var = wave_sum(sq) * (1.0f / DM);
    mb = rnd(mean);
    den = rnd(sqrtf(rnd(rnd(var) + 1e-5f)));
  };
  auto stage_wo = [&]() {
    const unsigned char* so = (const unsigned char*)(wo + (size_t)b * 16 * DM) + lane * 16;
#pragma unroll
    for (int i = 0; i < 18; ++i) glds16(so + i * 1024, lds0 + (L_WO + i * 256) * 4);
  };
  int ufirst, ustep, nt;
  {
    const unsigned fm = __builtin_rotateright32(ffn_mask(), (b >> 5) + (g & 1) * 16) & 0xffffu;
    const int fb = __builtin_popcount(fm & ((1u << s) - 1u)), ob = s - fb;
    constexpr int HT4 = 2048 - NSW * (4 * K_F + 12 * K_O), HT5 = 2048 - NSW * (5 * K_F + 11 * K_O);
    static_assert(HT4 >= 0 && HT5 >= 0 && HT4 <= 16 * 2 * NHELP && HT5 <= 16 * 2 * NHELP, "unit split");
    const int ht = __builtin_popcount(fm) == 4 ? HT4 : HT5;
    const int h0 = (s * ht) >> 4, hu = (((s + 1) * ht) >> 4) - h0;
    const int ubase = NSW * (fb * K_F + ob * K_O) + h0;
    const int j = w - NSW;
    ufirst = w < NSW ? ubase + hu + w : ubase + j;
    ustep = w < NSW ? NSW : NHELP;
    nt = w < NSW ? 2 * (isf ? K_F : K_O) : (hu > j ? 2 : 0) + (hu > j + NHELP ? 2 : 0);
  }
  const int sbase = w < NSW ? 4 * NHELP + w * 2 * K_O : (w - NSW) * 4;
  static_assert(4 * NHELP + NSW * 2 * K_O <= 288 && K_F <= K_O, "score tiles");
#define TOFF(t) ((ufirst + ((t) >> 1) * ustep) << 13)
  v4u buf[2 * TL];
#define tb(n, i) buf[(n) * TL + (i)]
  unsigned tag;
  unsigned gm[4];
#pragma unroll
  for (int r = 0; r < 4; ++r) gm[r] = ((lane & 7) >> 1) == r ? ((lane & 1) ? 0xffff0000u : 0x0000ffffu) : 0u;

  if (__builtin_expect(w == NW, 1)) {
    __builtin_amdgcn_s_setprio(3);
    unsigned xd[5], xq[3];
#pragma unroll
    for (int k = 0; k < 5; ++k) xd[k] = __builtin_amdgcn_raw_buffer_load_b32(rx, lane * 4 + k * 256, 0, 0);
#pragma unroll
    for (int k = 0; k < 3; ++k) xq[k] = __builtin_amdgcn_raw_buffer_load_b16(rx, (144 * (s >> 2) + lane + 64 * k) * 2, 0, 0);
    v4u wqv[18];
    {
      const __amdgpu_buffer_rsrc_t rq = mk_rsrc(wq, DM * QD * 2);
      const int o = (144 * (s >> 2) + (lane >> 3)) * (QD * 2) + (g * 4 + (s & 3)) * 128 + (lane & 7) * 16;
#pragma unroll
      for (int i = 0; i < 18; ++i) wqv[i] = __builtin_amdgcn_raw_buffer_load_b128(rq, o, i * 8 * QD * 2, AUX_NT);
    }
    tag = __builtin_amdgcn_raw_buffer_load_b32(rws, lane == 0 ? WS_EPOCH : OOB, 0, AUX_SC1) + 1u;
    __builtin_amdgcn_sched_barrier(0);
    __builtin_amdgcn_s_barrier();
    __builtin_amdgcn_sched_barrier(0);
    if (lane == 0) ldu[L_CNT] = 0u;
    float mb, den;
    unsigned xn[3];
    ln_stats(xd, mb, den);
#pragma unroll
    for (int k = 0; k < 3; ++k) { const float v = rnd(lo_f(xq[k]) - mb) / den; xn[k] = pk_bf16(v, v); }
    if (lane == 0) *reinterpret_cast<f32x2*>(&lds[L_LN]) = f32x2{mb, den};
    STAMP(1);
    unsigned xr[18];
#pragma unroll
    for (int i = 0; i < 18; ++i) xr[i] = __shfl(xn[i >> 3], (8 * i + (lane >> 3)) & 63);
#pragma unroll
    for (int i = 0; i < 18; ++i) asm volatile("" : "+v"(xr[i]));
    __builtin_amdgcn_sched_barrier(0);
    f32x4 qa = {0.f, 0.f, 0.f, 0.f};
#pragma unroll
    for (int i = 0; i < 18; ++i) { qa = mfma16(gemv_a(xr[i], gm), wqv[i], qa); __builtin_amdgcn_sched_barrier(0); }
    qa = gemv_fin(qa, lane);
    tag = __builtin_amdgcn_readfirstlane(tag);
    {
      const bool up2 = (lane & 8) != 0;
      const int col = (lane & 7) * 8 + 4 * ((lane >> 4) & 1) + (up2 ? 2 : 0);
      __builtin_amdgcn_raw_buffer_store_b128(v4u{__float_as_uint(up2 ? qa[2] : qa[0]), tag, __float_as_uint(up2 ? qa[3] : qa[1]), tag}, rws,
                                             lane < 32 ? WS_QG + ((g * 4 + (s >> 2)) * 256 + (s & 3) * 64 + col) * 8 : OOB, 0, AUX_SC1);
    }
    STAMP(2);
    float q4[4];
    for (unsigned spins = 0;; ++spins) {
      asm volatile("" ::: "memory");
      v4u a[8];
#pragma unroll
      for (int r = 0; r < 4; ++r) {
        a[2 * r] = __builtin_amdgcn_raw_buffer_load_b128(rws, WS_QG + g * 8192 + lane * 32, r * 2048, AUX_SC1);
        a[2 * r + 1] = __builtin_amdgcn_raw_buffer_load_b128(rws, WS_QG + g * 8192 + lane * 32 + 16, r * 2048, AUX_SC1);
      }
      unsigned bad = 0;
#pragma unroll
      for (int j = 0; j < 4; ++j) q4[j] = 0.f;
#pragma unroll
      for (int r = 0; r < 4; ++r) {
        bad |= (a[2 * r].y ^ tag) | (a[2 * r].w ^ tag) | (a[2 * r + 1].y ^ tag) | (a[2 * r + 1].w ^ tag);
        q4[0] += __uint_as_float(a[2 * r].x); q4[1] += __uint_as_float(a[2 * r].z);
        q4[2] += __uint_as_float(a[2 * r + 1].x); q4[3] += __uint_as_float(a[2 * r + 1].z);
      }
      if (__all(bad == 0) || spins > SPIN_CAP) break;
      __builtin_amdgcn_s_sleep(1);
    }
    *reinterpret_cast<v2u*>(&ldu[L_Q + 2 * lane]) = v2u{pk_bf16(q4[0], q4[1]), pk_bf16(q4[2], q4[3])};
    if (lane == 0) ldu[L_TAG] = tag;
    __syncthreads();
    STAMP(3);
    __builtin_amdgcn_s_setprio(0);
    if (KDM == 0) stage_wo();
  } else {
    __builtin_amdgcn_s_barrier();
    __builtin_amdgcn_sched_barrier(0);
    const int t0 = nt > 0 ? TOFF(0) : OOB;
#pragma unroll
    for (int i = 0; i < 2 * TL; ++i) buf[i] = __builtin_amdgcn_raw_buffer_load_b128(rk, voff + i * 1024, t0, AUX_NT);
    __builtin_amdgcn_sched_barrier(0);
    if (KDM == 1 && w == NW - 1) stage_wo();
    STAMP(1);
    __syncthreads();
    tag = ldu[L_TAG];
    STAMP(3);
    const int hf = (c >> 2) & 1;
    const v4u qf = *reinterpret_cast<const v4u*>(&ldu[L_Q + hf * 64 + 4 * c]);
    const v4u qs = *reinterpret_cast<const v4u*>(&ldu[L_Q + (hf ^ 1) * 64 + 4 * c]);
    constexpr float LOG2E = 1.4426950408889634f, SCALE = 0.08838834764831845f;
    const bool b0 = (c & 1) != 0, b1 = (c & 2) != 0;
    float* const scw = &lds[L_SC + sbase * 32 + (lane >> 4) * 8 + (lane & 7)];
    float m = -INFINITY;
#pragma unroll 1
    for (int t = 0; t < nt; t += 2) {
      const bool lastk = t + 2 >= nt;
      const __amdgpu_buffer_rsrc_t rn = lastk ? rv : rk;
      const int nxt = TOFF(lastk ? 0 : t + 2);
#pragma unroll
      for (int n = 0; n < 2; ++n) {
        float y[4];
#pragma unroll
        for (int i = 0; i < TL; ++i) {
          float xf = 0.f, xs = 0.f;
#pragma unroll
          for (int j = 0; j < 4; ++j) { xf = dot2(tb(n, i)[j], qf[j], xf); xs = dot2(tb(n, i)[j], qs[j], xs); }
          y[i] = xf + dpp<XOR7>(xs);
        }
        asm volatile("" : "+v"(y[0]), "+v"(y[1]), "+v"(y[2]), "+v"(y[3]));
        __builtin_amdgcn_sched_barrier(0);
#pragma unroll
        for (int i = 0; i < TL; ++i) tb(n, i) = __builtin_amdgcn_raw_buffer_load_b128(rn, voff + i * 1024, nxt + n * 4096, AUX_NT);
        __builtin_amdgcn_sched_barrier(0);
        const float u0 = (b0 ? y[1] : y[0]) + dpp<XOR1>(b0 ? y[0] : y[1]);
        const float u1 = (b0 ? y[3] : y[2]) + dpp<XOR1>(b0 ? y[2] : y[3]);
        float sc = (b1 ? u1 : u0) + dpp<XOR2>(b1 ? u0 : u1);
        sc += dpp<XOR8>(sc);
        float tl2;
        if (MIMIC) tl2 = bfr(bfr(sc) * SCALE) * LOG2E; else tl2 = sc * (SCALE * LOG2E);
        m = fmaxf(m, tl2);
        scw[(t + n) * 32] = tl2;
      }
    }
    m = fmaxf(m, dpp<XOR1>(m)); m = fmaxf(m, dpp<XOR2>(m));
    m = max_x32(max_x16(m));
    float lsum = 0.f;
    f32x4 acc = {0.f, 0.f, 0.f, 0.f};
    unsigned amask[4];
    {
      const int jp = (c & 3) + 4 * (c >> 3);
#pragma unroll
      for (int r = 0; r < 4; ++r) amask[r] = (jp >> 1) == r ? ((jp & 1) ? 0xffff0000u : 0x0000ffffu) : 0u;
    }
#pragma unroll 1
    for (int t = 0; t < nt; t += 2) {
      const int nxt = t + 2 < nt ? TOFF(t + 2) : OOB;
      const float s0 = scw[t * 32], s1 = scw[t * 32 + 32];
#pragma unroll
      for (int n = 0; n < 2; ++n) {
        const float p = __builtin_amdgcn_exp2f((n ? s1 : s0) - m);
        lsum += p;
        const unsigned pp = pk_bf16(p, p);
#pragma unroll
        for (int i = 0; i < TL; ++i) {
          const unsigned pb = i == 0 ? dppu<0x00>(pp) : i == 1 ? dppu<0x55>(pp) : i == 2 ? dppu<0xAA>(pp) : dppu<0xFF>(pp);
          const v4u av = {pb & amask[0], pb & amask[1], pb & amask[2], pb & amask[3]};
          acc = mfma16(av, tb(n, i), acc);
        }
        __builtin_amdgcn_sched_barrier(0);
#pragma unroll
        for (int i = 0; i < TL; ++i) tb(n, i) = __builtin_amdgcn_raw_buffer_load_b128(rv, voff + i * 1024, nxt + n * 4096, AUX_NT);
        __builtin_amdgcn_sched_barrier(0);
      }
    }
    STAMP(4);
    float l = lsum;
    l += dpp<XOR1>(l); l += dpp<XOR2>(l);
    l = sum_x32(sum_x16(l));
    if ((lane & 59) == 0) { *reinterpret_cast<f32x2*>(&lds[L_ML + w * 4 + (lane >> 2) * 2]) = f32x2{m, l}; }
    const int hh = (lane >> 4) & 1, dd = 8 * c + 4 * (lane >> 5);
    *reinterpret_cast<f32x4*>(&lds[L_ACC + w * 256 + hh * 128 + dd]) = acc;
  }

  if (isf && w >= NSW) {
    constexpr int NFV = NW + 1 - NSW, RG = (72 + NFV - 1) / NFV;
    const int fw = w - NSW;
    const __amdgpu_buffer_rsrc_t rfg = mk_rsrc(wgate, DM * FF * 2);
    const __amdgpu_buffer_rsrc_t rfu = mk_rsrc(wup, DM * FF * 2);
    const int foff = (8 * RG * fw + (lane >> 3)) * (FF * 2) + fidx * 128 + (lane & 7) * 16;
    unsigned xf[2];
#pragma unroll
    for (int k = 0; k < 2; ++k) xf[k] = __builtin_amdgcn_raw_buffer_load_b16(rx, (8 * RG * fw + lane + 64 * k) * 2, 0, 0);
    v4u fgu[2 * RG];
#pragma unroll
    for (int i = 0; i < RG; ++i) {
      fgu[2 * i] = __builtin_amdgcn_raw_buffer_load_b128(rfg, foff, i * 8 * FF * 2, AUX_NT);
      fgu[2 * i + 1] = __builtin_amdgcn_raw_buffer_load_b128(rfu, foff, i * 8 * FF * 2, AUX_NT);
    }
    __builtin_amdgcn_sched_barrier(0);
    const f32x2 ln = *reinterpret_cast<const f32x2*>(&lds[L_LN]);
    const float v0 = rnd(lo_f(xf[0]) - ln.x) / ln.y, v1 = rnd(lo_f(xf[1]) - ln.x) / ln.y;
    const unsigned xn0 = pk_bf16(v0, v0), xn1 = pk_bf16(v1, v1);
    f32x4 ag = {0.f, 0.f, 0.f, 0.f}, au = {0.f, 0.f, 0.f, 0.f};
#pragma unroll
    for (int i = 0; i < RG; ++i) {
      const v4u av = gemv_a(__shfl(i < 8 ? xn0 : xn1, (8 * i + (lane >> 3)) & 63), gm);
      ag = mfma16(av, fgu[2 * i], ag); au = mfma16(av, fgu[2 * i + 1], au);
    }
    asm volatile("" : "+v"(ag), "+v"(au));
    __builtin_amdgcn_sched_barrier(0);
    const int t = tid - NSW * 64;
    const __amdgpu_buffer_rsrc_t rwd = mk_rsrc(wdown + (size_t)fidx * 64 * DM, 64 * DM * 2);
    const int o = t < 288 ? t * 4 : OOB;
    unsigned wd[32];
#pragma unroll
    for (int j = 0; j < 32; ++j) wd[j] = __builtin_amdgcn_raw_buffer_load_b32(rwd, o, j * DM * 2, AUX_NT);
    __builtin_amdgcn_sched_barrier(0);
    ag = gemv_fin(ag, lane); au = gemv_fin(au, lane);
    if ((lane & 8) == 0) *reinterpret_cast<f32x4*>(&lds[L_FR + fw * 128 + (lane >> 5) * 64 + (lane & 7) * 8 + 4 * ((lane >> 4) & 1)]) = lane < 32 ? ag : au;
    asm volatile("s_waitcnt lgkmcnt(0)" ::: "memory");
    if (lane == 0) __hip_atomic_fetch_add((LAS unsigned*)&ldu[L_CNT], 1u, __ATOMIC_RELAXED, __HIP_MEMORY_SCOPE_WORKGROUP);
    for (unsigned spins = 0; spins < (SPIN_CAP << 4); ++spins) {
      if (__builtin_amdgcn_readfirstlane(*(volatile LAS unsigned*)&ldu[L_CNT]) >= (unsigned)NFV) break;
      __builtin_amdgcn_s_sleep(2);
    }
    asm volatile("" ::: "memory");
    float gt = 0.f, up = 0.f;
#pragma unroll
    for (int ww = 0; ww < NFV; ++ww) { gt += lds[L_FR + ww * 128 + lane]; up += lds[L_FR + ww * 128 + 64 + lane]; }
    gt = rnd(gt); up = rnd(up);
    const float inner = 0.7978845608028654f * (gt + 0.044715f * gt * gt * gt);
    const float hv = rnd(rnd(0.5f * gt * (1.0f + tanhf(inner))) * up);
    float y0 = 0.f, y1 = 0.f;
#pragma unroll
    for (int j = 0; j < 32; ++j) {
      const float h = rdlane(hv, j); y0 = fmaf(h, lo_f(wd[j]), y0); y1 = fmaf(h, hi_f(wd[j]), y1);
      asm volatile("" : "+v"(y0), "+v"(y1));
      wd[j] = __builtin_amdgcn_raw_buffer_load_b32(rwd, o, (32 + j) * DM * 2, AUX_NT);
    }
#pragma unroll
    for (int j = 0; j < 32; ++j) { const float h = rdlane(hv, 32 + j); y0 = fmaf(h, lo_f(wd[j]), y0); y1 = fmaf(h, hi_f(wd[j]), y1); }
    *reinterpret_cast<f32x2*>(&lds[L_YF + 2 * t]) = f32x2{y0, y1};
  }

  asm volatile("s_waitcnt vmcnt(0)" ::: "memory");
  __syncthreads();
  STAMP(7);
  if (tid < 256) {
    const int hh = tid >> 7;
    float mw[NW], lw[NW];
#pragma unroll
    for (int ww = 0; ww < NW; ++ww) { const f32x2 v = *reinterpret_cast<const f32x2*>(&lds[L_ML + ww * 4 + hh * 2]); mw[ww] = v.x; lw[ww] = v.y; }
    float M = mw[0];
#pragma unroll
    for (int ww = 1; ww < NW; ++ww) M = fmaxf(M, mw[ww]);
    float L = 0.f, o = 0.f;
#pragma unroll
    for (int ww = 0; ww < NW; ++ww) { const float e = __builtin_amdgcn_exp2f(mw[ww] - M); L = fmaf(e, lw[ww], L); o = fmaf(e, lds[L_ACC + ww * 256 + tid], o); }
    __builtin_amdgcn_raw_buffer_store_b64(v2u{__float_as_uint(o), tag}, rws, WS_P1O + (((g * 16 + (tid >> 4)) * 16 + s) * 16 + (tid & 15)) * 8, 0, AUX_SC1);
    if ((tid & 127) == 0)
      __builtin_amdgcn_raw_buffer_store_b128(v4u{__float_as_uint(M), tag, __float_as_uint(L), tag}, rws, WS_P1M + ((g * 2 + hh) * 16 + s) * 16, 0, AUX_SC1);
  }
  if (w == NW) {
    const int pr = lane >> 2, part = lane & 3;
    const int oo = WS_P1O + ((g * 16 + s) * 16 + pr) * 128 + part * 32, om = WS_P1M + ((g * 2 + (s >> 3)) * 16 + pr) * 16;
    v4u a0, a1, ml;
    for (unsigned spins = 0;; ++spins) {
      asm volatile("" ::: "memory");
      a0 = __builtin_amdgcn_raw_buffer_load_b128(rws, oo, 0, AUX_SC1);
      a1 = __builtin_amdgcn_raw_buffer_load_b128(rws, oo + 16, 0, AUX_SC1);
      ml = __builtin_amdgcn_raw_buffer_load_b128(rws, om, 0, AUX_SC1);
      const unsigned bad = (a0.y ^ tag) | (a0.w ^ tag) | (a1.y ^ tag) | (a1.w ^ tag) | (ml.y ^ tag) | (ml.w ^ tag);
      if (__all(bad == 0) || spins > SPIN_CAP) break;
      __builtin_amdgcn_s_sleep(1);
    }
    const float mp = __uint_as_float(ml.x);
    float M = mp;
    M = fmaxf(M, dpp<ROR4>(M)); M = fmaxf(M, dpp<XOR8>(M)); M = max_x32(max_x16(M));
    const float e = __builtin_amdgcn_exp2f(mp - M);
    float v5[5] = {e * __uint_as_float(a0.x), e * __uint_as_float(a0.z), e * __uint_as_float(a1.x), e * __uint_as_float(a1.z), e * __uint_as_float(ml.z)};
#pragma unroll
    for (int j = 0; j < 5; ++j) { float t = v5[j]; t += dpp<ROR4>(t); t += dpp<XOR8>(t); v5[j] = sum_x32(sum_x16(t)); }
    const float inv = 1.0f / v5[4];
    if (lane < 4) *reinterpret_cast<f32x4*>(&lds[L_O + lane * 4]) = f32x4{rnd(v5[0] * inv), rnd(v5[1] * inv), rnd(v5[2] * inv), rnd(v5[3] * inv)};
  }
  unsigned wod[16];
  f32x2 yf = {0.f, 0.f};
  if (tid < 288) {
#pragma unroll
    for (int j = 0; j < 16; ++j) wod[j] = ldu[L_WO + j * 288 + tid];
    yf = *reinterpret_cast<const f32x2*>(&lds[L_YF + 2 * tid]);
  }
  __syncthreads();
  STAMP(8);
  if (tid < 288) {
    float y0 = 0.f, y1 = 0.f;
#pragma unroll
    for (int j = 0; j < 16; ++j) { const float ov = lds[L_O + j]; y0 = fmaf(ov, lo_f(wod[j]), y0); y1 = fmaf(ov, hi_f(wod[j]), y1); }
    __builtin_amdgcn_raw_buffer_store_b128(v4u{__float_as_uint(y0), tag, __float_as_uint(y1), tag}, rws, WS_P2 + ((tid >> 3) * NWG + b) * 128 + (tid & 7) * 16, 0, AUX_SC1);
    if (isf) __builtin_amdgcn_raw_buffer_store_b128(v4u{__float_as_uint(yf.x), tag, __float_as_uint(yf.y), tag}, rws, WS_PF + ((tid >> 3) * NFW + fidx) * 128 + (tid & 7) * 16, 0, AUX_SC1);
  }
  STAMP(9);
  if (b >= NRED) { STAMP_FLUSH(); return; }
  {
    const int r = b;
    const unsigned xp = __builtin_amdgcn_raw_buffer_load_b32(rx, tid < 8 ? r * 32 + tid * 4 : OOB, 0, 0);
    float f0, f1, s0, s1;
    for (unsigned spins = 0;; ++spins) {
      asm volatile("" ::: "memory");
      v4u p2[4];
      const v4u pf = __builtin_amdgcn_raw_buffer_load_b128(rws, WS_PF + r * NFW * 128 + tid * 16, 0, AUX_SC1);
#pragma unroll
      for (int k = 0; k < 4; ++k) p2[k] = __builtin_amdgcn_raw_buffer_load_b128(rws, w < NW ? WS_P2 + r * NWG * 128 + tid * 16 : OOB, k * 8192, AUX_SC1);
      unsigned bad = 0; s0 = 0.f; s1 = 0.f;
#pragma unroll
      for (int k = 0; k < 4; ++k) { bad |= (p2[k].y ^ tag) | (p2[k].w ^ tag); s0 += __uint_as_float(p2[k].x); s1 += __uint_as_float(p2[k].z); }
      if (w == NW) bad = 0;
      bad |= (pf.y ^ tag) | (pf.w ^ tag); f0 = __uint_as_float(pf.x); f1 = __uint_as_float(pf.z);
      if (__all(bad == 0) || spins > SPIN_CAP) break;
      __builtin_amdgcn_s_sleep(1);
    }
    STAMP(10);
    float v4[4] = {s0, s1, f0, f1};
#pragma unroll
    for (int j = 0; j < 4; ++j) { float t = v4[j]; t += dpp<XOR8>(t); v4[j] = sum_x32(sum_x16(t)); }
    if (lane < 8) *reinterpret_cast<f32x4*>(&lds[L_ACC + w * 32 + lane * 4]) = f32x4{v4[0], v4[1], v4[2], v4[3]};
    __syncthreads();
    if (tid < 8) {
      f32x4 t = {0.f, 0.f, 0.f, 0.f};
#pragma unroll
      for (int ww = 0; ww < NW + 1; ++ww) t += *reinterpret_cast<const f32x4*>(&lds[L_ACC + ww * 32 + tid * 4]);
      float o2[2];
#pragma unroll
      for (int j = 0; j < 2; ++j) {
        const float sv = rnd(t[j]), yv = rnd(t[2 + j]);
        const float gv = rnd(yv / (1.0f + __expf(-yv)));
        o2[j] = (j ? hi_f(xp) : lo_f(xp)) + rnd(sv * gv);
      }
      *reinterpret_cast<unsigned*>(out + r * 16 + tid * 2) = pk_bf16(o2[0], o2[1]);
    }
    if (b == 0 && tid == 0) __builtin_amdgcn_raw_buffer_store_b32(tag, rws, WS_EPOCH, 0, AUX_SC1);
    STAMP(11);
    STAMP_FLUSH();
  }
}

}

extern "C" void kernel_launch(void* const* d_in, const int* in_sizes, int n_in, void* d_out, int out_size, void* d_ws, size_t ws_size,
                              hipStream_t stream) {
  (void)in_sizes; (void)n_in; (void)out_size; (void)ws_size;
  unsigned char* ws = (unsigned char*)d_ws;
  if (KWSEND) ws += (ws_size - (size_t)(4 << 20)) & ~(size_t)4095;
  fused_decode<<<NWG, NTHR, 0, stream>>>((const unsigned short*)d_in[0], (const unsigned short*)d_in[1], (const unsigned short*)d_in[2],
                                         (const unsigned short*)d_in[3], (const unsigned short*)d_in[4], (const unsigned short*)d_in[6],
                                         (const unsigned short*)d_in[7], (const unsigned short*)d_in[8], (unsigned short*)d_out, ws);
}
```

```cpp
#include <hip/hip_runtime.h>
#include <hip/hip_bf16.h>
#include <stdint.h>

namespace {

typedef unsigned v4u __attribute__((ext_vector_type(4)));
typedef unsigned v2u __attribute__((ext_vector_type(2)));
typedef float f32x4 __attribute__((ext_vector_type(4)));
typedef float f32x2 __attribute__((ext_vector_type(2)));
typedef __bf16 bf16x8 __attribute__((ext_vector_type(8)));
typedef __bf16 bf16x2 __attribute__((ext_vector_type(2)));
#define GAS __attribute__((address_space(1)))
#define LAS __attribute__((address_space(3)))

#define KNSW 3
#define KFB 4
#define KDM 1
#define KWSEND 0
#define KFD 6
#define MIMIC 1

constexpr int DM = 576, SK = 65536, FF = 4608, QD = 4096;
constexpr int NWG = 256, NW = 8, NTHR = (NW + 1) * 64;
constexpr int NSW = KNSW;
constexpr int NHELP = NW - NSW;
constexpr int NRED = 36;
constexpr int NFW = 72;
constexpr int TL = 4;
constexpr int FB = KFB;
constexpr int OOB = 0x70000000;

constexpr int WS_EPOCH = 0;
constexpr int WS_QG = 4096;
constexpr int WS_P1M = WS_QG + 16 * 4 * 256 * 8;
constexpr int WS_P1O = WS_P1M + 16 * 2 * 16 * 16;
constexpr int WS_P2 = WS_P1O + 16 * 16 * 16 * 128;
constexpr int WS_PF = WS_P2 + NRED * NWG * 128;
constexpr int WS_END = WS_PF + NRED * NFW * 128;

#define STAMP_DECL do {} while (0)
#define STAMP(i) do {} while (0)
#define STAMP_FLUSH() do {} while (0)
constexpr int AUX_NT = 2, AUX_SC1 = 16;
constexpr int RSRC_W3 = 0x00020000;
constexpr unsigned SPIN_CAP = 1u << 14;

constexpr int L_WO = 0;
constexpr int L_WD = L_WO + 4608;
constexpr int L_XN = L_WD + 18432;
constexpr int L_RED = L_XN + 576;
constexpr int L_YF = L_RED + 576;
constexpr int L_H = L_YF + 640;
constexpr int L_O = L_H + 64;
constexpr int L_ML = L_O + 16;
constexpr int L_ACC = L_ML + 32;
constexpr int L_FIN = L_ACC + 2048;
constexpr int L_Q = L_FIN + 256;
constexpr int L_TAG = L_Q + 128;
constexpr int L_SC = L_TAG + 4;
constexpr int L_FLOATS = 38 * 1024;
static_assert(L_SC + 288 * 32 <= 36 * 1024 + 512, "LDS map");

template <int CTRL> __device__ __forceinline__ float dpp(float x) {
  return __builtin_bit_cast(float, __builtin_amdgcn_mov_dpp(__builtin_bit_cast(int, x), CTRL, 0xf, 0xf, true));
}
template <int CTRL> __device__ __forceinline__ unsigned dppu(unsigned x) {
  return (unsigned)__builtin_amdgcn_mov_dpp((int)x, CTRL, 0xf, 0xf, true);
}
constexpr int XOR1 = 0xB1, XOR2 = 0x4E, XOR7 = 0x141, XOR8 = 0x128, ROR4 = 0x124;
__device__ __forceinline__ float sum_x16(float x) {
  auto s = __builtin_amdgcn_permlane16_swap(__float_as_uint(x), __float_as_uint(x), false, false);
  return __uint_as_float(s[0]) + __uint_as_float(s[1]);
}
__device__ __forceinline__ float sum_x32(float x) {
  auto s = __builtin_amdgcn_permlane32_swap(__float_as_uint(x), __float_as_uint(x), false, false);
  return __uint_as_float(s[0]) + __uint_as_float(s[1]);
}
__device__ __forceinline__ float max_x16(float x) {
  auto s = __builtin_amdgcn_permlane16_swap(__float_as_uint(x), __float_as_uint(x), false, false);
  return fmaxf(__uint_as_float(s[0]), __uint_as_float(s[1]));
}
__device__ __forceinline__ float max_x32(float x) {
  auto s = __builtin_amdgcn_permlane32_swap(__float_as_uint(x), __float_as_uint(x), false, false);
  return fmaxf(__uint_as_float(s[0]), __uint_as_float(s[1]));
}
__device__ __forceinline__ float wave_sum(float x) {
  x += dpp<XOR1>(x); x += dpp<XOR2>(x); x += dpp<ROR4>(x); x += dpp<XOR8>(x);
  return sum_x32(sum_x16(x));
}
__device__ __forceinline__ unsigned pk_bf16(float lo, float hi) {
  bf16x2 v = {(__bf16)lo, (__bf16)hi};
  return __builtin_bit_cast(unsigned, v);
}
__device__ __forceinline__ float bfr(float x) { return (float)(__bf16)x; }
__device__ __forceinline__ float rnd(float x) { return MIMIC ? bfr(x) : x; }
__device__ __forceinline__ float lo_f(unsigned u) { return __uint_as_float(u << 16); }
__device__ __forceinline__ float hi_f(unsigned u) { return __uint_as_float(u & 0xffff0000u); }
__device__ __forceinline__ float dot2(unsigned a, unsigned b, float c) {
  return __builtin_amdgcn_fdot2_f32_bf16(__builtin_bit_cast(bf16x2, a), __builtin_bit_cast(bf16x2, b), c, false);
}
__device__ __forceinline__ __amdgpu_buffer_rsrc_t mk_rsrc(const void* p, int bytes) {
  return __builtin_amdgcn_make_buffer_rsrc((void*)p, 0, bytes, RSRC_W3);
}
__device__ __forceinline__ void glds16(const void* gsrc, unsigned lds_dst) {
  unsigned keep;
  asm volatile("s_mov_b32 %0, m0\n\ts_mov_b32 m0, %2\n\ts_nop 0\n\tglobal_load_lds_dwordx4 %1, off nt\n\ts_mov_b32 m0, %0"
               : "=&s"(keep) : "v"(gsrc), "s"(lds_dst) : "memory");
}
__device__ __forceinline__ float rdlane(float x, int l) { return __uint_as_float(__builtin_amdgcn_readlane(__float_as_uint(x), l)); }

__global__ void __launch_bounds__(NTHR, 3)
fused_decode(const unsigned short* __restrict__ xg, const unsigned short* __restrict__ kg, const unsigned short* __restrict__ vg,
             const unsigned short* __restrict__ wq, const unsigned short* __restrict__ wo, const unsigned short* __restrict__ wgate,
             const unsigned short* __restrict__ wup, const unsigned short* __restrict__ wdown, unsigned short* __restrict__ out,
             unsigned char* __restrict__ ws) {
  __shared__ __attribute__((aligned(16))) float lds[L_FLOATS];
  const int tid = threadIdx.x, lane = tid & 63, w = __builtin_amdgcn_readfirstlane(tid >> 6);
  const int b = blockIdx.x, g = b >> 4, s = b & 15;
  const int c = lane & 15;
  const bool isf = ((b * 9) & 31) < 9;
  const int fidx = (b * 9) >> 5;
  unsigned* const ldu = reinterpret_cast<unsigned*>(lds);
  const unsigned lds0 = (unsigned)(uintptr_t)(LAS float*)lds;
  STAMP_DECL;

  const __amdgpu_buffer_rsrc_t rws = mk_rsrc(ws, WS_END);
  const __amdgpu_buffer_rsrc_t rx = mk_rsrc(xg, DM * 2);
  const __amdgpu_buffer_rsrc_t rk = mk_rsrc(kg + (size_t)g * SK * 128, SK * 256);
  const __amdgpu_buffer_rsrc_t rv = mk_rsrc(vg + (size_t)g * SK * 128, SK * 256);

  STAMP(0);
  unsigned xd[5];
#pragma unroll
  for (int k = 0; k < 5; ++k) xd[k] = __builtin_amdgcn_raw_buffer_load_b32(rx, lane * 4 + k * 256, 0, 0);
  const int qrow = 144 * (s >> 2) + 16 * w + (lane >> 3);
  const unsigned xq0 = __builtin_amdgcn_raw_buffer_load_b16(rx, qrow * 2, 0, 0);
  const unsigned xq1 = __builtin_amdgcn_raw_buffer_load_b16(rx, qrow * 2 + 16, 0, 0);
  v4u wqv[2];
  {
    const __amdgpu_buffer_rsrc_t rq = mk_rsrc(wq, DM * QD * 2);
    const int o = qrow * (QD * 2) + (g * 4 + (s & 3)) * 128 + (lane & 7) * 16;
    wqv[0] = __builtin_amdgcn_raw_buffer_load_b128(rq, o, 0, AUX_NT);
    wqv[1] = __builtin_amdgcn_raw_buffer_load_b128(rq, o, 8 * QD * 2, AUX_NT);
  }
  unsigned tag = __builtin_amdgcn_raw_buffer_load_b32(rws, tid == NW * 64 ? WS_EPOCH : OOB, 0, AUX_SC1) + 1u;
  __builtin_amdgcn_sched_barrier(0);
  __builtin_amdgcn_s_barrier();
  __builtin_amdgcn_sched_barrier(0);
  const int voff = lane * 16;
  v4u buf[2 * FB > 2 * TL ? 2 * FB : 2 * TL];
#define tb(n, i) buf[(n) * TL + (i)]
#define fg(i) buf[2 * (i)]
#define fu(i) buf[2 * (i) + 1]
  int ubase, ucnt;
  {
    int nf = 0, fb = 0;
#pragma unroll
    for (int i = 0; i < 16; ++i) { const int f = (((g * 16 + i) * 9) & 31) < 9 ? 1 : 0; nf += f; fb += i < s ? f : 0; }
    const int no = 16 - nf, ob = s - fb, tot = 2048 - nf * (128 - KFD);
    ubase = fb * (128 - KFD) + ob * tot / no;
    ucnt = isf ? 128 - KFD : (ob + 1) * tot / no - ob * tot / no;
  }
  const int ufirst = w < NSW ? NHELP + w : w - NSW;
  const int nt = w < NSW ? 2 * ((ucnt - NHELP - w + NSW - 1) / NSW) : 2;
#define TLOC(t) (2 * (ufirst + ((t) >> 1) * NSW) + ((t) & 1))
#define TOFF(t) ((2 * ubase + TLOC(t)) << 12)
  {
    const int t0 = w < NW ? TOFF(0) : OOB;
#pragma unroll
    for (int i = 0; i < 2 * TL; ++i) buf[i] = __builtin_amdgcn_raw_buffer_load_b128(rk, voff + i * 1024, t0, AUX_NT);
  }
  __builtin_amdgcn_sched_barrier(0);
  auto stage_wo = [&]() {
    const unsigned char* so = (const unsigned char*)(wo + (size_t)b * 16 * DM) + lane * 16;
#pragma unroll
    for (int i = 0; i < 18; ++i) glds16(so + i * 1024, lds0 + (L_WO + i * 256) * 4);
  };

  float xv[10], mb, den;
  {
#pragma unroll
    for (int k = 0; k < 5; ++k) { xv[2 * k] = lo_f(xd[k]); xv[2 * k + 1] = hi_f(xd[k]); }
    float sm = 0.f;
#pragma unroll
    for (int k = 0; k < 10; ++k) sm += xv[k];
    const float mean = wave_sum(sm) * (1.0f / DM);
    float sq = 0.f;
#pragma unroll
    for (int k = 0; k < 8; ++k) { const float d = xv[k] - mean; sq = fmaf(d, d, sq); }
    const float d8 = lane < 32 ? xv[8] - mean : 0.f, d9 = lane < 32 ? xv[9] - mean : 0.f;
    sq = fmaf(d8, d8, sq); sq = fmaf(d9, d9, sq);
    const float var = wave_sum(sq) * (1.0f / DM);
    mb = rnd(mean);
    den = rnd(sqrtf(rnd(rnd(var) + 1e-5f)));
  }
  STAMP(1);
  {
    const float x0 = rnd(rnd(lo_f(xq0) - mb) / den), x1 = rnd(rnd(lo_f(xq1) - mb) / den);
    float a8[8];
#pragma unroll
    for (int j = 0; j < 4; ++j) {
      a8[2 * j] = fmaf(x1, lo_f(wqv[1][j]), x0 * lo_f(wqv[0][j]));
      a8[2 * j + 1] = fmaf(x1, hi_f(wqv[1][j]), x0 * hi_f(wqv[0][j]));
    }
#pragma unroll
    for (int j = 0; j < 8; ++j) { float t = a8[j]; t += dpp<XOR8>(t); a8[j] = sum_x32(sum_x16(t)); }
    if (lane < 8) {
      *reinterpret_cast<f32x4*>(&lds[L_RED + w * 64 + lane * 8]) = f32x4{a8[0], a8[1], a8[2], a8[3]};
      *reinterpret_cast<f32x4*>(&lds[L_RED + w * 64 + lane * 8 + 4]) = f32x4{a8[4], a8[5], a8[6], a8[7]};
    }
  }
  __syncthreads();
  STAMP(2);
  if (KDM == 1 && w == NW - 1) stage_wo();
  if (w == NW) {
    tag = __builtin_amdgcn_readfirstlane(tag);
    float q0 = 0.f;
#pragma unroll
    for (int ww = 0; ww < NW + 1; ++ww) q0 += lds[L_RED + ww * 64 + lane];
    __builtin_amdgcn_raw_buffer_store_b64(v2u{__float_as_uint(q0), tag}, rws, WS_QG + ((g * 4 + (s >> 2)) * 256 + (s & 3) * 64 + lane) * 8, 0, AUX_SC1);
    float q4[4];
    for (unsigned spins = 0;; ++spins) {
      asm volatile("" ::: "memory");
      v4u a[8];
#pragma unroll
      for (int r = 0; r < 4; ++r) {
        a[2 * r] = __builtin_amdgcn_raw_buffer_load_b128(rws, WS_QG + g * 8192 + lane * 32, r * 2048, AUX_SC1);
        a[2 * r + 1] = __builtin_amdgcn_raw_buffer_load_b128(rws, WS_QG + g * 8192 + lane * 32 + 16, r * 2048, AUX_SC1);
      }
      unsigned bad = 0;
#pragma unroll
      for (int j = 0; j < 4; ++j) q4[j] = 0.f;
#pragma unroll
      for (int r = 0; r < 4; ++r) {
        bad |= (a[2 * r].y ^ tag) | (a[2 * r].w ^ tag) | (a[2 * r + 1].y ^ tag) | (a[2 * r + 1].w ^ tag);
        q4[0] += __uint_as_float(a[2 * r].x); q4[1] += __uint_as_float(a[2 * r].z);
        q4[2] += __uint_as_float(a[2 * r + 1].x); q4[3] += __uint_as_float(a[2 * r + 1].z);
      }
      if (__all(bad == 0) || spins > SPIN_CAP) break;
      __builtin_amdgcn_s_sleep(1);
    }
    *reinterpret_cast<v2u*>(&ldu[L_Q + 2 * lane]) = v2u{pk_bf16(q4[0], q4[1]), pk_bf16(q4[2], q4[3])};
    if (lane == 0) ldu[L_TAG] = tag;
  }
  __syncthreads();
  tag = ldu[L_TAG];
  STAMP(3);

  if (w == NW) {
    __builtin_amdgcn_s_setprio(3);
    if (KDM == 0) stage_wo();
    if (isf) {
      const __amdgpu_buffer_rsrc_t rfg = mk_rsrc(wgate, DM * FF * 2);
      const __amdgpu_buffer_rsrc_t rfu = mk_rsrc(wup, DM * FF * 2);
      const int foff = (lane >> 3) * (FF * 2) + fidx * 128 + (lane & 7) * 16;
#pragma unroll
      for (int i = 0; i < FB; ++i) {
        fg(i) = __builtin_amdgcn_raw_buffer_load_b128(rfg, foff, i * 8 * FF * 2, AUX_NT);
        fu(i) = __builtin_amdgcn_raw_buffer_load_b128(rfu, foff, i * 8 * FF * 2, AUX_NT);
      }
      __builtin_amdgcn_sched_barrier(0);
#pragma unroll
      for (int k = 0; k < 5; ++k) {
        if (k < 4 || lane < 32) {
          f32x2 v = {rnd(rnd(xv[2 * k] - mb) / den), rnd(rnd(xv[2 * k + 1] - mb) / den)};
          *reinterpret_cast<f32x2*>(&lds[L_XN + 2 * (lane + 64 * k)]) = v;
        }
      }
      __builtin_amdgcn_fence(__ATOMIC_RELEASE, "wavefront");
      __builtin_amdgcn_wave_barrier();
      float ag[8], au[8];
#pragma unroll
      for (int j = 0; j < 8; ++j) { ag[j] = 0.f; au[j] = 0.f; }
      for (int it = 0; it < 72 / FB; ++it) {
#pragma unroll
        for (int i = 0; i < FB; ++i) {
          const float xr = lds[L_XN + (it * FB + i) * 8 + (lane >> 3)];
          const v4u gv = fg(i), uv = fu(i);
#pragma unroll
          for (int j = 0; j < 4; ++j) {
            ag[2 * j] = fmaf(xr, lo_f(gv[j]), ag[2 * j]); ag[2 * j + 1] = fmaf(xr, hi_f(gv[j]), ag[2 * j + 1]);
            au[2 * j] = fmaf(xr, lo_f(uv[j]), au[2 * j]); au[2 * j + 1] = fmaf(xr, hi_f(uv[j]), au[2 * j + 1]);
          }
#pragma unroll
          for (int j = 0; j < 8; ++j) asm volatile("" : "+v"(ag[j]), "+v"(au[j]));
          fg(i) = __builtin_amdgcn_raw_buffer_load_b128(rfg, foff, ((it + 1) * FB + i) * 8 * FF * 2, AUX_NT);
          fu(i) = __builtin_amdgcn_raw_buffer_load_b128(rfu, foff, ((it + 1) * FB + i) * 8 * FF * 2, AUX_NT);
          __builtin_amdgcn_sched_barrier(0);
        }
      }
#pragma unroll
      for (int j = 0; j < 8; ++j) {
        float t = ag[j]; t += dpp<XOR8>(t); ag[j] = sum_x32(sum_x16(t));
        float u = au[j]; u += dpp<XOR8>(u); au[j] = sum_x32(sum_x16(u));
      }
      float gt = ag[0], up = au[0];
#pragma unroll
      for (int j = 1; j < 8; ++j) { gt = (lane >> 3) == j ? ag[j] : gt; up = (lane >> 3) == j ? au[j] : up; }
      gt = rnd(gt); up = rnd(up);
      const float inner = 0.7978845608028654f * (gt + 0.044715f * gt * gt * gt);
      const float ge = rnd(0.5f * gt * (1.0f + tanhf(inner)));
      lds[L_H + (lane & 7) * 8 + (lane >> 3)] = rnd(ge * up);
      STAMP(4);
      const unsigned char* sd = (const unsigned char*)(wdown + (size_t)fidx * 64 * DM) + lane * 16;
      for (int i = 0; i < 72; i += FB) {
#pragma unroll
        for (int k = 0; k < FB; ++k) glds16(sd + (i + k) * 1024, lds0 + (L_WD + (i + k) * 256) * 4);
        asm volatile("s_waitcnt vmcnt(%0)" :: "n"(FB) : "memory");
      }
      asm volatile("s_waitcnt vmcnt(0) lgkmcnt(0)" ::: "memory");
      STAMP(5);
      for (int k = 0; k < 5; ++k) {
        const int t = k * 64 + lane;
        float y0 = 0.f, y1 = 0.f;
#pragma unroll 16
        for (int j = 0; j < 64; ++j) { const float h = lds[L_H + j]; const unsigned u = ldu[L_WD + j * 288 + (t < 288 ? t : 0)]; y0 = fmaf(h, lo_f(u), y0); y1 = fmaf(h, hi_f(u), y1); }
        *reinterpret_cast<f32x2*>(&lds[L_YF + 2 * t]) = f32x2{y0, y1};
      }
      STAMP(6);
    }
  } else {
    const int hf = (c >> 2) & 1;
    const v4u qf = *reinterpret_cast<const v4u*>(&ldu[L_Q + hf * 64 + 4 * c]);
    const v4u qs = *reinterpret_cast<const v4u*>(&ldu[L_Q + (hf ^ 1) * 64 + 4 * c]);
    constexpr float LOG2E = 1.4426950408889634f, SCALE = 0.08838834764831845f;
    const bool b0 = (c & 1) != 0, b1 = (c & 2) != 0;
    float* const scw = &lds[L_SC + (lane >> 4) * 8 + (lane & 7)];
    float m = -INFINITY;
#pragma unroll 1
    for (int t = 0; t < nt; t += 2) {
      const bool lastk = t + 2 >= nt;
      const __amdgpu_buffer_rsrc_t rn = lastk ? rv : rk;
      const int nxt = TOFF(lastk ? 0 : t + 2);
#pragma unroll
      for (int n = 0; n < 2; ++n) {
        float y[4];
#pragma unroll
        for (int i = 0; i < TL; ++i) {
          float xf = 0.f, xs = 0.f;
#pragma unroll
          for (int j = 0; j < 4; ++j) { xf = dot2(tb(n, i)[j], qf[j], xf); xs = dot2(tb(n, i)[j], qs[j], xs); }
          y[i] = xf + dpp<XOR7>(xs);
        }
        asm volatile("" : "+v"(y[0]), "+v"(y[1]), "+v"(y[2]), "+v"(y[3]));
        __builtin_amdgcn_sched_barrier(0);
#pragma unroll
        for (int i = 0; i < TL; ++i) tb(n, i) = __builtin_amdgcn_raw_buffer_load_b128(rn, voff + i * 1024, nxt + n * 4096, AUX_NT);
        __builtin_amdgcn_sched_barrier(0);
        const float u0 = (b0 ? y[1] : y[0]) + dpp<XOR1>(b0 ? y[0] : y[1]);
        const float u1 = (b0 ? y[3] : y[2]) + dpp<XOR1>(b0 ? y[2] : y[3]);
        float sc = (b1 ? u1 : u0) + dpp<XOR2>(b1 ? u0 : u1);
        sc += dpp<XOR8>(sc);
        float tl2;
        if (MIMIC) tl2 = bfr(bfr(sc) * SCALE) * LOG2E; else tl2 = sc * (SCALE * LOG2E);
        m = fmaxf(m, tl2);
        scw[TLOC(t + n) * 32] = tl2;
      }
    }
    m = fmaxf(m, dpp<XOR1>(m)); m = fmaxf(m, dpp<XOR2>(m));
    m = max_x32(max_x16(m));
    float lsum = 0.f;
    f32x4 acc = {0.f, 0.f, 0.f, 0.f};
    unsigned amask[4];
    {
      const int jp = (c & 3) + 4 * (c >> 3);
#pragma unroll
      for (int r = 0; r < 4; ++r) amask[r] = (jp >> 1) == r ? ((jp & 1) ? 0xffff0000u : 0x0000ffffu) : 0u;
    }
#pragma unroll 1
    for (int t = 0; t < nt; t += 2) {
      const int nxt = t + 2 < nt ? TOFF(t + 2) : OOB;
      const float s0 = scw[TLOC(t) * 32], s1 = scw[TLOC(t) * 32 + 32];
#pragma unroll
      for (int n = 0; n < 2; ++n) {
        const float p = __builtin_amdgcn_exp2f((n ? s1 : s0) - m);
        lsum += p;
        const unsigned pp = pk_bf16(p, p);
#pragma unroll
        for (int i = 0; i < TL; ++i) {
          const unsigned pb = i == 0 ? dppu<0x00>(pp) : i == 1 ? dppu<0x55>(pp) : i == 2 ? dppu<0xAA>(pp) : dppu<0xFF>(pp);
          const v4u av = {pb & amask[0], pb & amask[1], pb & amask[2], pb & amask[3]};
          acc = __builtin_amdgcn_mfma_f32_16x16x32_bf16(__builtin_bit_cast(bf16x8, av), __builtin_bit_cast(bf16x8, tb(n, i)), acc, 0, 0, 0);
        }
        __builtin_amdgcn_sched_barrier(0);
#pragma unroll
        for (int i = 0; i < TL; ++i) tb(n, i) = __builtin_amdgcn_raw_buffer_load_b128(rv, voff + i * 1024, nxt + n * 4096, AUX_NT);
        __builtin_amdgcn_sched_barrier(0);
      }
    }
    STAMP(4);
    float l = lsum;
    l += dpp<XOR1>(l); l += dpp<XOR2>(l);
    l = sum_x32(sum_x16(l));
    if ((lane & 59) == 0) { *reinterpret_cast<f32x2*>(&lds[L_ML + w * 4 + (lane >> 2) * 2]) = f32x2{m, l}; }
    const int hh = (lane >> 4) & 1, dd = 8 * c + 4 * (lane >> 5);
    *reinterpret_cast<f32x4*>(&lds[L_ACC + w * 256 + hh * 128 + dd]) = acc;
  }

  asm volatile("s_waitcnt vmcnt(0)" ::: "memory");
  __syncthreads();
  STAMP(7);
  if (tid < 256) {
    const int hh = tid >> 7;
    float mw[NW], lw[NW];
#pragma unroll
    for (int ww = 0; ww < NW; ++ww) { const f32x2 v = *reinterpret_cast<const f32x2*>(&lds[L_ML + ww * 4 + hh * 2]); mw[ww] = v.x; lw[ww] = v.y; }
    float M = mw[0];
#pragma unroll
    for (int ww = 1; ww < NW; ++ww) M = fmaxf(M, mw[ww]);
    float L = 0.f, o = 0.f;
#pragma unroll
    for (int ww = 0; ww < NW; ++ww) { const float e = __builtin_amdgcn_exp2f(mw[ww] - M); L = fmaf(e, lw[ww], L); o = fmaf(e, lds[L_ACC + ww * 256 + tid], o); }
    __builtin_amdgcn_raw_buffer_store_b64(v2u{__float_as_uint(o), tag}, rws, WS_P1O + (((g * 16 + (tid >> 4)) * 16 + s) * 16 + (tid & 15)) * 8, 0, AUX_SC1);
    if ((tid & 127) == 0)
      __builtin_amdgcn_raw_buffer_store_b128(v4u{__float_as_uint(M), tag, __float_as_uint(L), tag}, rws, WS_P1M + ((g * 2 + hh) * 16 + s) * 16, 0, AUX_SC1);
  }
  if (w == NW) {
    const int pr = lane >> 2, part = lane & 3;
    const int oo = WS_P1O + ((g * 16 + s) * 16 + pr) * 128 + part * 32, om = WS_P1M + ((g * 2 + (s >> 3)) * 16 + pr) * 16;
    v4u a0, a1, ml;
    for (unsigned spins = 0;; ++spins) {
      asm volatile("" ::: "memory");
      a0 = __builtin_amdgcn_raw_buffer_load_b128(rws, oo, 0, AUX_SC1);
      a1 = __builtin_amdgcn_raw_buffer_load_b128(rws, oo + 16, 0, AUX_SC1);
      ml = __builtin_amdgcn_raw_buffer_load_b128(rws, om, 0, AUX_SC1);
      const unsigned bad = (a0.y ^ tag) | (a0.w ^ tag) | (a1.y ^ tag) | (a1.w ^ tag) | (ml.y ^ tag) | (ml.w ^ tag);
      if (__all(bad == 0) || spins > SPIN_CAP) break;
      __builtin_amdgcn_s_sleep(1);
    }
    const float mp = __uint_as_float(ml.x);
    float M = mp;
    M = fmaxf(M, dpp<ROR4>(M)); M = fmaxf(M, dpp<XOR8>(M)); M = max_x32(max_x16(M));
    const float e = __builtin_amdgcn_exp2f(mp - M);
    float v5[5] = {e * __uint_as_float(a0.x), e * __uint_as_float(a0.z), e * __uint_as_float(a1.x), e * __uint_as_float(a1.z), e * __uint_as_float(ml.z)};
#pragma unroll
    for (int j = 0; j < 5; ++j) { float t = v5[j]; t += dpp<ROR4>(t); t += dpp<XOR8>(t); v5[j] = sum_x32(sum_x16(t)); }
    const float inv = 1.0f / v5[4];
    if (lane < 4) *reinterpret_cast<f32x4*>(&lds[L_O + lane * 4]) = f32x4{rnd(v5[0] * inv), rnd(v5[1] * inv), rnd(v5[2] * inv), rnd(v5[3] * inv)};
  }
  unsigned wod[16];
  f32x2 yf = {0.f, 0.f};
  if (tid < 288) {
#pragma unroll
    for (int j = 0; j < 16; ++j) wod[j] = ldu[L_WO + j * 288 + tid];
    yf = *reinterpret_cast<const f32x2*>(&lds[L_YF + 2 * tid]);
  }
  __syncthreads();
  STAMP(8);
  if (tid < 288) {
    float y0 = 0.f, y1 = 0.f;
#pragma unroll
    for (int j = 0; j < 16; ++j) { const float ov = lds[L_O + j]; y0 = fmaf(ov, lo_f(wod[j]), y0); y1 = fmaf(ov, hi_f(wod[j]), y1); }
    __builtin_amdgcn_raw_buffer_store_b128(v4u{__float_as_uint(y0), tag, __float_as_uint(y1), tag}, rws, WS_P2 + ((tid >> 3) * NWG + b) * 128 + (tid & 7) * 16, 0, AUX_SC1);
    if (isf) __builtin_amdgcn_raw_buffer_store_b128(v4u{__float_as_uint(yf.x), tag, __float_as_uint(yf.y), tag}, rws, WS_PF + ((tid >> 3) * NFW + fidx) * 128 + (tid & 7) * 16, 0, AUX_SC1);
  }
  STAMP(9);
  if (b >= NRED) { STAMP_FLUSH(); return; }
  {
    const int r = b;
    const unsigned xp = __builtin_amdgcn_raw_buffer_load_b32(rx, tid < 8 ? r * 32 + tid * 4 : OOB, 0, 0);
    float f0, f1, s0, s1;
    for (unsigned spins = 0;; ++spins) {
      asm volatile("" ::: "memory");
      v4u p2[4];
      const v4u pf = __builtin_amdgcn_raw_buffer_load_b128(rws, WS_PF + r * NFW * 128 + tid * 16, 0, AUX_SC1);
#pragma unroll
      for (int k = 0; k < 4; ++k) p2[k] = __builtin_amdgcn_raw_buffer_load_b128(rws, w < NW ? WS_P2 + r * NWG * 128 + tid * 16 : OOB, k * 8192, AUX_SC1);
      unsigned bad = 0; s0 = 0.f; s1 = 0.f;
#pragma unroll
      for (int k = 0; k < 4; ++k) { bad |= (p2[k].y ^ tag) | (p2[k].w ^ tag); s0 += __uint_as_float(p2[k].x); s1 += __uint_as_float(p2[k].z); }
      if (w == NW) bad = 0;
      bad |= (pf.y ^ tag) | (pf.w ^ tag); f0 = __uint_as_float(pf.x); f1 = __uint_as_float(pf.z);
      if (__all(bad == 0) || spins > SPIN_CAP) break;
      __builtin_amdgcn_s_sleep(1);
    }
    STAMP(10);
    float v4[4] = {s0, s1, f0, f1};
#pragma unroll
    for (int j = 0; j < 4; ++j) { float t = v4[j]; t += dpp<XOR8>(t); v4[j] = sum_x32(sum_x16(t)); }
    if (lane < 8) *reinterpret_cast<f32x4*>(&lds[L_ACC + w * 32 + lane * 4]) = f32x4{v4[0], v4[1], v4[2], v4[3]};
    __syncthreads();
    if (tid < 8) {
      f32x4 t = {0.f, 0.f, 0.f, 0.f};
#pragma unroll
      for (int ww = 0; ww < NW + 1; ++ww) t += *reinterpret_cast<const f32x4*>(&lds[L_ACC + ww * 32 + tid * 4]);
      float o2[2];
#pragma unroll
      for (int j = 0; j < 2; ++j) {
        const float sv = rnd(t[j]), yv = rnd(t[2 + j]);
        const float gv = rnd(yv / (1.0f + __expf(-yv)));
        o2[j] = (j ? hi_f(xp) : lo_f(xp)) + rnd(sv * gv);
      }
      *reinterpret_cast<unsigned*>(out + r * 16 + tid * 2) = pk_bf16(o2[0], o2[1]);
    }
    if (b == 0 && tid == 0) __builtin_amdgcn_raw_buffer_store_b32(tag, rws, WS_EPOCH, 0, AUX_SC1);
    STAMP(11);
    STAMP_FLUSH();
  }
}

}

extern "C" void kernel_launch(void* const* d_in, const int* in_sizes, int n_in, void* d_out, int out_size, void* d_ws, size_t ws_size,
                              hipStream_t stream) {
  (void)in_sizes; (void)n_in; (void)out_size; (void)ws_size;
  unsigned char* ws = (unsigned char*)d_ws;
  if (KWSEND) ws += (ws_size - (size_t)(4 << 20)) & ~(size_t)4095;
  fused_decode<<<NWG, NTHR, 0, stream>>>((const unsigned short*)d_in[0], (const unsigned short*)d_in[1], (const unsigned short*)d_in[2],
                                         (const unsigned short*)d_in[3], (const unsigned short*)d_in[4], (const unsigned short*)d_in[6],
                                         (const unsigned short*)d_in[7], (const unsigned short*)d_in[8], (unsigned short*)d_out, ws);
}
```

```cpp
#include <hip/hip_runtime.h>
#include <hip/hip_bf16.h>
#include <stdint.h>

namespace {

typedef unsigned v4u __attribute__((ext_vector_type(4)));
typedef unsigned v2u __attribute__((ext_vector_type(2)));
typedef float f32x4 __attribute__((ext_vector_type(4)));
typedef float f32x2 __attribute__((ext_vector_type(2)));
typedef __bf16 bf16x8 __attribute__((ext_vector_type(8)));
typedef __bf16 bf16x2 __attribute__((ext_vector_type(2)));
#define GAS __attribute__((address_space(1)))
#define LAS __attribute__((address_space(3)))

#define KHD 8
#define KKEARLY 1
#define KKO 42
#define KQD 4
#define KXMID 12
#define KTK 82
#define KTV 82
#define KBURST 15
#define EXP_NOTAIL 0
#define EXP_NOFFN 0
#define KLATEPOLL 1
#define KWARM 0
#define KD2 4
#define KOPEN 1

constexpr int DM = 576, SK = 65536, FF = 4608, QD = 4096;
constexpr int NWG = 256, NW = 8, NTHR = (NW + 1) * 64;
constexpr int NSW = 3;
constexpr int NHELP = NW - NSW;
constexpr int NRED = 36;
constexpr int NFW = 72;
constexpr int TL = 4;
constexpr int D2 = KD2;
constexpr int XMID = KXMID;
constexpr int K_O = KKO;
constexpr int NEART = 4;
constexpr int OOB = 0x70000000;

constexpr int WS_EPOCH = 0;
constexpr int WS_QG = 4096;
constexpr int WS_P1M = WS_QG + 16 * 4 * 256 * 8;
constexpr int WS_P1O = WS_P1M + 16 * 2 * 16 * 16;
constexpr int WS_P2 = WS_P1O + 16 * 16 * 16 * 128;
constexpr int WS_PF = WS_P2 + NRED * NWG * 128;
constexpr int WS_GU = WS_PF + NRED * NFW * 128;
constexpr int WS_END = WS_GU + NFW * 2 * 64 * 16;

#define STAMP_DECL do {} while (0)
#define STAMP(i) do {} while (0)
#define STAMP_FLUSH() do {} while (0)
#define TRACE(i) do {} while (0)
constexpr int AUX_NT = 2, AUX_SC1 = 16;
constexpr int RSRC_W3 = 0x00020000;
constexpr unsigned SPIN_CAP = 1u << 14;

constexpr int L_FLAG = 0;
enum { F_TAG, F_QRDY, F_XRDY, F_FDONE, F_PISS, F_P1CNT, F_MRDY, F_NEAR, F_WRDY, F_ENDCNT, F_FINCNT, F_GRDY, F_PROG, F_S1CNT };
constexpr int L_MW = 16;
constexpr int L_M = L_MW + 16;
constexpr int L_WT = L_M + 4;
constexpr int L_G = L_WT + 16;
constexpr int L_Q = L_G + 28;
constexpr int L_XN = L_Q + 128;
constexpr int L_YF = L_XN + 640;
constexpr int L_FR = L_YF;
constexpr int L_FIN = L_YF + 576;
constexpr int L_ACC = L_FIN + 128;
constexpr int L_SC = L_ACC + 2304;
constexpr int SC_TILES = 4 * NHELP + NSW * 2 * K_O;
constexpr int L_FLOATS = 24 * 1024;
static_assert(L_SC % 4 == 0 && L_ACC % 4 == 0 && L_Q % 4 == 0 && L_SC + SC_TILES * 32 + 256 <= 22 * 1024, "LDS map");

template <int CTRL> __device__ __forceinline__ float dpp(float x) {
  return __builtin_bit_cast(float, __builtin_amdgcn_mov_dpp(__builtin_bit_cast(int, x), CTRL, 0xf, 0xf, true));
}
template <int CTRL> __device__ __forceinline__ unsigned dppu(unsigned x) {
  return (unsigned)__builtin_amdgcn_mov_dpp((int)x, CTRL, 0xf, 0xf, true);
}
constexpr int XOR1 = 0xB1, XOR2 = 0x4E, XOR7 = 0x141, XOR8 = 0x128, ROR4 = 0x124;
__device__ __forceinline__ float sum_x16(float x) {
  auto s = __builtin_amdgcn_permlane16_swap(__float_as_uint(x), __float_as_uint(x), false, false);
  return __uint_as_float(s[0]) + __uint_as_float(s[1]);
}
__device__ __forceinline__ float sum_x32(float x) {
  auto s = __builtin_amdgcn_permlane32_swap(__float_as_uint(x), __float_as_uint(x), false, false);
  return __uint_as_float(s[0]) + __uint_as_float(s[1]);
}
__device__ __forceinline__ float max_x16(float x) {
  auto s = __builtin_amdgcn_permlane16_swap(__float_as_uint(x), __float_as_uint(x), false, false);
  return fmaxf(__uint_as_float(s[0]), __uint_as_float(s[1]));
}
__device__ __forceinline__ float max_x32(float x) {
  auto s = __builtin_amdgcn_permlane32_swap(__float_as_uint(x), __float_as_uint(x), false, false);
  return fmaxf(__uint_as_float(s[0]), __uint_as_float(s[1]));
}
__device__ __forceinline__ float wave_sum(float x) {
  x += dpp<XOR1>(x); x += dpp<XOR2>(x); x += dpp<ROR4>(x); x += dpp<XOR8>(x);
  return sum_x32(sum_x16(x));
}
__device__ __forceinline__ unsigned pk_bf16(float lo, float hi) {
  bf16x2 v = {(__bf16)lo, (__bf16)hi};
  return __builtin_bit_cast(unsigned, v);
}
__device__ __forceinline__ float rnd(float x) { return (float)(__bf16)x; }
__device__ __forceinline__ float lo_f(unsigned u) { return __uint_as_float(u << 16); }
__device__ __forceinline__ float hi_f(unsigned u) { return __uint_as_float(u & 0xffff0000u); }
__device__ __forceinline__ float dot2(unsigned a, unsigned b, float c) {
  return __builtin_amdgcn_fdot2_f32_bf16(__builtin_bit_cast(bf16x2, a), __builtin_bit_cast(bf16x2, b), c, false);
}
__device__ __forceinline__ __amdgpu_buffer_rsrc_t mk_rsrc(const void* p, int bytes) {
  return __builtin_amdgcn_make_buffer_rsrc((void*)p, 0, bytes, RSRC_W3);
}
__device__ __forceinline__ v4u and4(unsigned x, const unsigned (&m)[4]) { return v4u{x & m[0], x & m[1], x & m[2], x & m[3]}; }
__device__ __forceinline__ f32x4 mfma16(v4u a, v4u bm, f32x4 cacc) {
  return __builtin_amdgcn_mfma_f32_16x16x32_bf16(__builtin_bit_cast(bf16x8, a), __builtin_bit_cast(bf16x8, bm), cacc, 0, 0, 0);
}
__device__ __forceinline__ f32x4 gemv_fin(f32x4 d, int lane) {
  const bool valid = (lane >> 5) == ((lane >> 3) & 1);
#pragma unroll
  for (int r = 0; r < 4; ++r) { float t = valid ? d[r] : 0.f; t += dpp<XOR8>(t); d[r] = sum_x32(t); }
  return d;
}
__device__ __forceinline__ int lds_inc(unsigned addr) {
  int r; unsigned long long save;
  asm volatile("s_mov_b64 %1, exec\n\ts_mov_b64 exec, 1\n\tds_add_rtn_u32 %0, %2, %3\n\ts_mov_b64 exec, %1"
               : "=&v"(r), "=&s"(save) : "v"(addr), "v"(1) : "memory");
  return r;
}
__device__ __forceinline__ void pace(unsigned& next, unsigned tau, unsigned burst) {
  int d;
  asm volatile("1:\n\ts_memrealtime vcc\n\ts_waitcnt lgkmcnt(0)\n\ts_sub_i32 %0, vcc_lo, %1\n\ts_cmp_ge_i32 %0, 0\n\ts_cbranch_scc1 2f\n\ts_sleep 1\n\ts_branch 1b\n2:"
               : "=&s"(d) : "s"(next) : "scc", "vcc");
  next += tau + (unsigned)max(0, d - (int)burst);
}

__global__ void __launch_bounds__(NTHR, 3)
fused_decode(const unsigned short* __restrict__ xg, const unsigned short* __restrict__ kg, const unsigned short* __restrict__ vg,
             const unsigned short* __restrict__ wq, const unsigned short* __restrict__ wo, const unsigned short* __restrict__ wgate,
             const unsigned short* __restrict__ wup, const unsigned short* __restrict__ wdown, unsigned short* __restrict__ out,
             unsigned char* __restrict__ ws) {
  __shared__ __attribute__((aligned(16))) float lds[L_FLOATS];
  asm volatile("" :: "s"(xg), "s"(kg), "s"(vg), "s"(wq), "s"(wo), "s"(ws));
  const int lane = threadIdx.x & 63, pw = __builtin_amdgcn_readfirstlane(threadIdx.x >> 6);
  const int w = pw == 0 ? NW : pw - 1;
  const int b = blockIdx.x, g = b >> 4, s = b & 15;
  const int c = lane & 15;
  const int sr = (s + 3 * g) & 15, ns2 = 5 - (g & 1);
  const bool is1 = sr < 9, is2 = !is1 && sr < 9 + ns2;
  const int f1 = g * 9 + sr, f2 = (g >> 1) * 9 + (g & 1) * 5 + sr - 9;
  unsigned* const ldu = reinterpret_cast<unsigned*>(lds);
  const unsigned lds0 = (unsigned)(uintptr_t)(LAS float*)lds;
  STAMP_DECL;

  const __amdgpu_buffer_rsrc_t rws = mk_rsrc(ws, WS_END);
  const __amdgpu_buffer_rsrc_t rx = mk_rsrc(xg, DM * 2);

  STAMP(0);
  auto wait_flag = [&](int idx, unsigned want) {
#pragma unroll 1
    for (unsigned spins = 0; spins < (SPIN_CAP << 6); ++spins) {
      if (__builtin_amdgcn_readfirstlane(*(volatile LAS unsigned*)&ldu[L_FLAG + idx]) >= want) break;
      __builtin_amdgcn_s_sleep(1);
    }
    asm volatile("" ::: "memory");
  };
  auto set_flag = [&](int idx) {
    asm volatile("" ::: "memory");
    if (lane == 0) *(volatile LAS unsigned*)&ldu[L_FLAG + idx] = 1u;
  };
  auto count_up = [&](int idx) {
    int r = lds_inc(lds0 + (L_FLAG + idx) * 4);
    asm volatile("s_waitcnt lgkmcnt(0)" : "+v"(r));
  };
  unsigned gm[4];
#pragma unroll
  for (int r = 0; r < 4; ++r) gm[r] = ((lane & 7) >> 1) == r ? ((lane & 1) ? 0xffff0000u : 0x0000ffffu) : 0u;
  unsigned tag;

  if (__builtin_expect(w == NW, 1)) {
    __builtin_amdgcn_s_setprio(3);
    unsigned xd[5], xq[3];
#pragma unroll
    for (int k = 0; k < 5; ++k) xd[k] = __builtin_amdgcn_raw_buffer_load_b32(rx, lane * 4 + k * 256, 0, 0);
#pragma unroll
    for (int k = 0; k < 3; ++k) xq[k] = __builtin_amdgcn_raw_buffer_load_b16(rx, (144 * (s >> 2) + lane + 64 * k) * 2, 0, 0);
    const unsigned epoch = __builtin_amdgcn_raw_buffer_load_b32(rws, lane == 0 ? WS_EPOCH : OOB, 0, AUX_SC1);
    v4u wqv[18];
    {
      const __amdgpu_buffer_rsrc_t rq = mk_rsrc(wq, DM * QD * 2);
      const int o = (144 * (s >> 2) + (lane >> 3)) * (QD * 2) + (g * 4 + (s & 3)) * 128 + (lane & 7) * 16;
#pragma unroll
      for (int i = 0; i < 18; ++i) wqv[i] = __builtin_amdgcn_raw_buffer_load_b128(rq, o, i * 8 * QD * 2, AUX_NT);
    }
    if (lane < 16) ldu[L_FLAG + lane] = 0u;
    asm volatile("s_waitcnt lgkmcnt(0)" ::: "memory");
    __builtin_amdgcn_sched_barrier(0);
    __builtin_amdgcn_s_barrier();
    __builtin_amdgcn_sched_barrier(0);
    float mb, den;
    {
      float xv[10];
#pragma unroll
      for (int k = 0; k < 5; ++k) { xv[2 * k] = lo_f(xd[k]); xv[2 * k + 1] = hi_f(xd[k]); }
      float sm = 0.f;
#pragma unroll
      for (int k = 0; k < 10; ++k) sm += xv[k];
      const float mean = wave_sum(sm) * (1.0f / DM);
      float sq = 0.f;
#pragma unroll
      for (int k = 0; k < 8; ++k) { const float d = xv[k] - mean; sq = fmaf(d, d, sq); }
      const float d8 = lane < 32 ? xv[8] - mean : 0.f, d9 = lane < 32 ? xv[9] - mean : 0.f;
      sq = fmaf(d8, d8, sq); sq = fmaf(d9, d9, sq);
      const float var = wave_sum(sq) * (1.0f / DM);
      mb = rnd(mean);
      den = rnd(sqrtf(rnd(rnd(var) + 1e-5f)));
    }
    unsigned xn[3];
#pragma unroll
    for (int k = 0; k < 3; ++k) { const float v = rnd(lo_f(xq[k]) - mb) / den; xn[k] = pk_bf16(v, v); }
    STAMP(1);
    unsigned xr[18];
#pragma unroll
    for (int i = 0; i < 18; ++i) xr[i] = __shfl(xn[i >> 3], (8 * i + (lane >> 3)) & 63);
#pragma unroll
    for (int i = 0; i < 18; ++i) asm volatile("" : "+v"(xr[i]));
    __builtin_amdgcn_sched_barrier(0);
    f32x4 qa = {0.f, 0.f, 0.f, 0.f};
#pragma unroll
    for (int i = 0; i < 18; ++i) { qa = mfma16(and4(xr[i], gm), wqv[i], qa); __builtin_amdgcn_sched_barrier(0); }
    qa = gemv_fin(qa, lane);
    tag = __builtin_amdgcn_readfirstlane(epoch) + 1u;
    {
      const bool up2 = (lane & 8) != 0;
      const int col = (lane & 7) * 8 + 4 * ((lane >> 4) & 1) + (up2 ? 2 : 0);
      __builtin_amdgcn_raw_buffer_store_b128(v4u{__float_as_uint(up2 ? qa[2] : qa[0]), tag, __float_as_uint(up2 ? qa[3] : qa[1]), tag}, rws,
                                             lane < 32 ? WS_QG + ((g * 4 + (s >> 2)) * 256 + (s & 3) * 64 + col) * 8 : OOB, 0, AUX_SC1);
    }
    STAMP(2);
    if (lane == 0) ldu[L_FLAG + F_TAG] = tag;
    if (is1) {
#pragma unroll
      for (int k = 0; k < 5; ++k) {
        const float v0 = rnd(lo_f(xd[k]) - mb) / den, v1 = rnd(hi_f(xd[k]) - mb) / den;
        *reinterpret_cast<v2u*>(&ldu[L_XN + 2 * (lane + 64 * k)]) = v2u{pk_bf16(v0, v0), pk_bf16(v1, v1)};
      }
      set_flag(F_XRDY);
    }
    for (int i = 0; i < KQD; ++i) __builtin_amdgcn_s_sleep(8);
    float q4[4];
    for (unsigned spins = 0;; ++spins) {
      asm volatile("" ::: "memory");
      v4u a[8];
#pragma unroll
      for (int r = 0; r < 4; ++r) {
        a[2 * r] = __builtin_amdgcn_raw_buffer_load_b128(rws, WS_QG + g * 8192 + lane * 32, r * 2048, AUX_SC1);
        a[2 * r + 1] = __builtin_amdgcn_raw_buffer_load_b128(rws, WS_QG + g * 8192 + lane * 32 + 16, r * 2048, AUX_SC1);
      }
      __builtin_amdgcn_sched_barrier(0);
      set_flag(F_PISS);
      __builtin_amdgcn_sched_barrier(0);
      unsigned bad = 0;
#pragma unroll
      for (int j = 0; j < 4; ++j) q4[j] = 0.f;
#pragma unroll
      for (int r = 0; r < 4; ++r) {
        bad |= (a[2 * r].y ^ tag) | (a[2 * r].w ^ tag) | (a[2 * r + 1].y ^ tag) | (a[2 * r + 1].w ^ tag);
        q4[0] += __uint_as_float(a[2 * r].x); q4[1] += __uint_as_float(a[2 * r].z);
        q4[2] += __uint_as_float(a[2 * r + 1].x); q4[3] += __uint_as_float(a[2 * r + 1].z);
      }
      if (__all(bad == 0) || spins > SPIN_CAP) break;
      __builtin_amdgcn_s_sleep(1);
    }
    *reinterpret_cast<v2u*>(&ldu[L_Q + 2 * lane]) = v2u{pk_bf16(q4[0], q4[1]), pk_bf16(q4[2], q4[3])};
    set_flag(F_QRDY);
    STAMP(3);
    __builtin_amdgcn_s_setprio(0);
    wait_flag(F_P1CNT, NW);
    float M0, M1, L0, L1;
    {
      float mw = lds[L_MW + (lane & 15)];
      float mx = fmaxf(mw, dpp<XOR2>(mw)); mx = fmaxf(mx, dpp<ROR4>(mx)); mx = fmaxf(mx, dpp<XOR8>(mx));
      M0 = __uint_as_float(__builtin_amdgcn_readlane(__float_as_uint(mx), 0));
      M1 = __uint_as_float(__builtin_amdgcn_readlane(__float_as_uint(mx), 1));
      const float ew = __builtin_amdgcn_exp2f(mw - mx);
      f32x4 hs = {0.f, 0.f, 0.f, 0.f};
#pragma unroll
      for (int ww = NSW; ww < NW; ++ww) hs += __shfl(ew, 2 * ww + (lane >> 5)) * *reinterpret_cast<const f32x4*>(&lds[L_ACC + ww * 256 + lane * 4]);
      *reinterpret_cast<f32x4*>(&lds[L_ACC + NW * 256 + lane * 4]) = hs;
      if (lane == 0) *reinterpret_cast<f32x2*>(&lds[L_M]) = f32x2{M0, M1};
      set_flag(F_MRDY);
      const float mh = (lane & 1) ? M1 : M0;
      float l = 0.f;
#pragma unroll 2
      for (int i = 0; i < (SC_TILES + 7) / 8; ++i) {
        const f32x4 v = *reinterpret_cast<const f32x4*>(&lds[L_SC + i * 256 + lane * 4]);
        l += (__builtin_amdgcn_exp2f(v[0] - mh) + __builtin_amdgcn_exp2f(v[1] - mh)) + (__builtin_amdgcn_exp2f(v[2] - mh) + __builtin_amdgcn_exp2f(v[3] - mh));
      }
      l += dpp<XOR2>(l); l += dpp<ROR4>(l); l += dpp<XOR8>(l); l = sum_x32(sum_x16(l));
      L0 = __uint_as_float(__builtin_amdgcn_readlane(__float_as_uint(l), 0));
      L1 = __uint_as_float(__builtin_amdgcn_readlane(__float_as_uint(l), 1));
    }
    STAMP(4);
    if (KWARM & 1) {
      const __amdgpu_buffer_rsrc_t ro = mk_rsrc(out, DM * 2);
      unsigned t0 = __builtin_amdgcn_raw_buffer_load_b32(rws, lane < 36 ? lane * 65536 + b * 128 : OOB, 0, AUX_SC1);
      unsigned t1 = __builtin_amdgcn_raw_buffer_load_b32(ro, lane == 0 && b < NRED ? b * 32 : OOB, 0, AUX_SC1);
      asm volatile("" :: "v"(t0), "v"(t1));
    }
    if (KWARM & 2) {
      unsigned t0 = __builtin_amdgcn_raw_buffer_load_b32(rws, lane < 36 ? WS_P2 + (lane * NWG + b) * 128 : lane < 52 ? WS_P1O + ((g * 16 + lane - 36) * 16 + s) * 128 : OOB, 0, AUX_SC1);
      asm volatile("" :: "v"(t0));
    }
    const unsigned xp = __builtin_amdgcn_raw_buffer_load_b32(rx, lane < 8 ? b * 32 + lane * 4 : OOB, 0, 0);
    if (EXP_NOTAIL) { if (b == 0 && lane == 0) { wait_flag(F_ENDCNT, NSW); __builtin_amdgcn_raw_buffer_store_b32(tag, rws, WS_EPOCH, 0, AUX_SC1); } if (L0 + L1 == 1.2345f) out[0] = 1; return; }
    wait_flag(F_PROG, 128 - NEART);
    __builtin_amdgcn_raw_buffer_store_b128(v4u{__float_as_uint(lane ? M1 : M0), tag, __float_as_uint(lane ? L1 : L0), tag}, rws,
                                           lane < 2 ? WS_P1M + ((g * 2 + lane) * 16 + s) * 16 : OOB, 0, AUX_SC1);
    if (is2) {
      wait_flag(F_FDONE, 3);
#pragma unroll
      for (int k = 0; k < 5; ++k) {
        const int t = k * 64 + lane;
        const f32x2 yf = *reinterpret_cast<const f32x2*>(&lds[L_YF + 2 * (t < 288 ? t : 0)]);
        __builtin_amdgcn_raw_buffer_store_b128(v4u{__float_as_uint(yf.x), tag, __float_as_uint(yf.y), tag}, rws,
                                               t < 288 ? WS_PF + ((t >> 3) * NFW + f2) * 128 + (t & 7) * 16 : OOB, 0, AUX_SC1);
      }
    }
    STAMP(5);
    if (KLATEPOLL) wait_flag(F_ENDCNT, NSW);
    {
      v4u ml;
      for (unsigned spins = 0;; ++spins) {
        asm volatile("" ::: "memory");
        ml = __builtin_amdgcn_raw_buffer_load_b128(rws, WS_P1M + ((g * 2 + (s >> 3)) * 16 + c) * 16, 0, AUX_SC1);
        if (__all(((ml.y ^ tag) | (ml.w ^ tag)) == 0) || spins > SPIN_CAP) break;
        __builtin_amdgcn_s_sleep(2);
      }
      const float mp = __uint_as_float(ml.x);
      float M = fmaxf(mp, dpp<XOR1>(mp)); M = fmaxf(M, dpp<XOR2>(M)); M = fmaxf(M, dpp<ROR4>(M)); M = fmaxf(M, dpp<XOR8>(M));
      const float e = __builtin_amdgcn_exp2f(mp - M);
      float L = e * __uint_as_float(ml.z);
      L += dpp<XOR1>(L); L += dpp<XOR2>(L); L += dpp<ROR4>(L); L += dpp<XOR8>(L);
      if (lane < 16) lds[L_WT + lane] = e / L;
      set_flag(F_WRDY);
    }
    STAMP(6);
    if (b < NRED) {
      float f0, f1v;
      for (unsigned spins = 0;; ++spins) {
        asm volatile("" ::: "memory");
        v4u pf[9];
#pragma unroll
        for (int k = 0; k < 9; ++k) pf[k] = __builtin_amdgcn_raw_buffer_load_b128(rws, WS_PF + b * NFW * 128 + lane * 16, k * 1024, AUX_SC1);
        unsigned bad = 0; f0 = 0.f; f1v = 0.f;
#pragma unroll
        for (int k = 0; k < 9; ++k) { bad |= (pf[k].y ^ tag) | (pf[k].w ^ tag); f0 += __uint_as_float(pf[k].x); f1v += __uint_as_float(pf[k].z); }
        if (__all(bad == 0) || spins > SPIN_CAP) break;
        __builtin_amdgcn_s_sleep(2);
      }
      f0 += dpp<XOR8>(f0); f0 = rnd(sum_x32(sum_x16(f0)));
      f1v += dpp<XOR8>(f1v); f1v = rnd(sum_x32(sum_x16(f1v)));
      if (lane < 8) {
        *reinterpret_cast<f32x2*>(&lds[L_G + lane * 2]) = f32x2{rnd(f0 / (1.0f + __expf(-f0))), rnd(f1v / (1.0f + __expf(-f1v)))};
        ldu[L_G + 16 + lane] = xp;
      }
      set_flag(F_GRDY);
      STAMP(7);
    }
    STAMP_FLUSH();
    return;
  }

  static_assert(2048 - 16 * NSW * K_O == 32, "unit split");
  const int nrole = is1 ? 4 : is2 ? 3 : 0;
  int ufirst, nt;
  {
    const int fr = sr - 9 - ns2;
    const int hu = is1 ? 1 : is2 ? 2 : (g & 1) ? 5 : 7 - fr;
    const int h0 = is1 ? sr : is2 ? 9 + 2 * (sr - 9) : 9 + 2 * ns2 + ((g & 1) ? 5 : 7) * fr;
    const int ubase = NSW * K_O * sr + h0;
    const int j = w - NSW - nrole, nh = NHELP - nrole;
    if (w < NSW) {
      ufirst = ubase + hu + w; nt = 2 * K_O;
    } else {
      const int n1 = hu - nh;
      ufirst = ubase + (j < n1 ? 2 * j : n1 + j); nt = j < n1 ? 4 : 2;
    }
  }
  const int ustep = w < NSW ? NSW : 1;
  const int sbase = w < NSW ? 4 * NHELP + w * 2 * K_O : (w - NSW) * 4;
  const int voff = lane * 16;
  auto no_keys = [&]() {
    *reinterpret_cast<f32x2*>(&lds[L_SC + sbase * 32 + lane * 2]) = f32x2{-INFINITY, -INFINITY};
    *reinterpret_cast<f32x4*>(&lds[L_ACC + w * 256 + lane * 4]) = f32x4{0.f, 0.f, 0.f, 0.f};
    if (lane < 2) lds[L_MW + 2 * w + lane] = -INFINITY;
    count_up(F_P1CNT);
  };
  __builtin_amdgcn_sched_barrier(0);
  __builtin_amdgcn_s_barrier();
  __builtin_amdgcn_sched_barrier(0);

  if (w >= NSW && w < NSW + nrole) {
    const int fw = w - NSW;
    v4u fr[24];
    if (is1) {
      const __amdgpu_buffer_rsrc_t rfg = mk_rsrc(wgate, EXP_NOFFN ? 0 : DM * FF * 2);
      const __amdgpu_buffer_rsrc_t rfu = mk_rsrc(wup, EXP_NOFFN ? 0 : DM * FF * 2);
      const int row0 = 288 * (f1 & 1) + 72 * fw + (lane >> 3);
      const int foff = row0 * (FF * 2) + (f1 >> 1) * 128 + (lane & 7) * 16;
#pragma unroll
      for (int i = 0; i < 9; ++i) {
        fr[2 * i] = __builtin_amdgcn_raw_buffer_load_b128(rfg, foff, i * 8 * FF * 2, AUX_NT);
        fr[2 * i + 1] = __builtin_amdgcn_raw_buffer_load_b128(rfu, foff, i * 8 * FF * 2, AUX_NT);
      }
      __builtin_amdgcn_sched_barrier(0);
      no_keys();
      wait_flag(F_XRDY, 1);
      tag = ldu[L_FLAG + F_TAG];
      f32x4 ag = {0.f, 0.f, 0.f, 0.f}, au = {0.f, 0.f, 0.f, 0.f};
#pragma unroll
      for (int i = 0; i < 9; ++i) {
        const v4u av = and4(ldu[L_XN + row0 + 8 * i], gm);
        ag = mfma16(av, fr[2 * i], ag); au = mfma16(av, fr[2 * i + 1], au);
      }
      ag = gemv_fin(ag, lane); au = gemv_fin(au, lane);
      if ((lane & 8) == 0) *reinterpret_cast<f32x4*>(&lds[L_FR + fw * 128 + (lane >> 5) * 64 + (lane & 7) * 8 + 4 * ((lane >> 4) & 1)]) = lane < 32 ? ag : au;
      int old = lds_inc(lds0 + (L_FLAG + F_S1CNT) * 4);
      float gs = 0.f, us = 0.f;
#pragma unroll
      for (int ww = 0; ww < 4; ++ww) { gs += lds[L_FR + ww * 128 + lane]; us += lds[L_FR + ww * 128 + 64 + lane]; }
      asm volatile("s_waitcnt lgkmcnt(0)" : "+v"(old), "+v"(gs), "+v"(us));
      if (__builtin_amdgcn_readfirstlane(old) == 3)
        __builtin_amdgcn_raw_buffer_store_b128(v4u{__float_as_uint(gs), tag, __float_as_uint(us), tag}, rws, WS_GU + (f1 * 64 + lane) * 16, 0, AUX_SC1);
    } else {
      const __amdgpu_buffer_rsrc_t rwd = mk_rsrc(wdown + (size_t)f2 * 64 * DM, EXP_NOFFN ? 0 : 64 * DM * 2);
      const int doff = (lane >> 3) * (DM * 2) + fw * 384 + (lane & 7) * 16;
#pragma unroll
      for (int rg = 0; rg < 8; ++rg)
#pragma unroll
        for (int k = 0; k < 3; ++k) fr[rg * 3 + k] = __builtin_amdgcn_raw_buffer_load_b128(rwd, doff + k * 128, rg * 8 * DM * 2, AUX_NT);
      __builtin_amdgcn_sched_barrier(0);
      no_keys();
      wait_flag(F_QRDY, 1);
      tag = ldu[L_FLAG + F_TAG];
      for (int i = 0; i < KHD; ++i) __builtin_amdgcn_s_sleep(8);
      v4u p0, p1;
      for (unsigned spins = 0;; ++spins) {
        asm volatile("" ::: "memory");
        p0 = __builtin_amdgcn_raw_buffer_load_b128(rws, WS_GU + (f2 * 128 + lane) * 16, 0, AUX_SC1);
        p1 = __builtin_amdgcn_raw_buffer_load_b128(rws, WS_GU + (f2 * 128 + lane) * 16, 1024, AUX_SC1);
        if (__all(((p0.y ^ tag) | (p0.w ^ tag) | (p1.y ^ tag) | (p1.w ^ tag)) == 0) || spins > SPIN_CAP) break;
        __builtin_amdgcn_s_sleep(16);
      }
      const float gt = rnd(__uint_as_float(p0.x) + __uint_as_float(p1.x)), up = rnd(__uint_as_float(p0.z) + __uint_as_float(p1.z));
      const float inner = 0.7978845608028654f * (gt + 0.044715f * gt * gt * gt);
      const float hv = rnd(0.5f * gt * (1.0f + tanhf(inner))) * up;
      const unsigned hp = pk_bf16(hv, hv);
      f32x4 ya[3];
#pragma unroll
      for (int k = 0; k < 3; ++k) ya[k] = f32x4{0.f, 0.f, 0.f, 0.f};
#pragma unroll
      for (int rg = 0; rg < 8; ++rg) {
        const v4u av = and4(__shfl(hp, rg * 8 + (lane >> 3)), gm);
#pragma unroll
        for (int k = 0; k < 3; ++k) ya[k] = mfma16(av, fr[rg * 3 + k], ya[k]);
      }
#pragma unroll
      for (int k = 0; k < 3; ++k) {
        ya[k] = gemv_fin(ya[k], lane);
        if (lane < 32 && (lane & 8) == 0) *reinterpret_cast<f32x4*>(&lds[L_YF + fw * 192 + k * 64 + (lane & 7) * 8 + 4 * (lane >> 4)]) = ya[k];
      }
      count_up(F_FDONE);
    }
    STAMP(4);
  } else {
    const __amdgpu_buffer_rsrc_t rk = mk_rsrc(kg + (size_t)g * SK * 128, SK * 256);
    const __amdgpu_buffer_rsrc_t rv = mk_rsrc(vg + (size_t)g * SK * 128, SK * 256);
#define TOFF(t) ((ufirst + ((t) >> 1) * ustep) << 13)
#define tb(n, i) buf[(n) * TL + (i)]
    v4u buf[(D2 > 4 ? D2 : 4) * TL];
    if (!KKEARLY && nrole) wait_flag(F_PISS, 1);
    {
      const int t0 = nt > 0 ? TOFF(0) : OOB;
#pragma unroll
      for (int i = 0; i < 2 * TL; ++i) buf[i] = __builtin_amdgcn_raw_buffer_load_b128(rk, voff + i * 1024, t0, AUX_NT);
    }
    __builtin_amdgcn_sched_barrier(0);
    {
      const int cap = w < NSW ? 2 * K_O + (w == NSW - 1 ? 8 : 0) : 4;
#pragma unroll 1
      for (int t = nt; t < cap; t += 2) lds[L_SC + (sbase + t) * 32 + lane] = -INFINITY;
    }
    STAMP(1);
    const int hf = (c >> 2) & 1;
    v4u qf, qs;
    auto get_q = [&]() {
      wait_flag(F_QRDY, 1);
      STAMP(3);
      qf = *reinterpret_cast<const v4u*>(&ldu[L_Q + hf * 64 + 4 * c]);
      qs = *reinterpret_cast<const v4u*>(&ldu[L_Q + (hf ^ 1) * 64 + 4 * c]);
    };
    constexpr float LOG2E = 1.4426950408889634f, SCALE = 0.08838834764831845f;
    const bool b0 = (c & 1) != 0, b1 = (c & 2) != 0;
    float* const scw = &lds[L_SC + sbase * 32 + (lane >> 4) * 8 + (lane & 7)];
    float m = -INFINITY;
    float y[4];
    auto qk1 = [&](const v4u* kt) {
#pragma unroll
      for (int i = 0; i < TL; ++i) {
        float xf = 0.f, xs = 0.f;
#pragma unroll
        for (int j = 0; j < 4; ++j) { xf = dot2(kt[i][j], qf[j], xf); xs = dot2(kt[i][j], qs[j], xs); }
        y[i] = xf + dpp<XOR7>(xs);
      }
      asm volatile("" : "+v"(y[0]), "+v"(y[1]), "+v"(y[2]), "+v"(y[3]));
      __builtin_amdgcn_sched_barrier(0);
    };
    auto qk2 = [&](int t) {
      const float u0 = (b0 ? y[1] : y[0]) + dpp<XOR1>(b0 ? y[0] : y[1]);
      const float u1 = (b0 ? y[3] : y[2]) + dpp<XOR1>(b0 ? y[2] : y[3]);
      float sc = (b1 ? u1 : u0) + dpp<XOR2>(b1 ? u0 : u1);
      sc += dpp<XOR8>(sc);
      const float tl2 = rnd(rnd(sc) * SCALE) * LOG2E;
      m = fmaxf(m, tl2);
      scw[t * 32] = tl2;
    };
    int tk = 0;
    if (KOPEN && w < NSW) {
      wait_flag(F_PISS, 1);
      const int t2 = TOFF(2), t4 = TOFF(4);
#pragma unroll
      for (int i = 0; i < 2 * TL; ++i) buf[2 * TL + i] = __builtin_amdgcn_raw_buffer_load_b128(rk, voff + i * 1024, t2, AUX_NT);
      __builtin_amdgcn_sched_barrier(0);
      get_q();
#pragma unroll
      for (int n = 0; n < 2; ++n) {
        qk1(&tb(n, 0));
#pragma unroll
        for (int i = 0; i < TL; ++i) tb(n, i) = __builtin_amdgcn_raw_buffer_load_b128(rk, voff + i * 1024, t4 + n * 4096, AUX_NT);
        __builtin_amdgcn_sched_barrier(0);
        qk2(n);
      }
      TRACE(0);
      qk1(&tb(2, 0)); qk2(2);
      qk1(&tb(3, 0)); qk2(3);
      TRACE(1);
      tk = 4;
    } else get_q();
    unsigned next = (unsigned)__builtin_amdgcn_s_memrealtime();
#pragma unroll 1
    for (int t = tk; t < nt; t += 2) {
      const bool lastk = t + 2 >= nt;
      const __amdgpu_buffer_rsrc_t rn = lastk ? rv : rk;
      const int nxt = TOFF(lastk ? 0 : t + 2);
#pragma unroll
      for (int n = 0; n < 2; ++n) {
        qk1(&tb(n, 0));
        if (n == 0 && KTK) pace(next, KTK, KBURST);
#pragma unroll
        for (int i = 0; i < TL; ++i) tb(n, i) = __builtin_amdgcn_raw_buffer_load_b128(rn, voff + i * 1024, nxt + n * 4096, AUX_NT);
        __builtin_amdgcn_sched_barrier(0);
        qk2(t + n);
      }
      TRACE(t >> 1);
    }
    m = fmaxf(m, dpp<XOR1>(m)); m = fmaxf(m, dpp<XOR2>(m));
    m = max_x32(max_x16(m));
    if ((lane & 59) == 0) lds[L_MW + 2 * w + (lane >> 2)] = m;
    if (w < NSW) count_up(F_P1CNT);
    f32x4 acc = {0.f, 0.f, 0.f, 0.f};
    unsigned amask[4];
    {
      const int jp = (c & 3) + 4 * (c >> 3);
#pragma unroll
      for (int r = 0; r < 4; ++r) amask[r] = (jp >> 1) == r ? ((jp & 1) ? 0xffff0000u : 0x0000ffffu) : 0u;
    }
    auto pv = [&](float sv, const v4u* vt) {
      const float p = __builtin_amdgcn_exp2f(sv - m);
      const unsigned pp = pk_bf16(p, p);
#pragma unroll
      for (int i = 0; i < TL; ++i) {
        const unsigned pb = i == 0 ? dppu<0x00>(pp) : i == 1 ? dppu<0x55>(pp) : i == 2 ? dppu<0xAA>(pp) : dppu<0xFF>(pp);
        acc = mfma16(and4(pb, amask), vt[i], acc);
      }
      asm volatile("" : "+v"(acc));
      __builtin_amdgcn_sched_barrier(0);
    };
#define VOFF(t) ((t) < nt ? TOFF(t) + ((t) & 1) * 4096 : OOB)
    int t = 0;
    auto vrun = [&](int lim, unsigned tau) {
#pragma unroll 1
      for (; t < lim; t += 2) {
        const int nxt = TOFF(t + 2);
        const float s0 = scw[t * 32], s1 = scw[t * 32 + 32];
#pragma unroll
        for (int n = 0; n < 2; ++n) {
          pv(n ? s1 : s0, &tb(n, 0));
          if (n == 0 && KTV) pace(next, tau, KBURST);
#pragma unroll
          for (int i = 0; i < TL; ++i) tb(n, i) = __builtin_amdgcn_raw_buffer_load_b128(rv, voff + i * 1024, nxt + n * 4096, AUX_NT);
          __builtin_amdgcn_sched_barrier(0);
        }
        TRACE((nt + t) >> 1);
        if (w == 0 && lane == 0) *(volatile LAS int*)&ldu[L_FLAG + F_PROG] = t + 2 + 128 - nt;
      }
    };
    vrun(nt - XMID, KTV);
#pragma unroll
    for (int k = 2; k < D2; ++k) {
      const int o = VOFF(t + k);
#pragma unroll
      for (int i = 0; i < TL; ++i) tb(k, i) = __builtin_amdgcn_raw_buffer_load_b128(rv, voff + i * 1024, o, AUX_NT);
    }
    __builtin_amdgcn_sched_barrier(0);
    float ed = 1.f;
    if (w < NSW) {
      wait_flag(F_MRDY, 1);
      const int hh = (lane >> 4) & 1;
      ed = __builtin_amdgcn_exp2f(lds[L_MW + 2 * w + hh] - lds[L_M + hh]);
    }
    tag = ldu[L_FLAG + F_TAG];
#pragma unroll 1
    for (; t < nt; t += D2) {
      if (w == 0 && lane == 0) *(volatile LAS int*)&ldu[L_FLAG + F_PROG] = t + D2 - 2 + 128 - nt;
#pragma unroll
      for (int k = 0; k < D2; ++k) {
        const float sv = t + k < nt ? scw[(t + k) * 32] : -INFINITY;
        const int o = VOFF(t + k + D2);
        pv(sv, &tb(k, 0));
#pragma unroll
        for (int i = 0; i < TL; ++i) tb(k, i) = __builtin_amdgcn_raw_buffer_load_b128(rv, voff + i * 1024, o, AUX_NT);
        __builtin_amdgcn_sched_barrier(0);
      }
    }
    STAMP(4);
    const int fo = ((lane >> 4) & 1) * 128 + 8 * c + 4 * (lane >> 5);
    *reinterpret_cast<f32x4*>(&lds[L_ACC + w * 256 + fo]) = acc * ed;
    if (w >= NSW) count_up(F_P1CNT);
    else {
      int old = lds_inc(lds0 + (L_FLAG + F_ENDCNT) * 4);
      f32x4 o = *reinterpret_cast<const f32x4*>(&lds[L_ACC + NW * 256 + fo]);
#pragma unroll
      for (int ww = 0; ww < NSW; ++ww) o += *reinterpret_cast<const f32x4*>(&lds[L_ACC + ww * 256 + fo]);
      asm volatile("s_waitcnt lgkmcnt(0)" : "+v"(old), "+v"(o));
      if (EXP_NOTAIL) { if (o[0] == 1.2345f) out[1] = 2; return; }
      if (__builtin_amdgcn_readfirstlane(old) == NSW - 1) {
        const int po = WS_P1O + (((g * 16 + (fo >> 4)) * 16 + s) * 16 + (fo & 15)) * 8;
        __builtin_amdgcn_raw_buffer_store_b128(v4u{__float_as_uint(o[0]), tag, __float_as_uint(o[1]), tag}, rws, po, 0, AUX_SC1);
        __builtin_amdgcn_raw_buffer_store_b128(v4u{__float_as_uint(o[2]), tag, __float_as_uint(o[3]), tag}, rws, po + 16, 0, AUX_SC1);
      }
      STAMP(7);
    }
  }

  if (w >= NSW) {
    const int t = (w - NSW) * 64 + lane;
    float wf[32];
    {
      const __amdgpu_buffer_rsrc_t rwo = mk_rsrc(wo + (size_t)b * 16 * DM, 16 * DM * 2);
      unsigned wod[16];
#pragma unroll
      for (int j = 0; j < 16; ++j) wod[j] = __builtin_amdgcn_raw_buffer_load_b32(rwo, t < 288 ? t * 4 : OOB, j * DM * 2, AUX_NT);
#pragma unroll
      for (int j = 0; j < 16; ++j) { wf[2 * j] = lo_f(wod[j]); wf[2 * j + 1] = hi_f(wod[j]); }
#pragma unroll
      for (int j = 0; j < 32; ++j) asm volatile("" : "+v"(wf[j]));
    }
    if (EXP_NOTAIL) { if (wf[0] + wf[31] == 1.2345f) out[2] = 2; return; }
#pragma unroll 1
    for (unsigned spins = 0; spins < (SPIN_CAP << 4); ++spins) {
      if (__builtin_amdgcn_readfirstlane(*(volatile LAS unsigned*)&ldu[L_FLAG + F_PROG]) >= 128u - NEART) break;
      __builtin_amdgcn_s_sleep(8);
    }
    wait_flag(F_WRDY, 1);
    tag = ldu[L_FLAG + F_TAG];
    const float wt = lds[L_WT + (lane >> 2)];
    STAMP(8);
    int oo = WS_P1O + ((g * 16 + s) * 16 + (lane >> 2)) * 128 + (lane & 3) * 32;
    f32x4 ov = {0.f, 0.f, 0.f, 0.f};
    for (unsigned spins = 0;; ++spins) {
      asm volatile("" ::: "memory");
      const v4u a0 = __builtin_amdgcn_raw_buffer_load_b128(rws, oo, 0, AUX_SC1);
      const v4u a1 = __builtin_amdgcn_raw_buffer_load_b128(rws, oo, 16, AUX_SC1);
      const bool ok = oo != OOB && ((a0.y ^ tag) | (a0.w ^ tag) | (a1.y ^ tag) | (a1.w ^ tag)) == 0;
      if (ok) { ov = f32x4{__uint_as_float(a0.x), __uint_as_float(a0.z), __uint_as_float(a1.x), __uint_as_float(a1.z)}; oo = OOB; }
      if (__all(oo == OOB) || spins > SPIN_CAP) break;
    }
    STAMP(9);
#pragma unroll
    for (int k = 0; k < 4; ++k) { float v = ov[k] * wt; v += dpp<ROR4>(v); v += dpp<XOR8>(v); ov[k] = rnd(sum_x32(sum_x16(v))); }
    float y0 = 0.f, y1 = 0.f;
#pragma unroll
    for (int k = 0; k < 4; ++k) {
      y0 = fmaf(dpp<0x00>(ov[k]), wf[2 * k], y0);        y1 = fmaf(dpp<0x00>(ov[k]), wf[2 * k + 1], y1);
      y0 = fmaf(dpp<0x55>(ov[k]), wf[8 + 2 * k], y0);    y1 = fmaf(dpp<0x55>(ov[k]), wf[8 + 2 * k + 1], y1);
      y0 = fmaf(dpp<0xAA>(ov[k]), wf[16 + 2 * k], y0);   y1 = fmaf(dpp<0xAA>(ov[k]), wf[16 + 2 * k + 1], y1);
      y0 = fmaf(dpp<0xFF>(ov[k]), wf[24 + 2 * k], y0);   y1 = fmaf(dpp<0xFF>(ov[k]), wf[24 + 2 * k + 1], y1);
    }
    __builtin_amdgcn_raw_buffer_store_b128(v4u{__float_as_uint(y0), tag, __float_as_uint(y1), tag}, rws,
                                           t < 288 ? WS_P2 + ((t >> 3) * NWG + b) * 128 + (t & 7) * 16 : OOB, 0, AUX_SC1);
    STAMP(10);
  }

  if (b < NRED) {
    if (w < NSW) wait_flag(F_WRDY, 1);
    int o4[4];
    float sv[8];
#pragma unroll
    for (int k = 0; k < 4; ++k) { o4[k] = WS_P2 + b * NWG * 128 + (w * 64 + lane) * 16 + k * 8192; sv[2 * k] = 0.f; sv[2 * k + 1] = 0.f; }
    for (unsigned spins = 0;; ++spins) {
      asm volatile("" ::: "memory");
      v4u p2[4];
#pragma unroll
      for (int k = 0; k < 4; ++k) p2[k] = __builtin_amdgcn_raw_buffer_load_b128(rws, o4[k], 0, AUX_SC1);
      bool done = true;
#pragma unroll
      for (int k = 0; k < 4; ++k) {
        const bool ok = o4[k] != OOB && ((p2[k].y ^ tag) | (p2[k].w ^ tag)) == 0;
        if (ok) { sv[2 * k] = __uint_as_float(p2[k].x); sv[2 * k + 1] = __uint_as_float(p2[k].z); o4[k] = OOB; }
        done = done && o4[k] == OOB;
      }
      if (__all(done) || spins > SPIN_CAP) break;
    }
    STAMP(2);
    float s0 = (sv[0] + sv[2]) + (sv[4] + sv[6]), s1 = (sv[1] + sv[3]) + (sv[5] + sv[7]);
    s0 += dpp<XOR8>(s0); s0 = sum_x32(sum_x16(s0));
    s1 += dpp<XOR8>(s1); s1 = sum_x32(sum_x16(s1));
    if (lane < 8) *reinterpret_cast<f32x2*>(&lds[L_FIN + w * 16 + lane * 2]) = f32x2{s0, s1};
    int old = lds_inc(lds0 + (L_FLAG + F_FINCNT) * 4);
    f32x2 t2 = *reinterpret_cast<const f32x2*>(&lds[L_FIN + lane * 2]);
    asm volatile("s_waitcnt lgkmcnt(0)" : "+v"(old), "+v"(t2));
    if (__builtin_amdgcn_readfirstlane(old) == NW - 1) {
      wait_flag(F_GRDY, 1);
      const f32x2 gv = *reinterpret_cast<const f32x2*>(&lds[L_G + (lane & 7) * 2]);
      const unsigned xp = ldu[L_G + 16 + (lane & 7)];
      t2.x += dpp<XOR8>(t2.x); t2.x = sum_x32(sum_x16(t2.x));
      t2.y += dpp<XOR8>(t2.y); t2.y = sum_x32(sum_x16(t2.y));
      const unsigned r = pk_bf16(lo_f(xp) + rnd(rnd(t2.x) * gv.x), hi_f(xp) + rnd(rnd(t2.y) * gv.y));
      if (lane < 8) *reinterpret_cast<unsigned*>(out + b * 16 + lane * 2) = r;
      if (b == 0 && lane == 0) __builtin_amdgcn_raw_buffer_store_b32(tag, rws, WS_EPOCH, 0, AUX_SC1);
      STAMP(11);
    }
  }
  STAMP_FLUSH();
}

}

extern "C" void kernel_launch(void* const* d_in, const int* in_sizes, int n_in, void* d_out, int out_size, void* d_ws, size_t ws_size,
                              hipStream_t stream) {
  (void)in_sizes; (void)n_in; (void)out_size; (void)ws_size;
  fused_decode<<<NWG, NTHR, 0, stream>>>((const unsigned short*)d_in[0], (const unsigned short*)d_in[1], (const unsigned short*)d_in[2],
                                         (const unsigned short*)d_in[3], (const unsigned short*)d_in[4], (const unsigned short*)d_in[6],
                                         (const unsigned short*)d_in[7], (const unsigned short*)d_in[8], (unsigned short*)d_out,
                                         (unsigned char*)d_ws);
}
```

```cpp
#include <hip/hip_runtime.h>
#include <hip/hip_bf16.h>
#include <stdint.h>

namespace {

typedef unsigned v4u __attribute__((ext_vector_type(4)));
typedef unsigned v2u __attribute__((ext_vector_type(2)));
typedef float f32x4 __attribute__((ext_vector_type(4)));
typedef float f32x2 __attribute__((ext_vector_type(2)));
typedef __bf16 bf16x8 __attribute__((ext_vector_type(8)));
typedef __bf16 bf16x2 __attribute__((ext_vector_type(2)));
#define GAS __attribute__((address_space(1)))
#define LAS __attribute__((address_space(3)))

#define KHD 8
#define KKEARLY 1
#define KKO 42
#define KQD 4
#define KXMID 4
#define KOUTSC 0
#define KTAU 656
#define KTAULO 636
#define KTAUHI 680
#define KTEND 7600
#define KCALM 15
#define KSTALL 100
#define KXMAX 28
#define KXC 90
#define KTAUN 652
#define KTCAL 16
#define KBURST 20
#define EXP_NOTAIL 0
#define EXP_NOFFN 0
#define EXP_P2 0
#define KPGAP 5
#define KNEAR 4
#define KPFE 0
#define KMLT 64
#define KP1D 3
#define KP2D 4
#define KD2 4
#define KOPEN 1

constexpr int DM = 576, SK = 65536, FF = 4608, QD = 4096;
constexpr int NWG = 256, NW = 8, NTHR = (NW + 1) * 64;
constexpr int NSW = 3;
constexpr int NHELP = NW - NSW;
constexpr int NRED = 36;
constexpr int NFW = 72;
constexpr int TL = 4;
constexpr int D2 = KD2;
constexpr int XMID = KXMID;
constexpr int K_O = KKO;
constexpr int NEART = KNEAR;
constexpr int OOB = 0x70000000;

constexpr int WS_EPOCH = 0;
constexpr int WS_QG = 4096;
constexpr int WS_P1M = WS_QG + 16 * 4 * 256 * 8;
constexpr int WS_P1O = WS_P1M + NWG * 128;
constexpr int WS_P2 = WS_P1O + 16 * 16 * 16 * 128;
constexpr int WS_PF = WS_P2 + NRED * NWG * 128;
constexpr int WS_GU = WS_PF + NRED * NFW * 128;
constexpr int WS_END = WS_GU + NFW * 2 * 64 * 16;

#define STAMP_DECL do {} while (0)
#define STAMP(i) do {} while (0)
#define STAMP_FLUSH() do {} while (0)
#define TRACE(i) do {} while (0)
constexpr int AUX_NT = 2, AUX_SC1 = 16;
constexpr int RSRC_W3 = 0x00020000;
constexpr unsigned SPIN_CAP = 1u << 14;

constexpr int L_FLAG = 0;
enum { F_TAG, F_QRDY, F_XRDY, F_FDONE, F_PISS, F_P1CNT, F_MRDY, F_NEAR, F_WRDY, F_ENDCNT, F_FINCNT, F_GRDY, F_PROG, F_S1CNT };
constexpr int L_MW = 16;
constexpr int L_M = L_MW + 16;
constexpr int L_WT = L_M + 4;
constexpr int L_G = L_WT + 16;
constexpr int L_Q = L_G + 28;
constexpr int L_XN = L_Q + 128;
constexpr int L_YF = L_XN + 640;
constexpr int L_FR = L_YF;
constexpr int L_FIN = L_YF + 576;
constexpr int L_ACC = L_FIN + 128;
constexpr int L_SC = L_ACC + 2304;
constexpr int SC_TILES = 4 * NHELP + NSW * 2 * K_O;
constexpr int L_FLOATS = 24 * 1024;
static_assert(L_SC % 4 == 0 && L_ACC % 4 == 0 && L_Q % 4 == 0 && L_SC + SC_TILES * 32 + 256 <= 22 * 1024, "LDS map");

template <int CTRL> __device__ __forceinline__ float dpp(float x) {
  return __builtin_bit_cast(float, __builtin_amdgcn_mov_dpp(__builtin_bit_cast(int, x), CTRL, 0xf, 0xf, true));
}
template <int CTRL> __device__ __forceinline__ unsigned dppu(unsigned x) {
  return (unsigned)__builtin_amdgcn_mov_dpp((int)x, CTRL, 0xf, 0xf, true);
}
constexpr int XOR1 = 0xB1, XOR2 = 0x4E, XOR7 = 0x141, XOR8 = 0x128, ROR4 = 0x124;
__device__ __forceinline__ float sum_x16(float x) {
  auto s = __builtin_amdgcn_permlane16_swap(__float_as_uint(x), __float_as_uint(x), false, false);
  return __uint_as_float(s[0]) + __uint_as_float(s[1]);
}
__device__ __forceinline__ float sum_x32(float x) {
  auto s = __builtin_amdgcn_permlane32_swap(__float_as_uint(x), __float_as_uint(x), false, false);
  return __uint_as_float(s[0]) + __uint_as_float(s[1]);
}
__device__ __forceinline__ float max_x16(float x) {
  auto s = __builtin_amdgcn_permlane16_swap(__float_as_uint(x), __float_as_uint(x), false, false);
  return fmaxf(__uint_as_float(s[0]), __uint_as_float(s[1]));
}
__device__ __forceinline__ float max_x32(float x) {
  auto s = __builtin_amdgcn_permlane32_swap(__float_as_uint(x), __float_as_uint(x), false, false);
  return fmaxf(__uint_as_float(s[0]), __uint_as_float(s[1]));
}
__device__ __forceinline__ float wave_sum(float x) {
  x += dpp<XOR1>(x); x += dpp<XOR2>(x); x += dpp<ROR4>(x); x += dpp<XOR8>(x);
  return sum_x32(sum_x16(x));
}
__device__ __forceinline__ unsigned pk_bf16(float lo, float hi) {
  bf16x2 v = {(__bf16)lo, (__bf16)hi};
  return __builtin_bit_cast(unsigned, v);
}
__device__ __forceinline__ float rnd(float x) { return (float)(__bf16)x; }
__device__ __forceinline__ float lo_f(unsigned u) { return __uint_as_float(u << 16); }
__device__ __forceinline__ float hi_f(unsigned u) { return __uint_as_float(u & 0xffff0000u); }
__device__ __forceinline__ float dot2(unsigned a, unsigned b, float c) {
  return __builtin_amdgcn_fdot2_f32_bf16(__builtin_bit_cast(bf16x2, a), __builtin_bit_cast(bf16x2, b), c, false);
}
__device__ __forceinline__ __amdgpu_buffer_rsrc_t mk_rsrc(const void* p, int bytes) {
  return __builtin_amdgcn_make_buffer_rsrc((void*)p, 0, bytes, RSRC_W3);
}
__device__ __forceinline__ v4u and4(unsigned x, const unsigned (&m)[4]) { return v4u{x & m[0], x & m[1], x & m[2], x & m[3]}; }
__device__ __forceinline__ f32x4 mfma16(v4u a, v4u bm, f32x4 cacc) {
  return __builtin_amdgcn_mfma_f32_16x16x32_bf16(__builtin_bit_cast(bf16x8, a), __builtin_bit_cast(bf16x8, bm), cacc, 0, 0, 0);
}
__device__ __forceinline__ f32x4 gemv_fin(f32x4 d, int lane) {
  const bool valid = (lane >> 5) == ((lane >> 3) & 1);
#pragma unroll
  for (int r = 0; r < 4; ++r) { float t = valid ? d[r] : 0.f; t += dpp<XOR8>(t); d[r] = sum_x32(t); }
  return d;
}
__device__ __forceinline__ int lds_inc(unsigned addr) {
  int r; unsigned long long save;
  asm volatile("s_mov_b64 %1, exec\n\ts_mov_b64 exec, 1\n\tds_add_rtn_u32 %0, %2, %3\n\ts_mov_b64 exec, %1"
               : "=&v"(r), "=&s"(save) : "v"(addr), "v"(1) : "memory");
  return r;
}
template <int N>
__device__ __forceinline__ void gather(__amdgpu_buffer_rsrc_t rs, int (&off)[N], unsigned tag, float (&val)[2 * N]) {
  v4u p[3][N];
  auto issue = [&](int j) {
    asm volatile("" ::: "memory");
#pragma unroll
    for (int k = 0; k < N; ++k) p[j][k] = __builtin_amdgcn_raw_buffer_load_b128(rs, off[k], 0, AUX_SC1);
    __builtin_amdgcn_sched_barrier(0);
  };
  auto check = [&](int j) {
    bool done = true;
#pragma unroll
    for (int k = 0; k < N; ++k) {
      const bool ok = off[k] != OOB && ((p[j][k].y ^ tag) | (p[j][k].w ^ tag)) == 0;
      val[2 * k] = ok ? __uint_as_float(p[j][k].x) : val[2 * k]; val[2 * k + 1] = ok ? __uint_as_float(p[j][k].z) : val[2 * k + 1];
      off[k] = ok ? OOB : off[k];
      done = done && off[k] == OOB;
    }
    return __all(done) != 0;
  };
#pragma unroll
  for (int k = 0; k < 2 * N; ++k) val[k] = 0.f;
  issue(0); __builtin_amdgcn_s_sleep(KPGAP); issue(1); __builtin_amdgcn_s_sleep(KPGAP);
#pragma unroll 1
  for (unsigned spins = 0; spins < SPIN_CAP; ++spins) {
    issue(2); if (check(0)) break;
    issue(0); if (check(1)) break;
    issue(1); if (check(2)) break;
  }
}
__device__ __forceinline__ void pace(unsigned& next, unsigned tau, unsigned& tend) {
  int d;
  asm volatile("1:\n\ts_memrealtime vcc\n\ts_waitcnt lgkmcnt(0)\n\ts_lshl_b32 %0, vcc_lo, 4\n\ts_sub_i32 %0, %0, %1\n\ts_cmp_ge_i32 %0, 0\n\ts_cbranch_scc1 2f\n\ts_sleep 1\n\ts_branch 1b\n2:"
               : "=&s"(d) : "s"(next) : "scc", "vcc");
  next += tau + (unsigned)max(0, d - KBURST * 16);
  tend += d > KSTALL * 16 ? d : 0;
}

__global__ void __launch_bounds__(NTHR, 3)
fused_decode(const unsigned short* __restrict__ xg, const unsigned short* __restrict__ kg, const unsigned short* __restrict__ vg,
             const unsigned short* __restrict__ wq, const unsigned short* __restrict__ wo, const unsigned short* __restrict__ wgate,
             const unsigned short* __restrict__ wup, const unsigned short* __restrict__ wdown, unsigned short* __restrict__ out,
             unsigned char* __restrict__ ws) {
  __shared__ __attribute__((aligned(16))) float lds[L_FLOATS];
  asm volatile("" :: "s"(xg), "s"(kg), "s"(vg), "s"(wq), "s"(wo), "s"(ws));
  const int lane = threadIdx.x & 63, pw = __builtin_amdgcn_readfirstlane(threadIdx.x >> 6);
  const int w = pw == 0 ? NW : pw - 1;
  const int b = blockIdx.x, g = b >> 4, s = b & 15;
  const int c = lane & 15;
  const int sr = (s + 3 * g) & 15, ns2 = 5 - (g & 1);
  const bool is1 = sr < 9, is2 = !is1 && sr < 9 + ns2;
  const int f1 = g * 9 + sr, f2 = (g >> 1) * 9 + (g & 1) * 5 + sr - 9;
  unsigned* const ldu = reinterpret_cast<unsigned*>(lds);
  const unsigned lds0 = (unsigned)(uintptr_t)(LAS float*)lds;
  STAMP_DECL;

  const __amdgpu_buffer_rsrc_t rws = mk_rsrc(ws, WS_END);
  const __amdgpu_buffer_rsrc_t rx = mk_rsrc(xg, DM * 2);

  STAMP(0);
  const unsigned tend0 = ((unsigned)__builtin_amdgcn_s_memrealtime() + KTEND) << 4;
  unsigned tend = tend0;
  auto wait_flag = [&](int idx, unsigned want) {
#pragma unroll 1
    for (unsigned spins = 0; spins < (SPIN_CAP << 6); ++spins) {
      if (__builtin_amdgcn_readfirstlane(*(volatile LAS unsigned*)&ldu[L_FLAG + idx]) >= want) break;
      __builtin_amdgcn_s_sleep(1);
    }
    asm volatile("" ::: "memory");
  };
  auto set_flag = [&](int idx) {
    asm volatile("" ::: "memory");
    if (lane == 0) *(volatile LAS unsigned*)&ldu[L_FLAG + idx] = 1u;
  };
  auto count_up = [&](int idx) {
    int r = lds_inc(lds0 + (L_FLAG + idx) * 4);
    asm volatile("s_waitcnt lgkmcnt(0)" : "+v"(r));
  };
  unsigned gm[4];
#pragma unroll
  for (int r = 0; r < 4; ++r) gm[r] = ((lane & 7) >> 1) == r ? ((lane & 1) ? 0xffff0000u : 0x0000ffffu) : 0u;
  unsigned tag;
  auto gate_vals = [&]() {
      const unsigned xp = __builtin_amdgcn_raw_buffer_load_b32(rx, lane < 8 ? b * 32 + lane * 4 : OOB, 0, 0);
      float f0, f1v;
      for (unsigned spins = 0;; ++spins) {
        asm volatile("" ::: "memory");
        v4u pf[9];
#pragma unroll
        for (int k = 0; k < 9; ++k) pf[k] = __builtin_amdgcn_raw_buffer_load_b128(rws, WS_PF + b * NFW * 128 + lane * 16, k * 1024, AUX_SC1);
        unsigned bad = 0; f0 = 0.f; f1v = 0.f;
#pragma unroll
        for (int k = 0; k < 9; ++k) { bad |= (pf[k].y ^ tag) | (pf[k].w ^ tag); f0 += __uint_as_float(pf[k].x); f1v += __uint_as_float(pf[k].z); }
        if (__all(bad == 0) || spins > SPIN_CAP) break;
        __builtin_amdgcn_s_sleep(8);
      }
      f0 += dpp<XOR8>(f0); f0 = rnd(sum_x32(sum_x16(f0)));
      f1v += dpp<XOR8>(f1v); f1v = rnd(sum_x32(sum_x16(f1v)));
      if (lane < 8) {
        *reinterpret_cast<f32x2*>(&lds[L_G + lane * 2]) = f32x2{rnd(f0 / (1.0f + __expf(-f0))), rnd(f1v / (1.0f + __expf(-f1v)))};
        ldu[L_G + 16 + lane] = xp;
      }
      STAMP(8);
  };

  if (__builtin_expect(w == NW, 1)) {
    __builtin_amdgcn_s_setprio(3);
    unsigned xd[5], xq[3];
#pragma unroll
    for (int k = 0; k < 5; ++k) xd[k] = __builtin_amdgcn_raw_buffer_load_b32(rx, lane * 4 + k * 256, 0, 0);
#pragma unroll
    for (int k = 0; k < 3; ++k) xq[k] = __builtin_amdgcn_raw_buffer_load_b16(rx, (144 * (s >> 2) + lane + 64 * k) * 2, 0, 0);
    const unsigned epoch = __builtin_amdgcn_raw_buffer_load_b32(rws, lane == 0 ? WS_EPOCH : OOB, 0, AUX_SC1);
    v4u wqv[18];
    {
      const __amdgpu_buffer_rsrc_t rq = mk_rsrc(wq, DM * QD * 2);
      const int o = (144 * (s >> 2) + (lane >> 3)) * (QD * 2) + (g * 4 + (s & 3)) * 128 + (lane & 7) * 16;
#pragma unroll
      for (int i = 0; i < 18; ++i) wqv[i] = __builtin_amdgcn_raw_buffer_load_b128(rq, o, i * 8 * QD * 2, AUX_NT);
    }
    if (lane < 16) ldu[L_FLAG + lane] = 0u;
    asm volatile("s_waitcnt lgkmcnt(0)" ::: "memory");
    __builtin_amdgcn_sched_barrier(0);
    __builtin_amdgcn_s_barrier();
    __builtin_amdgcn_sched_barrier(0);
    float mb, den;
    {
      float xv[10];
#pragma unroll
      for (int k = 0; k < 5; ++k) { xv[2 * k] = lo_f(xd[k]); xv[2 * k + 1] = hi_f(xd[k]); }
      float sm = 0.f;
#pragma unroll
      for (int k = 0; k < 10; ++k) sm += xv[k];
      const float mean = wave_sum(sm) * (1.0f / DM);
      float sq = 0.f;
#pragma unroll
      for (int k = 0; k < 8; ++k) { const float d = xv[k] - mean; sq = fmaf(d, d, sq); }
      const float d8 = lane < 32 ? xv[8] - mean : 0.f, d9 = lane < 32 ? xv[9] - mean : 0.f;
      sq = fmaf(d8, d8, sq); sq = fmaf(d9, d9, sq);
      const float var = wave_sum(sq) * (1.0f / DM);
      mb = rnd(mean);
      den = rnd(sqrtf(rnd(rnd(var) + 1e-5f)));
    }
    unsigned xn[3];
#pragma unroll
    for (int k = 0; k < 3; ++k) { const float v = rnd(lo_f(xq[k]) - mb) / den; xn[k] = pk_bf16(v, v); }
    STAMP(1);
    unsigned xr[18];
#pragma unroll
    for (int i = 0; i < 18; ++i) xr[i] = __shfl(xn[i >> 3], (8 * i + (lane >> 3)) & 63);
#pragma unroll
    for (int i = 0; i < 18; ++i) asm volatile("" : "+v"(xr[i]));
    __builtin_amdgcn_sched_barrier(0);
    f32x4 qa = {0.f, 0.f, 0.f, 0.f};
#pragma unroll
    for (int i = 0; i < 18; ++i) { qa = mfma16(and4(xr[i], gm), wqv[i], qa); __builtin_amdgcn_sched_barrier(0); }
    qa = gemv_fin(qa, lane);
    tag = __builtin_amdgcn_readfirstlane(epoch) + 1u;
    {
      const bool up2 = (lane & 8) != 0;
      const int col = (lane & 7) * 8 + 4 * ((lane >> 4) & 1) + (up2 ? 2 : 0);
      __builtin_amdgcn_raw_buffer_store_b128(v4u{__float_as_uint(up2 ? qa[2] : qa[0]), tag, __float_as_uint(up2 ? qa[3] : qa[1]), tag}, rws,
                                             lane < 32 ? WS_QG + ((g * 4 + (s >> 2)) * 256 + (s & 3) * 64 + col) * 8 : OOB, 0, AUX_SC1);
    }
    STAMP(2);
    if (lane == 0) ldu[L_FLAG + F_TAG] = tag;
    if (is1) {
#pragma unroll
      for (int k = 0; k < 5; ++k) {
        const float v0 = rnd(lo_f(xd[k]) - mb) / den, v1 = rnd(hi_f(xd[k]) - mb) / den;
        *reinterpret_cast<v2u*>(&ldu[L_XN + 2 * (lane + 64 * k)]) = v2u{pk_bf16(v0, v0), pk_bf16(v1, v1)};
      }
      set_flag(F_XRDY);
    }
    for (int i = 0; i < KQD; ++i) __builtin_amdgcn_s_sleep(8);
    float q4[4];
    for (unsigned spins = 0;; ++spins) {
      asm volatile("" ::: "memory");
      v4u a[8];
#pragma unroll
      for (int r = 0; r < 4; ++r) {
        a[2 * r] = __builtin_amdgcn_raw_buffer_load_b128(rws, WS_QG + g * 8192 + lane * 32, r * 2048, AUX_SC1);
        a[2 * r + 1] = __builtin_amdgcn_raw_buffer_load_b128(rws, WS_QG + g * 8192 + lane * 32 + 16, r * 2048, AUX_SC1);
      }
      __builtin_amdgcn_sched_barrier(0);
      set_flag(F_PISS);
      __builtin_amdgcn_sched_barrier(0);
      unsigned bad = 0;
#pragma unroll
      for (int j = 0; j < 4; ++j) q4[j] = 0.f;
#pragma unroll
      for (int r = 0; r < 4; ++r) {
        bad |= (a[2 * r].y ^ tag) | (a[2 * r].w ^ tag) | (a[2 * r + 1].y ^ tag) | (a[2 * r + 1].w ^ tag);
        q4[0] += __uint_as_float(a[2 * r].x); q4[1] += __uint_as_float(a[2 * r].z);
        q4[2] += __uint_as_float(a[2 * r + 1].x); q4[3] += __uint_as_float(a[2 * r + 1].z);
      }
      if (__all(bad == 0) || spins > SPIN_CAP) break;
      __builtin_amdgcn_s_sleep(1);
    }
    *reinterpret_cast<v2u*>(&ldu[L_Q + 2 * lane]) = v2u{pk_bf16(q4[0], q4[1]), pk_bf16(q4[2], q4[3])};
    set_flag(F_QRDY);
    STAMP(3);
    __builtin_amdgcn_s_setprio(0);
    auto pub_pf = [&]() {
      wait_flag(F_FDONE, 3);
#pragma unroll
      for (int k = 0; k < 5; ++k) {
        const int t = k * 64 + lane;
        const f32x2 yf = *reinterpret_cast<const f32x2*>(&lds[L_YF + 2 * (t < 288 ? t : 0)]);
        __builtin_amdgcn_raw_buffer_store_b128(v4u{__float_as_uint(yf.x), tag, __float_as_uint(yf.y), tag}, rws,
                                               t < 288 ? WS_PF + ((t >> 3) * NFW + f2) * 128 + (t & 7) * 16 : OOB, 0, AUX_SC1);
      }
    };
    if (KPFE && is2) pub_pf();
    wait_flag(F_P1CNT, NW);
    {
      const float mw = lds[L_MW + (lane & 15)];
      float mx = fmaxf(mw, dpp<XOR2>(mw)); mx = fmaxf(mx, dpp<ROR4>(mx)); mx = fmaxf(mx, dpp<XOR8>(mx));
      float l = 0.f;
#pragma unroll 2
      for (int i = 0; i < (SC_TILES + 7) / 8; ++i) {
        const f32x4 v = *reinterpret_cast<const f32x4*>(&lds[L_SC + i * 256 + lane * 4]);
        l += (__builtin_amdgcn_exp2f(v[0] - mx) + __builtin_amdgcn_exp2f(v[1] - mx)) + (__builtin_amdgcn_exp2f(v[2] - mx) + __builtin_amdgcn_exp2f(v[3] - mx));
      }
      l += dpp<XOR2>(l); l += dpp<ROR4>(l); l += dpp<XOR8>(l); l = sum_x32(sum_x16(l));
      __builtin_amdgcn_raw_buffer_store_b128(v4u{__float_as_uint(mx), tag, __float_as_uint(l), tag}, rws,
                                             lane < 8 ? WS_P1M + b * 128 + lane * 16 : OOB, 0, AUX_SC1);
      STAMP(4);
      wait_flag(F_PROG, 128 - KMLT);
      v4u ml;
      for (unsigned spins = 0;; ++spins) {
        asm volatile("" ::: "memory");
        ml = __builtin_amdgcn_raw_buffer_load_b128(rws, WS_P1M + (g * 16 + ((lane >> 1) & 15)) * 128 + (lane & 1) * 16, 0, AUX_SC1);
        if (__all(((ml.y ^ tag) | (ml.w ^ tag)) == 0) || spins > SPIN_CAP) break;
        __builtin_amdgcn_s_sleep(32);
      }
      const float mp = __uint_as_float(ml.x);
      float M = fmaxf(mp, dpp<XOR2>(mp)); M = fmaxf(M, dpp<ROR4>(M)); M = fmaxf(M, dpp<XOR8>(M)); M = max_x16(M);
      float L = __builtin_amdgcn_exp2f(mp - M) * __uint_as_float(ml.z);
      asm volatile("" : "+v"(L));
      L += dpp<XOR2>(L); L += dpp<ROR4>(L); L += dpp<XOR8>(L); L = sum_x16(L);
      const float ew = __builtin_amdgcn_exp2f(mw - M) / L;
      f32x4 hs = {0.f, 0.f, 0.f, 0.f};
#pragma unroll
      for (int ww = NSW; ww < NW; ++ww) hs += __shfl(ew, 2 * ww + (lane >> 5)) * *reinterpret_cast<const f32x4*>(&lds[L_ACC + ww * 256 + lane * 4]);
      *reinterpret_cast<f32x4*>(&lds[L_ACC + NW * 256 + lane * 4]) = hs;
      if (lane < 16) lds[L_MW + lane] = ew;
      set_flag(F_MRDY);
    }
    if (KPFE && b < NRED) gate_vals();
    STAMP(5);
    if (EXP_NOTAIL) { if (b == 0 && lane == 0) { wait_flag(F_ENDCNT, NSW); __builtin_amdgcn_raw_buffer_store_b32(tag, rws, WS_EPOCH, 0, AUX_SC1); } return; }
    if (!KPFE && is2) { wait_flag(F_PROG, 128 - NEART); pub_pf(); }
    wait_flag(F_ENDCNT, NSW);
    for (int i = 0; i < KP1D; ++i) __builtin_amdgcn_s_sleep(4);
    {
      int o2[2] = {WS_P1O + (g * 16 + s) * 2048 + lane * 16, WS_P1O + (g * 16 + s) * 2048 + 1024 + lane * 16};
      float pv4[4];
      gather<2>(rws, o2, tag, pv4);
      STAMP(6);
      auto h0 = __builtin_amdgcn_permlane32_swap(__float_as_uint(pv4[0] + pv4[2]), __float_as_uint(pv4[1] + pv4[3]), false, false);
      float v = sum_x16(__uint_as_float(h0[0]) + __uint_as_float(h0[1]));
      v += dpp<XOR8>(v);
      if ((lane & 24) == 0) lds[L_WT + 2 * (lane & 7) + (lane >> 5)] = rnd(v);
      set_flag(F_WRDY);
      STAMP(7);
    }
    STAMP_FLUSH();
    return;
  }

  static_assert(2048 - 16 * NSW * K_O == 32, "unit split");
  const int nrole = is1 ? 4 : is2 ? 3 : 0;
  int ufirst, nt;
  {
    const int fr = sr - 9 - ns2;
    const int hu = is1 ? 1 : is2 ? 2 : (g & 1) ? 5 : 7 - fr;
    const int h0 = is1 ? sr : is2 ? 9 + 2 * (sr - 9) : 9 + 2 * ns2 + ((g & 1) ? 5 : 7) * fr;
    const int ubase = NSW * K_O * sr + h0;
    const int j = w - NSW - nrole, nh = NHELP - nrole;
    if (w < NSW) {
      ufirst = ubase + hu + w; nt = 2 * K_O;
    } else {
      const int n1 = hu - nh;
      ufirst = ubase + (j < n1 ? 2 * j : n1 + j); nt = j < n1 ? 4 : 2;
    }
  }
  const int ustep = w < NSW ? NSW : 1;
  const int sbase = w < NSW ? 4 * NHELP + w * 2 * K_O : (w - NSW) * 4;
  const int voff = lane * 16;
  auto no_keys = [&]() {
    *reinterpret_cast<f32x2*>(&lds[L_SC + sbase * 32 + lane * 2]) = f32x2{-INFINITY, -INFINITY};
    *reinterpret_cast<f32x4*>(&lds[L_ACC + w * 256 + lane * 4]) = f32x4{0.f, 0.f, 0.f, 0.f};
    if (lane < 2) lds[L_MW + 2 * w + lane] = -INFINITY;
    count_up(F_P1CNT);
  };
  __builtin_amdgcn_sched_barrier(0);
  __builtin_amdgcn_s_barrier();
  __builtin_amdgcn_sched_barrier(0);

  if (w >= NSW && w < NSW + nrole) {
    const int fw = w - NSW;
    v4u fr[24];
    if (is1) {
      const __amdgpu_buffer_rsrc_t rfg = mk_rsrc(wgate, EXP_NOFFN ? 0 : DM * FF * 2);
      const __amdgpu_buffer_rsrc_t rfu = mk_rsrc(wup, EXP_NOFFN ? 0 : DM * FF * 2);
      const int row0 = 288 * (f1 & 1) + 72 * fw + (lane >> 3);
      const int foff = row0 * (FF * 2) + (f1 >> 1) * 128 + (lane & 7) * 16;
#pragma unroll
      for (int i = 0; i < 9; ++i) {
        fr[2 * i] = __builtin_amdgcn_raw_buffer_load_b128(rfg, foff, i * 8 * FF * 2, AUX_NT);
        fr[2 * i + 1] = __builtin_amdgcn_raw_buffer_load_b128(rfu, foff, i * 8 * FF * 2, AUX_NT);
      }
      __builtin_amdgcn_sched_barrier(0);
      no_keys();
      wait_flag(F_XRDY, 1);
      tag = ldu[L_FLAG + F_TAG];
      f32x4 ag = {0.f, 0.f, 0.f, 0.f}, au = {0.f, 0.f, 0.f, 0.f};
#pragma unroll
      for (int i = 0; i < 9; ++i) {
        const v4u av = and4(ldu[L_XN + row0 + 8 * i], gm);
        ag = mfma16(av, fr[2 * i], ag); au = mfma16(av, fr[2 * i + 1], au);
      }
      ag = gemv_fin(ag, lane); au = gemv_fin(au, lane);
      if ((lane & 8) == 0) *reinterpret_cast<f32x4*>(&lds[L_FR + fw * 128 + (lane >> 5) * 64 + (lane & 7) * 8 + 4 * ((lane >> 4) & 1)]) = lane < 32 ? ag : au;
      int old = lds_inc(lds0 + (L_FLAG + F_S1CNT) * 4);
      float gs = 0.f, us = 0.f;
#pragma unroll
      for (int ww = 0; ww < 4; ++ww) { gs += lds[L_FR + ww * 128 + lane]; us += lds[L_FR + ww * 128 + 64 + lane]; }
      asm volatile("s_waitcnt lgkmcnt(0)" : "+v"(old), "+v"(gs), "+v"(us));
      if (__builtin_amdgcn_readfirstlane(old) == 3)
        __builtin_amdgcn_raw_buffer_store_b128(v4u{__float_as_uint(gs), tag, __float_as_uint(us), tag}, rws, WS_GU + (f1 * 64 + lane) * 16, 0, AUX_SC1);
    } else {
      const __amdgpu_buffer_rsrc_t rwd = mk_rsrc(wdown + (size_t)f2 * 64 * DM, EXP_NOFFN ? 0 : 64 * DM * 2);
      const int doff = (lane >> 3) * (DM * 2) + fw * 384 + (lane & 7) * 16;
#pragma unroll
      for (int rg = 0; rg < 8; ++rg)
#pragma unroll
        for (int k = 0; k < 3; ++k) fr[rg * 3 + k] = __builtin_amdgcn_raw_buffer_load_b128(rwd, doff + k * 128, rg * 8 * DM * 2, AUX_NT);
      __builtin_amdgcn_sched_barrier(0);
      no_keys();
      wait_flag(F_QRDY, 1);
      tag = ldu[L_FLAG + F_TAG];
      for (int i = 0; i < KHD; ++i) __builtin_amdgcn_s_sleep(8);
      v4u p0, p1;
      for (unsigned spins = 0;; ++spins) {
        asm volatile("" ::: "memory");
        p0 = __builtin_amdgcn_raw_buffer_load_b128(rws, WS_GU + (f2 * 128 + lane) * 16, 0, AUX_SC1);
        p1 = __builtin_amdgcn_raw_buffer_load_b128(rws, WS_GU + (f2 * 128 + lane) * 16, 1024, AUX_SC1);
        if (__all(((p0.y ^ tag) | (p0.w ^ tag) | (p1.y ^ tag) | (p1.w ^ tag)) == 0) || spins > SPIN_CAP) break;
        __builtin_amdgcn_s_sleep(16);
      }
      const float gt = rnd(__uint_as_float(p0.x) + __uint_as_float(p1.x)), up = rnd(__uint_as_float(p0.z) + __uint_as_float(p1.z));
      const float inner = 0.7978845608028654f * (gt + 0.044715f * gt * gt * gt);
      const float hv = rnd(0.5f * gt * (1.0f + tanhf(inner))) * up;
      const unsigned hp = pk_bf16(hv, hv);
      f32x4 ya[3];
#pragma unroll
      for (int k = 0; k < 3; ++k) ya[k] = f32x4{0.f, 0.f, 0.f, 0.f};
#pragma unroll
      for (int rg = 0; rg < 8; ++rg) {
        const v4u av = and4(__shfl(hp, rg * 8 + (lane >> 3)), gm);
#pragma unroll
        for (int k = 0; k < 3; ++k) ya[k] = mfma16(av, fr[rg * 3 + k], ya[k]);
      }
#pragma unroll
      for (int k = 0; k < 3; ++k) {
        ya[k] = gemv_fin(ya[k], lane);
        if (lane < 32 && (lane & 8) == 0) *reinterpret_cast<f32x4*>(&lds[L_YF + fw * 192 + k * 64 + (lane & 7) * 8 + 4 * (lane >> 4)]) = ya[k];
      }
      count_up(F_FDONE);
    }
    STAMP(4);
  } else {
    const __amdgpu_buffer_rsrc_t rk = mk_rsrc(kg + (size_t)g * SK * 128, SK * 256);
    const __amdgpu_buffer_rsrc_t rv = mk_rsrc(vg + (size_t)g * SK * 128, SK * 256);
#define TOFF(t) ((ufirst + ((t) >> 1) * ustep) << 13)
#define tb(n, i) buf[(n) * TL + (i)]
    v4u buf[(D2 > 4 ? D2 : 4) * TL];
    if (!KKEARLY && nrole) wait_flag(F_PISS, 1);
    {
      const int t0 = nt > 0 ? TOFF(0) : OOB;
#pragma unroll
      for (int i = 0; i < 2 * TL; ++i) buf[i] = __builtin_amdgcn_raw_buffer_load_b128(rk, voff + i * 1024, t0, AUX_NT);
    }
    __builtin_amdgcn_sched_barrier(0);
    {
      const int cap = w < NSW ? 2 * K_O + (w == NSW - 1 ? 8 : 0) : 4;
#pragma unroll 1
      for (int t = nt; t < cap; t += 2) lds[L_SC + (sbase + t) * 32 + lane] = -INFINITY;
    }
    STAMP(1);
    const int hf = (c >> 2) & 1;
    v4u qf, qs;
    auto get_q = [&]() {
      wait_flag(F_QRDY, 1);
      STAMP(3);
      qf = *reinterpret_cast<const v4u*>(&ldu[L_Q + hf * 64 + 4 * c]);
      qs = *reinterpret_cast<const v4u*>(&ldu[L_Q + (hf ^ 1) * 64 + 4 * c]);
    };
    constexpr float LOG2E = 1.4426950408889634f, SCALE = 0.08838834764831845f;
    const bool b0 = (c & 1) != 0, b1 = (c & 2) != 0;
    float* const scw = &lds[L_SC + sbase * 32 + (lane >> 4) * 8 + (lane & 7)];
    float m = -INFINITY;
    float y[4];
    auto qk1 = [&](const v4u* kt) {
#pragma unroll
      for (int i = 0; i < TL; ++i) {
        float xf = 0.f, xs = 0.f;
#pragma unroll
        for (int j = 0; j < 4; ++j) { xf = dot2(kt[i][j], qf[j], xf); xs = dot2(kt[i][j], qs[j], xs); }
        y[i] = xf + dpp<XOR7>(xs);
      }
      asm volatile("" : "+v"(y[0]), "+v"(y[1]), "+v"(y[2]), "+v"(y[3]));
      __builtin_amdgcn_sched_barrier(0);
    };
    auto qk2 = [&](int t) {
      const float u0 = (b0 ? y[1] : y[0]) + dpp<XOR1>(b0 ? y[0] : y[1]);
      const float u1 = (b0 ? y[3] : y[2]) + dpp<XOR1>(b0 ? y[2] : y[3]);
      float sc = (b1 ? u1 : u0) + dpp<XOR2>(b1 ? u0 : u1);
      sc += dpp<XOR8>(sc);
      const float tl2 = rnd(rnd(sc) * SCALE) * LOG2E;
      m = fmaxf(m, tl2);
      scw[t * 32] = tl2;
    };
    int tk = 0;
    if (KOPEN && w < NSW) {
      wait_flag(F_PISS, 1);
      const int t2 = TOFF(2), t4 = TOFF(4);
#pragma unroll
      for (int i = 0; i < 2 * TL; ++i) buf[2 * TL + i] = __builtin_amdgcn_raw_buffer_load_b128(rk, voff + i * 1024, t2, AUX_NT);
      __builtin_amdgcn_sched_barrier(0);
      get_q();
#pragma unroll
      for (int n = 0; n < 2; ++n) {
        qk1(&tb(n, 0));
#pragma unroll
        for (int i = 0; i < TL; ++i) tb(n, i) = __builtin_amdgcn_raw_buffer_load_b128(rk, voff + i * 1024, t4 + n * 4096, AUX_NT);
        __builtin_amdgcn_sched_barrier(0);
        qk2(n);
      }
      TRACE(0);
      qk1(&tb(2, 0)); qk2(2);
      qk1(&tb(3, 0)); qk2(3);
      TRACE(1);
      tk = 4;
    } else get_q();
    unsigned next = (unsigned)__builtin_amdgcn_s_memrealtime() << 4, tau = KTAU;
    auto aim = [&](int left) {
      const int v = __builtin_amdgcn_readfirstlane((int)((float)(int)(tend - next) * __builtin_amdgcn_rcpf((float)left)));
      tau = (unsigned)min(KTAUHI, max(KTAULO, v));
    };
#pragma unroll 1
    for (int t = tk; t < nt; t += 2) {
      const bool lastk = t + 2 >= nt;
      const __amdgpu_buffer_rsrc_t rn = lastk ? rv : rk;
      const int nxt = TOFF(lastk ? 0 : t + 2);
      if (t == KTCAL) tend = tend0;
      if (t >= KTCAL && ((t - KTCAL) & KCALM) == 0) aim(4 * K_O - t);
#pragma unroll
      for (int n = 0; n < 2; ++n) {
        qk1(&tb(n, 0));
        pace(next, tau, tend);
#pragma unroll
        for (int i = 0; i < TL; ++i) tb(n, i) = __builtin_amdgcn_raw_buffer_load_b128(rn, voff + i * 1024, nxt + n * 4096, AUX_NT);
        __builtin_amdgcn_sched_barrier(0);
        qk2(t + n);
      }
      TRACE(t >> 1);
    }
    m = fmaxf(m, dpp<XOR1>(m)); m = fmaxf(m, dpp<XOR2>(m));
    m = max_x32(max_x16(m));
    if ((lane & 59) == 0) lds[L_MW + 2 * w + (lane >> 2)] = m;
    if (w < NSW) count_up(F_P1CNT);
    f32x4 acc = {0.f, 0.f, 0.f, 0.f};
    unsigned amask[4];
    {
      const int jp = (c & 3) + 4 * (c >> 3);
#pragma unroll
      for (int r = 0; r < 4; ++r) amask[r] = (jp >> 1) == r ? ((jp & 1) ? 0xffff0000u : 0x0000ffffu) : 0u;
    }
    auto pv = [&](float sv, const v4u* vt) {
      const float p = __builtin_amdgcn_exp2f(sv - m);
      const unsigned pp = pk_bf16(p, p);
#pragma unroll
      for (int i = 0; i < TL; ++i) {
        const unsigned pb = i == 0 ? dppu<0x00>(pp) : i == 1 ? dppu<0x55>(pp) : i == 2 ? dppu<0xAA>(pp) : dppu<0xFF>(pp);
        acc = mfma16(and4(pb, amask), vt[i], acc);
      }
      asm volatile("" : "+v"(acc));
      __builtin_amdgcn_sched_barrier(0);
    };
#define VOFF(t) ((t) < nt ? TOFF(t) + ((t) & 1) * 4096 : OOB)
    int t = 0;
    auto vrun = [&](int lim) {
#pragma unroll 1
      for (; t < lim; t += 2) {
        if (((t + 2 * K_O - KTCAL) & KCALM) == 0) aim(2 * K_O - t);
        const int nxt = TOFF(t + 2);
        const float s0 = scw[t * 32], s1 = scw[t * 32 + 32];
#pragma unroll
        for (int n = 0; n < 2; ++n) {
          pv(n ? s1 : s0, &tb(n, 0));
          pace(next, tau, tend);
#pragma unroll
          for (int i = 0; i < TL; ++i) tb(n, i) = __builtin_amdgcn_raw_buffer_load_b128(rv, voff + i * 1024, nxt + n * 4096, AUX_NT);
          __builtin_amdgcn_sched_barrier(0);
        }
        TRACE((nt + t) >> 1);
        if (w == 0 && lane == 0) *(volatile LAS int*)&ldu[L_FLAG + F_PROG] = t + 2 + 128 - nt;
      }
    };
    vrun(nt - KXMAX);
    {
      const int late = (int)(next + KXMAX * KTAUN - tend);
      vrun(nt - min(KXMAX, XMID + 2 * max(0, (late * KXC) >> 15)));
    }
#pragma unroll
    for (int k = 2; k < D2; ++k) {
      const int o = VOFF(t + k);
#pragma unroll
      for (int i = 0; i < TL; ++i) tb(k, i) = __builtin_amdgcn_raw_buffer_load_b128(rv, voff + i * 1024, o, AUX_NT);
    }
    __builtin_amdgcn_sched_barrier(0);
    float ed = 1.f;
    if (w < NSW) {
      wait_flag(F_MRDY, 1);
      const int hh = (lane >> 4) & 1;
      ed = lds[L_MW + 2 * w + hh];
    }
    tag = ldu[L_FLAG + F_TAG];
#pragma unroll 1
    for (; t < nt; t += D2) {
      if (w == 0 && lane == 0) *(volatile LAS int*)&ldu[L_FLAG + F_PROG] = t + D2 - 2 + 128 - nt;
#pragma unroll
      for (int k = 0; k < D2; ++k) {
        const float sv = t + k < nt ? scw[(t + k) * 32] : -INFINITY;
        const int o = VOFF(t + k + D2);
        pv(sv, &tb(k, 0));
#pragma unroll
        for (int i = 0; i < TL; ++i) tb(k, i) = __builtin_amdgcn_raw_buffer_load_b128(rv, voff + i * 1024, o, AUX_NT);
        __builtin_amdgcn_sched_barrier(0);
      }
    }
    STAMP(4);
    const int fo = ((lane >> 4) & 1) * 128 + 8 * c + 4 * (lane >> 5);
    *reinterpret_cast<f32x4*>(&lds[L_ACC + w * 256 + fo]) = acc * ed;
    if (w >= NSW) count_up(F_P1CNT);
    else {
      int old = lds_inc(lds0 + (L_FLAG + F_ENDCNT) * 4);
      f32x2 o0 = *reinterpret_cast<const f32x2*>(&lds[L_ACC + NW * 256 + 2 * lane]), o1 = *reinterpret_cast<const f32x2*>(&lds[L_ACC + NW * 256 + 128 + 2 * lane]);
#pragma unroll
      for (int ww = 0; ww < NSW; ++ww) { o0 += *reinterpret_cast<const f32x2*>(&lds[L_ACC + ww * 256 + 2 * lane]); o1 += *reinterpret_cast<const f32x2*>(&lds[L_ACC + ww * 256 + 128 + 2 * lane]); }
      asm volatile("s_waitcnt lgkmcnt(0)" : "+v"(old), "+v"(o0), "+v"(o1));
      if (EXP_NOTAIL) { if (o0[0] == 1.2345f) out[1] = 2; return; }
      if (__builtin_amdgcn_readfirstlane(old) == NSW - 1) {
        const int po = WS_P1O + ((g * 16 + (lane >> 3)) * 16 + s) * 128 + (lane & 7) * 16;
        __builtin_amdgcn_raw_buffer_store_b128(v4u{__float_as_uint(o0[0]), tag, __float_as_uint(o0[1]), tag}, rws, po, 0, AUX_SC1);
        __builtin_amdgcn_raw_buffer_store_b128(v4u{__float_as_uint(o1[0]), tag, __float_as_uint(o1[1]), tag}, rws, po, 8 * 2048, AUX_SC1);
      }
      STAMP(7);
    }
  }

  if (w >= NSW) {
    const int t = (w - NSW) * 64 + lane;
    float wf[32];
    {
      const __amdgpu_buffer_rsrc_t rwo = mk_rsrc(wo + (size_t)b * 16 * DM, 16 * DM * 2);
      unsigned wod[16];
#pragma unroll
      for (int j = 0; j < 16; ++j) wod[j] = __builtin_amdgcn_raw_buffer_load_b32(rwo, t < 288 ? t * 4 : OOB, j * DM * 2, AUX_NT);
#pragma unroll
      for (int j = 0; j < 16; ++j) { wf[2 * j] = lo_f(wod[j]); wf[2 * j + 1] = hi_f(wod[j]); }
#pragma unroll
      for (int j = 0; j < 32; ++j) asm volatile("" : "+v"(wf[j]));
    }
    if (EXP_NOTAIL) { if (wf[0] + wf[31] == 1.2345f) out[2] = 2; return; }
#pragma unroll 1
    for (unsigned spins = 0; spins < (SPIN_CAP << 4); ++spins) {
      if (__builtin_amdgcn_readfirstlane(*(volatile LAS unsigned*)&ldu[L_FLAG + F_PROG]) >= 128u - NEART) break;
      __builtin_amdgcn_s_sleep(8);
    }
    tag = ldu[L_FLAG + F_TAG];
    const int po = t < 288 && (!EXP_P2 || (b & 3) == 0) ? WS_P2 + ((t >> 3) * NWG + b) * 128 + (t & 7) * 16 : OOB;
    f32x4 o4[4];
#pragma unroll 1
    for (unsigned spins = 0; spins < (SPIN_CAP << 8); ++spins) {
      unsigned f = *(volatile LAS unsigned*)&ldu[L_FLAG + F_WRDY];
      asm volatile("" ::: "memory");
#pragma unroll
      for (int k = 0; k < 4; ++k) o4[k] = *reinterpret_cast<const f32x4*>(&lds[L_WT + 4 * k]);
      asm volatile("" : "+v"(o4[0]), "+v"(o4[1]), "+v"(o4[2]), "+v"(o4[3]) :: "memory");
      if (__builtin_amdgcn_readfirstlane(f) != 0u) break;
    }
    STAMP(9);
    f32x2 ya = {0.f, 0.f}, yb = {0.f, 0.f};
#pragma unroll
    for (int j = 0; j < 16; j += 2) {
      ya += o4[j >> 2][j & 3] * f32x2{wf[2 * j], wf[2 * j + 1]};
      yb += o4[j >> 2][(j & 3) + 1] * f32x2{wf[2 * j + 2], wf[2 * j + 3]};
    }
    const float y0 = ya.x + yb.x, y1 = ya.y + yb.y;
    __builtin_amdgcn_raw_buffer_store_b128(v4u{__float_as_uint(y0), tag, __float_as_uint(y1), tag}, rws, po, 0, AUX_SC1);
    STAMP(10);
  }

  if (b < NRED) {
    if (!KPFE && w == 0) gate_vals();
    if (w < NSW) wait_flag(F_WRDY, 1);
    for (int i = 0; i < KP2D; ++i) __builtin_amdgcn_s_sleep(4);
    int o4[4];
    float sv[8];
#pragma unroll
    for (int k = 0; k < 4; ++k) o4[k] = EXP_P2 && ((lane >> 3) & 3) ? OOB : WS_P2 + b * NWG * 128 + (w * 64 + lane) * 16 + k * 8192;
    gather<4>(rws, o4, tag, sv);
    STAMP(2);
    float s0 = (sv[0] + sv[2]) + (sv[4] + sv[6]), s1 = (sv[1] + sv[3]) + (sv[5] + sv[7]);
    s0 += dpp<XOR8>(s0); s0 = sum_x32(sum_x16(s0));
    s1 += dpp<XOR8>(s1); s1 = sum_x32(sum_x16(s1));
    if (lane < 8) *reinterpret_cast<f32x2*>(&lds[L_FIN + w * 16 + lane * 2]) = f32x2{s0, s1};
    int old = lds_inc(lds0 + (L_FLAG + F_FINCNT) * 4);
    f32x2 t2 = *reinterpret_cast<const f32x2*>(&lds[L_FIN + lane * 2]);
    f32x2 gv = *reinterpret_cast<const f32x2*>(&lds[L_G + (lane & 7) * 2]);
    unsigned xp = ldu[L_G + 16 + (lane & 7)];
    asm volatile("s_waitcnt lgkmcnt(0)" : "+v"(old), "+v"(t2), "+v"(gv), "+v"(xp));
    if (__builtin_amdgcn_readfirstlane(old) == NW - 1) {
      t2.x += dpp<XOR8>(t2.x); t2.x = sum_x32(sum_x16(t2.x));
      t2.y += dpp<XOR8>(t2.y); t2.y = sum_x32(sum_x16(t2.y));
      const unsigned r = pk_bf16(lo_f(xp) + rnd(rnd(t2.x) * gv.x), hi_f(xp) + rnd(rnd(t2.y) * gv.y));
      __builtin_amdgcn_raw_buffer_store_b32(r, mk_rsrc(out, DM * 2), lane < 8 ? b * 32 + lane * 4 : OOB, 0, KOUTSC);
      if (b == 0 && lane == 0) __builtin_amdgcn_raw_buffer_store_b32(tag, rws, WS_EPOCH, 0, AUX_SC1);
      STAMP(11);
    }
  }
  STAMP_FLUSH();
}

}

extern "C" void kernel_launch(void* const* d_in, const int* in_sizes, int n_in, void* d_out, int out_size, void* d_ws, size_t ws_size,
                              hipStream_t stream) {
  (void)in_sizes; (void)n_in; (void)out_size; (void)ws_size;
  fused_decode<<<NWG, NTHR, 0, stream>>>((const unsigned short*)d_in[0], (const unsigned short*)d_in[1], (const unsigned short*)d_in[2],
                                         (const unsigned short*)d_in[3], (const unsigned short*)d_in[4], (const unsigned short*)d_in[6],
                                         (const unsigned short*)d_in[7], (const unsigned short*)d_in[8], (unsigned short*)d_out,
                                         (unsigned char*)d_ws);
}
```

```cpp
#include <hip/hip_runtime.h>
#include <hip/hip_bf16.h>
#include <stdint.h>

namespace {

typedef unsigned v4u __attribute__((ext_vector_type(4)));
typedef unsigned v2u __attribute__((ext_vector_type(2)));
typedef float f32x4 __attribute__((ext_vector_type(4)));
typedef float f32x2 __attribute__((ext_vector_type(2)));
typedef __bf16 bf16x8 __attribute__((ext_vector_type(8)));
typedef __bf16 bf16x2 __attribute__((ext_vector_type(2)));
#define GAS __attribute__((address_space(1)))
#define LAS __attribute__((address_space(3)))

#define KHD 8
#define KKEARLY 1
#define KKO 42
#define KQD 4
#define KXMID 4
#define KOUTSC 0
#define KTAU 656
#define KTAULO 648
#define KTAUHI 680
#define KTEND 7500
#define KCALM 7
#define KSTALL 100
#define KXMAX 28
#define KXC 90
#define KTAUN 652
#define KVDUR 3440
#define KEB 0
#define KFT 8
#define KST 16
#define KLD 16
#define KKDUR 6230
#define KCPD 8
#define EXP_NOGU 0
#define EXP_NOWO 0
#define KWOL 2
#define KWOT 1
#define KPRIO 1
#define KTCAL 16
#define KBURST 20
#define EXP_NOTAIL 0
#define EXP_NOFFN 0
#define EXP_P2 0
#define KPGAP 5
#define KNEAR 4
#define KPFE 0
#define KMLT 72
#define KP1D 3
#define KP2D 4
#define KD2 4
#define KOPEN 1

constexpr int DM = 576, SK = 65536, FF = 4608, QD = 4096;
constexpr int NWG = 256, NW = 8, NTHR = (NW + 1) * 64;
constexpr int NSW = 3;
constexpr int NHELP = NW - NSW;
constexpr int NRED = 36;
constexpr int NFW = 72;
constexpr int TL = 4;
constexpr int D2 = KD2;
constexpr int XMID = KXMID;
constexpr int K_O = KKO;
constexpr int NEART = KNEAR;
constexpr int OOB = 0x70000000;

constexpr int WS_EPOCH = 0;
constexpr int WS_QG = 4096;
constexpr int WS_P1M = WS_QG + 16 * 4 * 256 * 8;
constexpr int WS_CP = WS_P1M + NWG * 128;
constexpr int WS_P1O = WS_CP + NWG * 128;
constexpr int WS_P2 = WS_P1O + 16 * 16 * 16 * 128;
constexpr int WS_PF = WS_P2 + NRED * NWG * 128;
constexpr int WS_GU = WS_PF + NRED * NFW * 128;
constexpr int WS_END = WS_GU + NFW * 2 * 64 * 16;

#define STAMP_DECL do {} while (0)
#define STAMP(i) do {} while (0)
#define STAMP_FLUSH() do {} while (0)
#define TRACE(i) do {} while (0)
constexpr int AUX_NT = 2, AUX_SC1 = KST, AUX_LD = KLD;
constexpr int RSRC_W3 = 0x00020000;
constexpr unsigned SPIN_CAP = 1u << 14;

constexpr int L_FLAG = 0;
enum { F_TAG, F_QRDY, F_XRDY, F_FDONE, F_PISS, F_P1CNT, F_MRDY, F_NEAR, F_WRDY, F_ENDCNT, F_FINCNT, F_GRDY, F_PROG, F_S1CNT, F_TEND, F_KPROG = F_NEAR };
constexpr int L_MW = 16;
constexpr int L_M = L_MW + 16;
constexpr int L_WT = L_M + 4;
constexpr int L_G = L_WT + 16;
constexpr int L_Q = L_G + 28;
constexpr int L_XN = L_Q + 128;
constexpr int L_YF = L_XN + 640;
constexpr int L_FR = L_YF;
constexpr int L_FIN = L_YF + 576;
constexpr int L_ACC = L_FIN + 128;
constexpr int L_SC = L_ACC + 2304;
constexpr int SC_TILES = 4 * NHELP + NSW * 2 * K_O;
constexpr int L_FLOATS = 24 * 1024;
static_assert(L_SC % 4 == 0 && L_ACC % 4 == 0 && L_Q % 4 == 0 && L_SC + SC_TILES * 32 + 256 <= 22 * 1024, "LDS map");

template <int CTRL> __device__ __forceinline__ float dpp(float x) {
  return __builtin_bit_cast(float, __builtin_amdgcn_mov_dpp(__builtin_bit_cast(int, x), CTRL, 0xf, 0xf, true));
}
template <int CTRL> __device__ __forceinline__ unsigned dppu(unsigned x) {
  return (unsigned)__builtin_amdgcn_mov_dpp((int)x, CTRL, 0xf, 0xf, true);
}
constexpr int XOR1 = 0xB1, XOR2 = 0x4E, XOR7 = 0x141, XOR8 = 0x128, ROR4 = 0x124;
__device__ __forceinline__ float sum_x16(float x) {
  auto s = __builtin_amdgcn_permlane16_swap(__float_as_uint(x), __float_as_uint(x), false, false);
  return __uint_as_float(s[0]) + __uint_as_float(s[1]);
}
__device__ __forceinline__ float sum_x32(float x) {
  auto s = __builtin_amdgcn_permlane32_swap(__float_as_uint(x), __float_as_uint(x), false, false);
  return __uint_as_float(s[0]) + __uint_as_float(s[1]);
}
__device__ __forceinline__ float max_x16(float x) {
  auto s = __builtin_amdgcn_permlane16_swap(__float_as_uint(x), __float_as_uint(x), false, false);
  return fmaxf(__uint_as_float(s[0]), __uint_as_float(s[1]));
}
__device__ __forceinline__ float max_x32(float x) {
  auto s = __builtin_amdgcn_permlane32_swap(__float_as_uint(x), __float_as_uint(x), false, false);
  return fmaxf(__uint_as_float(s[0]), __uint_as_float(s[1]));
}
__device__ __forceinline__ float wave_sum(float x) {
  x += dpp<XOR1>(x); x += dpp<XOR2>(x); x += dpp<ROR4>(x); x += dpp<XOR8>(x);
  return sum_x32(sum_x16(x));
}
__device__ __forceinline__ unsigned pk_bf16(float lo, float hi) {
  bf16x2 v = {(__bf16)lo, (__bf16)hi};
  return __builtin_bit_cast(unsigned, v);
}
__device__ __forceinline__ float rnd(float x) { return (float)(__bf16)x; }
__device__ __forceinline__ float lo_f(unsigned u) { return __uint_as_float(u << 16); }
__device__ __forceinline__ float hi_f(unsigned u) { return __uint_as_float(u & 0xffff0000u); }
__device__ __forceinline__ float dot2(unsigned a, unsigned b, float c) {
  return __builtin_amdgcn_fdot2_f32_bf16(__builtin_bit_cast(bf16x2, a), __builtin_bit_cast(bf16x2, b), c, false);
}
__device__ __forceinline__ __amdgpu_buffer_rsrc_t mk_rsrc(const void* p, int bytes) {
  return __builtin_amdgcn_make_buffer_rsrc((void*)p, 0, bytes, RSRC_W3);
}
__device__ __forceinline__ v4u and4(unsigned x, const unsigned (&m)[4]) { return v4u{x & m[0], x & m[1], x & m[2], x & m[3]}; }
__device__ __forceinline__ f32x4 mfma16(v4u a, v4u bm, f32x4 cacc) {
  return __builtin_amdgcn_mfma_f32_16x16x32_bf16(__builtin_bit_cast(bf16x8, a), __builtin_bit_cast(bf16x8, bm), cacc, 0, 0, 0);
}
__device__ __forceinline__ f32x4 gemv_fin(f32x4 d, int lane) {
  const bool valid = (lane >> 5) == ((lane >> 3) & 1);
#pragma unroll
  for (int r = 0; r < 4; ++r) { float t = valid ? d[r] : 0.f; t += dpp<XOR8>(t); d[r] = sum_x32(t); }
  return d;
}
__device__ __forceinline__ int lds_inc(unsigned addr) {
  int r; unsigned long long save;
  asm volatile("s_mov_b64 %1, exec\n\ts_mov_b64 exec, 1\n\tds_add_rtn_u32 %0, %2, %3\n\ts_mov_b64 exec, %1"
               : "=&v"(r), "=&s"(save) : "v"(addr), "v"(1) : "memory");
  return r;
}
template <int N>
__device__ __forceinline__ void gather_end(v4u (&p)[3][N]) {
#pragma unroll
  for (int j = 0; j < 3; ++j)
#pragma unroll
    for (int k = 0; k < N; ++k) asm volatile("" :: "v"(p[j][k]));
}
template <int N>
__device__ __forceinline__ void gather(__amdgpu_buffer_rsrc_t rs, int (&off)[N], unsigned tag, float (&val)[2 * N], v4u (&p)[3][N]) {
  auto issue = [&](int j) {
    asm volatile("" ::: "memory");
#pragma unroll
    for (int k = 0; k < N; ++k) p[j][k] = __builtin_amdgcn_raw_buffer_load_b128(rs, off[k], 0, AUX_LD);
    __builtin_amdgcn_sched_barrier(0);
  };
  auto check = [&](int j) {
    bool done = true;
#pragma unroll
    for (int k = 0; k < N; ++k) {
      const bool ok = off[k] != OOB && ((p[j][k].y ^ tag) | (p[j][k].w ^ tag)) == 0;
      val[2 * k] = ok ? __uint_as_float(p[j][k].x) : val[2 * k]; val[2 * k + 1] = ok ? __uint_as_float(p[j][k].z) : val[2 * k + 1];
      off[k] = ok ? OOB : off[k];
      done = done && off[k] == OOB;
    }
    return __all(done) != 0;
  };
#pragma unroll
  for (int k = 0; k < 2 * N; ++k) val[k] = 0.f;
  issue(0); __builtin_amdgcn_s_sleep(KPGAP); issue(1); __builtin_amdgcn_s_sleep(KPGAP);
#pragma unroll 1
  for (unsigned spins = 0; spins < SPIN_CAP; ++spins) {
    issue(2); if (check(0)) break;
    issue(0); if (check(1)) break;
    issue(1); if (check(2)) break;
  }
}
__device__ __forceinline__ void pace(unsigned& next, unsigned tau, unsigned& tend) {
  int d;
  asm volatile("1:\n\ts_memrealtime vcc\n\ts_waitcnt lgkmcnt(0)\n\ts_lshl_b32 %0, vcc_lo, 4\n\ts_sub_i32 %0, %0, %1\n\ts_cmp_ge_i32 %0, 0\n\ts_cbranch_scc1 2f\n\ts_sleep 1\n\ts_branch 1b\n2:"
               : "=&s"(d) : "s"(next) : "scc", "vcc");
  next += tau + (unsigned)max(0, d - KBURST * 16);
  tend += d > KSTALL * 16 ? d : 0;
}

__global__ void __launch_bounds__(NTHR, 3)
fused_decode(const unsigned short* __restrict__ xg, const unsigned short* __restrict__ kg, const unsigned short* __restrict__ vg,
             const unsigned short* __restrict__ wq, const unsigned short* __restrict__ wo, const unsigned short* __restrict__ wgate,
             const unsigned short* __restrict__ wup, const unsigned short* __restrict__ wdown, unsigned short* __restrict__ out,
             unsigned char* __restrict__ ws) {
  __shared__ __attribute__((aligned(16))) float lds[L_FLOATS];
  asm volatile("" :: "s"(xg), "s"(kg), "s"(vg), "s"(wq), "s"(wo), "s"(wgate), "s"(wup), "s"(wdown), "s"(ws));
  const int lane = threadIdx.x & 63, pw = __builtin_amdgcn_readfirstlane(threadIdx.x >> 6);
  const int w = pw == 0 ? NW : pw - 1;
  const int b = blockIdx.x, g = b >> 4, s = b & 15;
  const int c = lane & 15;
  const int sr = (s + 3 * g) & 15, ns2 = 5 - (g & 1);
  const bool is1 = sr < 9, is2 = !is1 && sr < 9 + ns2;
  const int f1 = g * 9 + sr, f2 = (g >> 1) * 9 + (g & 1) * 5 + sr - 9;
  unsigned* const ldu = reinterpret_cast<unsigned*>(lds);
  const unsigned lds0 = (unsigned)(uintptr_t)(LAS float*)lds;
  STAMP_DECL;

  const __amdgpu_buffer_rsrc_t rws = mk_rsrc(ws, WS_END);
  const __amdgpu_buffer_rsrc_t rx = mk_rsrc(xg, DM * 2);

  STAMP(0);
  const unsigned tend0 = ((unsigned)__builtin_amdgcn_s_memrealtime() + KTEND) << 4;
  unsigned tend = tend0;
  auto wait_flag = [&](int idx, unsigned want) {
#pragma unroll 1
    for (unsigned spins = 0; spins < (SPIN_CAP << 6); ++spins) {
      if (__builtin_amdgcn_readfirstlane(*(volatile LAS unsigned*)&ldu[L_FLAG + idx]) >= want) break;
      __builtin_amdgcn_s_sleep(1);
    }
    asm volatile("" ::: "memory");
  };
  auto set_flag = [&](int idx) {
    asm volatile("" ::: "memory");
    if (lane == 0) *(volatile LAS unsigned*)&ldu[L_FLAG + idx] = 1u;
  };
  auto count_up = [&](int idx) {
    int r = lds_inc(lds0 + (L_FLAG + idx) * 4);
    asm volatile("s_waitcnt lgkmcnt(0)" : "+v"(r));
  };
  unsigned gm[4];
#pragma unroll
  for (int r = 0; r < 4; ++r) gm[r] = ((lane & 7) >> 1) == r ? ((lane & 1) ? 0xffff0000u : 0x0000ffffu) : 0u;
  unsigned tag;
  auto gate_vals = [&]() {
      const unsigned xp = __builtin_amdgcn_raw_buffer_load_b32(rx, lane < 8 ? b * 32 + lane * 4 : OOB, 0, 0);
      float f0, f1v;
      for (unsigned spins = 0;; ++spins) {
        asm volatile("" ::: "memory");
        v4u pf[9];
#pragma unroll
        for (int k = 0; k < 9; ++k) pf[k] = __builtin_amdgcn_raw_buffer_load_b128(rws, WS_PF + b * NFW * 128 + lane * 16, k * 1024, AUX_LD);
        unsigned bad = 0; f0 = 0.f; f1v = 0.f;
#pragma unroll
        for (int k = 0; k < 9; ++k) { bad |= (pf[k].y ^ tag) | (pf[k].w ^ tag); f0 += __uint_as_float(pf[k].x); f1v += __uint_as_float(pf[k].z); }
        if (__all(bad == 0) || spins > SPIN_CAP) break;
        __builtin_amdgcn_s_sleep(8);
      }
      f0 += dpp<XOR8>(f0); f0 = rnd(sum_x32(sum_x16(f0)));
      f1v += dpp<XOR8>(f1v); f1v = rnd(sum_x32(sum_x16(f1v)));
      if (lane < 8) {
        *reinterpret_cast<f32x2*>(&lds[L_G + lane * 2]) = f32x2{rnd(f0 / (1.0f + __expf(-f0))), rnd(f1v / (1.0f + __expf(-f1v)))};
        ldu[L_G + 16 + lane] = xp;
      }
      STAMP(8);
  };

  if (__builtin_expect(w == NW, 1)) {
    __builtin_amdgcn_s_setprio(3);
    if (KEB) {
      if (lane < 16) ldu[L_FLAG + lane] = 0u;
      asm volatile("s_waitcnt lgkmcnt(0)" ::: "memory");
      __builtin_amdgcn_s_barrier();
      __builtin_amdgcn_sched_barrier(0);
    }
    unsigned xd[5], xq[3];
#pragma unroll
    for (int k = 0; k < 5; ++k) xd[k] = __builtin_amdgcn_raw_buffer_load_b32(rx, lane * 4 + k * 256, 0, 0);
#pragma unroll
    for (int k = 0; k < 3; ++k) xq[k] = __builtin_amdgcn_raw_buffer_load_b16(rx, (144 * (s >> 2) + lane + 64 * k) * 2, 0, 0);
    const unsigned epoch = __builtin_amdgcn_raw_buffer_load_b32(rws, lane == 0 ? WS_EPOCH : OOB, 0, AUX_LD);
    v4u wqv[18];
    {
      const __amdgpu_buffer_rsrc_t rq = mk_rsrc(wq, DM * QD * 2);
      const int o = (144 * (s >> 2) + (lane >> 3)) * (QD * 2) + (g * 4 + (s & 3)) * 128 + (lane & 7) * 16;
#pragma unroll
      for (int i = 0; i < 18; ++i) wqv[i] = __builtin_amdgcn_raw_buffer_load_b128(rq, o, i * 8 * QD * 2, AUX_NT);
    }
    if (!KEB) {
      if (lane < 16) ldu[L_FLAG + lane] = 0u;
      asm volatile("s_waitcnt lgkmcnt(0)" ::: "memory");
      __builtin_amdgcn_sched_barrier(0);
      __builtin_amdgcn_s_barrier();
    }
    __builtin_amdgcn_sched_barrier(0);
    float mb, den;
    {
      float xv[10];
#pragma unroll
      for (int k = 0; k < 5; ++k) { xv[2 * k] = lo_f(xd[k]); xv[2 * k + 1] = hi_f(xd[k]); }
      float sm = 0.f;
#pragma unroll
      for (int k = 0; k < 10; ++k) sm += xv[k];
      const float mean = wave_sum(sm) * (1.0f / DM);
      float sq = 0.f;
#pragma unroll
      for (int k = 0; k < 8; ++k) { const float d = xv[k] - mean; sq = fmaf(d, d, sq); }
      const float d8 = lane < 32 ? xv[8] - mean : 0.f, d9 = lane < 32 ? xv[9] - mean : 0.f;
      sq = fmaf(d8, d8, sq); sq = fmaf(d9, d9, sq);
      const float var = wave_sum(sq) * (1.0f / DM);
      mb = rnd(mean);
      den = rnd(sqrtf(rnd(rnd(var) + 1e-5f)));
    }
    unsigned xn[3];
#pragma unroll
    for (int k = 0; k < 3; ++k) { const float v = rnd(lo_f(xq[k]) - mb) / den; xn[k] = pk_bf16(v, v); }
    STAMP(1);
    unsigned xr[18];
#pragma unroll
    for (int i = 0; i < 18; ++i) xr[i] = __shfl(xn[i >> 3], (8 * i + (lane >> 3)) & 63);
#pragma unroll
    for (int i = 0; i < 18; ++i) asm volatile("" : "+v"(xr[i]));
    __builtin_amdgcn_sched_barrier(0);
    f32x4 qa = {0.f, 0.f, 0.f, 0.f};
#pragma unroll
    for (int i = 0; i < 18; ++i) { qa = mfma16(and4(xr[i], gm), wqv[i], qa); __builtin_amdgcn_sched_barrier(0); }
    qa = gemv_fin(qa, lane);
    tag = __builtin_amdgcn_readfirstlane(epoch) + 1u;
    {
      const bool up2 = (lane & 8) != 0;
      const int col = (lane & 7) * 8 + 4 * ((lane >> 4) & 1) + (up2 ? 2 : 0);
      __builtin_amdgcn_raw_buffer_store_b128(v4u{__float_as_uint(up2 ? qa[2] : qa[0]), tag, __float_as_uint(up2 ? qa[3] : qa[1]), tag}, rws,
                                             lane < 32 ? WS_QG + ((g * 4 + (s >> 2)) * 256 + (s & 3) * 64 + col) * 8 : OOB, 0, AUX_SC1);
    }
    STAMP(2);
    if (lane == 0) ldu[L_FLAG + F_TAG] = tag;
    if (is1) {
#pragma unroll
      for (int k = 0; k < 5; ++k) {
        const float v0 = rnd(lo_f(xd[k]) - mb) / den, v1 = rnd(hi_f(xd[k]) - mb) / den;
        *reinterpret_cast<v2u*>(&ldu[L_XN + 2 * (lane + 64 * k)]) = v2u{pk_bf16(v0, v0), pk_bf16(v1, v1)};
      }
      set_flag(F_XRDY);
    }
    for (int i = 0; i < KQD; ++i) __builtin_amdgcn_s_sleep(8);
    float q4[4];
    for (unsigned spins = 0;; ++spins) {
      asm volatile("" ::: "memory");
      v4u a[8];
#pragma unroll
      for (int r = 0; r < 4; ++r) {
        a[2 * r] = __builtin_amdgcn_raw_buffer_load_b128(rws, WS_QG + g * 8192 + lane * 32, r * 2048, AUX_LD);
        a[2 * r + 1] = __builtin_amdgcn_raw_buffer_load_b128(rws, WS_QG + g * 8192 + lane * 32 + 16, r * 2048, AUX_LD);
      }
      __builtin_amdgcn_sched_barrier(0);
      set_flag(F_PISS);
      __builtin_amdgcn_sched_barrier(0);
      unsigned bad = 0;
#pragma unroll
      for (int j = 0; j < 4; ++j) q4[j] = 0.f;
#pragma unroll
      for (int r = 0; r < 4; ++r) {
        bad |= (a[2 * r].y ^ tag) | (a[2 * r].w ^ tag) | (a[2 * r + 1].y ^ tag) | (a[2 * r + 1].w ^ tag);
        q4[0] += __uint_as_float(a[2 * r].x); q4[1] += __uint_as_float(a[2 * r].z);
        q4[2] += __uint_as_float(a[2 * r + 1].x); q4[3] += __uint_as_float(a[2 * r + 1].z);
      }
      if (__all(bad == 0) || spins > SPIN_CAP) break;
      __builtin_amdgcn_s_sleep(1);
    }
    *reinterpret_cast<v2u*>(&ldu[L_Q + 2 * lane]) = v2u{pk_bf16(q4[0], q4[1]), pk_bf16(q4[2], q4[3])};
    set_flag(F_QRDY);
    STAMP(3);
    __builtin_amdgcn_s_setprio(0);
    auto pub_pf = [&]() {
      wait_flag(F_FDONE, 3);
#pragma unroll
      for (int k = 0; k < 5; ++k) {
        const int t = k * 64 + lane;
        const f32x2 yf = *reinterpret_cast<const f32x2*>(&lds[L_YF + 2 * (t < 288 ? t : 0)]);
        __builtin_amdgcn_raw_buffer_store_b128(v4u{__float_as_uint(yf.x), tag, __float_as_uint(yf.y), tag}, rws,
                                               t < 288 ? WS_PF + ((t >> 3) * NFW + f2) * 128 + (t & 7) * 16 : OOB, 0, AUX_SC1);
      }
    };
    if (KPFE && is2) pub_pf();
    if (KKDUR) {
      wait_flag(F_KPROG, KTCAL);
      const unsigned t1 = (unsigned)__builtin_amdgcn_s_memrealtime();
      __builtin_amdgcn_raw_buffer_store_b128(v4u{t1, tag, t1, tag}, rws, lane < 8 ? WS_CP + b * 128 + lane * 16 : OOB, 0, AUX_SC1);
      wait_flag(F_KPROG, KTCAL + KCPD);
      const v2u cp = __builtin_amdgcn_raw_buffer_load_b64(rws, WS_CP + (g * 16 + c) * 128, 0, AUX_LD);
      float dt = cp.y == tag ? (float)(int)(cp.x - t1) : (float)(KCPD * 41);
      dt += dpp<XOR1>(dt); dt += dpp<XOR2>(dt); dt += dpp<ROR4>(dt); dt += dpp<XOR8>(dt);
      if (lane == 0) ldu[L_FLAG + F_TEND] = ((t1 + KKDUR) << 4) + (unsigned)(int)dt | 1u;
    }
    wait_flag(F_P1CNT, NW);
    {
      const unsigned tk = (unsigned)__builtin_amdgcn_s_memrealtime();
      const float mw = lds[L_MW + (lane & 15)];
      float mx = fmaxf(mw, dpp<XOR2>(mw)); mx = fmaxf(mx, dpp<ROR4>(mx)); mx = fmaxf(mx, dpp<XOR8>(mx));
      float l = 0.f;
#pragma unroll 2
      for (int i = 0; i < (SC_TILES + 7) / 8; ++i) {
        const f32x4 v = *reinterpret_cast<const f32x4*>(&lds[L_SC + i * 256 + lane * 4]);
        l += (__builtin_amdgcn_exp2f(v[0] - mx) + __builtin_amdgcn_exp2f(v[1] - mx)) + (__builtin_amdgcn_exp2f(v[2] - mx) + __builtin_amdgcn_exp2f(v[3] - mx));
      }
      l += dpp<XOR2>(l); l += dpp<ROR4>(l); l += dpp<XOR8>(l); l = sum_x32(sum_x16(l));
      __builtin_amdgcn_raw_buffer_store_b128(v4u{(lane & 2) ? tk : __float_as_uint(mx), tag, __float_as_uint(l), tag}, rws,
                                             lane < 8 ? WS_P1M + b * 128 + lane * 16 : OOB, 0, AUX_SC1);
      STAMP(4);
      wait_flag(F_PROG, 128 - KMLT);
      v4u ml;
      for (unsigned spins = 0;; ++spins) {
        asm volatile("" ::: "memory");
        ml = __builtin_amdgcn_raw_buffer_load_b128(rws, WS_P1M + (g * 16 + ((lane >> 1) & 15)) * 128 + (lane & 1) * 16 + (lane >> 5) * 32, 0, AUX_LD);
        if (__all(((ml.y ^ tag) | (ml.w ^ tag)) == 0) || spins > SPIN_CAP) break;
        __builtin_amdgcn_s_sleep(32);
      }
      if (KVDUR) {
        float dt = (float)(int)(ml.x - tk);
        dt += dpp<XOR2>(dt); dt += dpp<ROR4>(dt); dt += dpp<XOR8>(dt); dt = sum_x16(dt);
        if (lane == 32) ldu[L_FLAG + F_TEND] = ((tk + KVDUR) << 4) + (unsigned)(int)dt | 1u;
      }
      const float mp = __uint_as_float(ml.x);
      float M = fmaxf(mp, dpp<XOR2>(mp)); M = fmaxf(M, dpp<ROR4>(M)); M = fmaxf(M, dpp<XOR8>(M)); M = max_x16(M);
      float L = __builtin_amdgcn_exp2f(mp - M) * __uint_as_float(ml.z);
      asm volatile("" : "+v"(L));
      L += dpp<XOR2>(L); L += dpp<ROR4>(L); L += dpp<XOR8>(L); L = sum_x16(L);
      const float ew = __builtin_amdgcn_exp2f(mw - M) / L;
      f32x4 hs = {0.f, 0.f, 0.f, 0.f};
#pragma unroll
      for (int ww = NSW; ww < NW; ++ww) hs += __shfl(ew, 2 * ww + (lane >> 5)) * *reinterpret_cast<const f32x4*>(&lds[L_ACC + ww * 256 + lane * 4]);
      *reinterpret_cast<f32x4*>(&lds[L_ACC + NW * 256 + lane * 4]) = hs;
      if (lane < 16) lds[L_MW + lane] = ew;
      set_flag(F_MRDY);
    }
    if (KPFE && b < NRED) gate_vals();
    STAMP(5);
    if (EXP_NOTAIL) { if (b == 0 && lane == 0) { wait_flag(F_ENDCNT, NSW); __builtin_amdgcn_raw_buffer_store_b32(tag, rws, WS_EPOCH, 0, AUX_SC1); } return; }
    if (!KPFE && is2) { wait_flag(F_PROG, 128 - NEART); pub_pf(); }
    if (KPRIO) __builtin_amdgcn_s_setprio(3);
    wait_flag(F_ENDCNT, NSW);
    for (int i = 0; i < KP1D; ++i) __builtin_amdgcn_s_sleep(4);
    {
      int o2[2] = {WS_P1O + (g * 16 + s) * 2048 + lane * 16, WS_P1O + (g * 16 + s) * 2048 + 1024 + lane * 16};
      float pv4[4];
      v4u pl[3][2];
      gather<2>(rws, o2, tag, pv4, pl);
      STAMP(6);
      auto h0 = __builtin_amdgcn_permlane32_swap(__float_as_uint(pv4[0] + pv4[2]), __float_as_uint(pv4[1] + pv4[3]), false, false);
      float v = sum_x16(__uint_as_float(h0[0]) + __uint_as_float(h0[1]));
      v += dpp<XOR8>(v);
      if ((lane & 24) == 0) lds[L_WT + 2 * (lane & 7) + (lane >> 5)] = rnd(v);
      set_flag(F_WRDY);
      STAMP(7);
      gather_end<2>(pl);
    }
    STAMP_FLUSH();
    return;
  }

  static_assert(2048 - 16 * NSW * K_O == 32, "unit split");
  const int nrole = is1 ? 4 : is2 ? 3 : 0;
  int ufirst, nt;
  {
    const int fr = sr - 9 - ns2;
    const int hu = is1 ? 1 : is2 ? 2 : (g & 1) ? 5 : 7 - fr;
    const int h0 = is1 ? sr : is2 ? 9 + 2 * (sr - 9) : 9 + 2 * ns2 + ((g & 1) ? 5 : 7) * fr;
    const int ubase = NSW * K_O * sr + h0;
    const int j = w - NSW - nrole, nh = NHELP - nrole;
    if (w < NSW) {
      ufirst = ubase + hu + w; nt = 2 * K_O;
    } else {
      const int n1 = hu - nh;
      ufirst = ubase + (j < n1 ? 2 * j : n1 + j); nt = j < n1 ? 4 : 2;
    }
  }
  const int ustep = w < NSW ? NSW : 1;
  const int sbase = w < NSW ? 4 * NHELP + w * 2 * K_O : (w - NSW) * 4;
  const int voff = lane * 16;
  auto no_keys = [&]() {
    *reinterpret_cast<f32x2*>(&lds[L_SC + sbase * 32 + lane * 2]) = f32x2{-INFINITY, -INFINITY};
    *reinterpret_cast<f32x4*>(&lds[L_ACC + w * 256 + lane * 4]) = f32x4{0.f, 0.f, 0.f, 0.f};
    if (lane < 2) lds[L_MW + 2 * w + lane] = -INFINITY;
    count_up(F_P1CNT);
  };
  __builtin_amdgcn_sched_barrier(0);
  __builtin_amdgcn_s_barrier();
  __builtin_amdgcn_sched_barrier(0);

  if (w >= NSW && w < NSW + nrole) {
    const int fw = w - NSW;
    v4u fr[24];
    if (is1) {
      const __amdgpu_buffer_rsrc_t rfg = mk_rsrc(wgate, EXP_NOFFN ? 0 : DM * FF * 2);
      const __amdgpu_buffer_rsrc_t rfu = mk_rsrc(wup, EXP_NOFFN ? 0 : DM * FF * 2);
      const int row0 = 288 * (f1 & 1) + 72 * fw + (lane >> 3);
      const int foff = row0 * (FF * 2) + (f1 >> 1) * 128 + (lane & 7) * 16;
#pragma unroll
      for (int i = 0; i < 9; ++i) {
        fr[2 * i] = __builtin_amdgcn_raw_buffer_load_b128(rfg, foff, i * 8 * FF * 2, AUX_NT);
        fr[2 * i + 1] = __builtin_amdgcn_raw_buffer_load_b128(rfu, foff, i * 8 * FF * 2, AUX_NT);
        if (KFT) { __builtin_amdgcn_sched_barrier(0); asm volatile("s_waitcnt vmcnt(%0)" :: "n"(KFT - 2)); __builtin_amdgcn_sched_barrier(0); }
      }
      __builtin_amdgcn_sched_barrier(0);
      no_keys();
      wait_flag(F_XRDY, 1);
      tag = ldu[L_FLAG + F_TAG];
      f32x4 ag = {0.f, 0.f, 0.f, 0.f}, au = {0.f, 0.f, 0.f, 0.f};
#pragma unroll
      for (int i = 0; i < 9; ++i) {
        const v4u av = and4(ldu[L_XN + row0 + 8 * i], gm);
        ag = mfma16(av, fr[2 * i], ag); au = mfma16(av, fr[2 * i + 1], au);
      }
      ag = gemv_fin(ag, lane); au = gemv_fin(au, lane);
      if ((lane & 8) == 0) *reinterpret_cast<f32x4*>(&lds[L_FR + fw * 128 + (lane >> 5) * 64 + (lane & 7) * 8 + 4 * ((lane >> 4) & 1)]) = lane < 32 ? ag : au;
      int old = lds_inc(lds0 + (L_FLAG + F_S1CNT) * 4);
      float gs = 0.f, us = 0.f;
#pragma unroll
      for (int ww = 0; ww < 4; ++ww) { gs += lds[L_FR + ww * 128 + lane]; us += lds[L_FR + ww * 128 + 64 + lane]; }
      asm volatile("s_waitcnt lgkmcnt(0)" : "+v"(old), "+v"(gs), "+v"(us));
      if (__builtin_amdgcn_readfirstlane(old) == 3)
        __builtin_amdgcn_raw_buffer_store_b128(v4u{__float_as_uint(gs), tag, __float_as_uint(us), tag}, rws, EXP_NOGU ? OOB : WS_GU + (f1 * 64 + lane) * 16, 0, AUX_SC1);
    } else {
      const __amdgpu_buffer_rsrc_t rwd = mk_rsrc(wdown + (size_t)f2 * 64 * DM, EXP_NOFFN ? 0 : 64 * DM * 2);
      const int doff = (lane >> 3) * (DM * 2) + fw * 384 + (lane & 7) * 16;
#pragma unroll
      for (int rg = 0; rg < 8; ++rg) {
#pragma unroll
        for (int k = 0; k < 3; ++k) fr[rg * 3 + k] = __builtin_amdgcn_raw_buffer_load_b128(rwd, doff + k * 128, rg * 8 * DM * 2, AUX_NT);
        if (KFT) { __builtin_amdgcn_sched_barrier(0); asm volatile("s_waitcnt vmcnt(%0)" :: "n"(KFT * 4 / 3 - 3)); __builtin_amdgcn_sched_barrier(0); }
      }
      __builtin_amdgcn_sched_barrier(0);
      no_keys();
      wait_flag(F_QRDY, 1);
      tag = ldu[L_FLAG + F_TAG];
      for (int i = 0; i < KHD; ++i) __builtin_amdgcn_s_sleep(8);
      v4u p0, p1;
      for (unsigned spins = 0;; ++spins) {
        asm volatile("" ::: "memory");
        p0 = __builtin_amdgcn_raw_buffer_load_b128(rws, EXP_NOGU ? OOB : WS_GU + (f2 * 128 + lane) * 16, 0, AUX_LD);
        p1 = __builtin_amdgcn_raw_buffer_load_b128(rws, EXP_NOGU ? OOB : WS_GU + (f2 * 128 + lane) * 16, 1024, AUX_LD);
        if (__all(((p0.y ^ tag) | (p0.w ^ tag) | (p1.y ^ tag) | (p1.w ^ tag)) == 0) || spins > SPIN_CAP || EXP_NOGU) break;
        __builtin_amdgcn_s_sleep(16);
      }
      const float gt = rnd(__uint_as_float(p0.x) + __uint_as_float(p1.x)), up = rnd(__uint_as_float(p0.z) + __uint_as_float(p1.z));
      const float inner = 0.7978845608028654f * (gt + 0.044715f * gt * gt * gt);
      const float hv = rnd(0.5f * gt * (1.0f + tanhf(inner))) * up;
      const unsigned hp = pk_bf16(hv, hv);
      f32x4 ya[3];
#pragma unroll
      for (int k = 0; k < 3; ++k) ya[k] = f32x4{0.f, 0.f, 0.f, 0.f};
#pragma unroll
      for (int rg = 0; rg < 8; ++rg) {
        const v4u av = and4(__shfl(hp, rg * 8 + (lane >> 3)), gm);
#pragma unroll
        for (int k = 0; k < 3; ++k) ya[k] = mfma16(av, fr[rg * 3 + k], ya[k]);
      }
#pragma unroll
      for (int k = 0; k < 3; ++k) {
        ya[k] = gemv_fin(ya[k], lane);
        if (lane < 32 && (lane & 8) == 0) *reinterpret_cast<f32x4*>(&lds[L_YF + fw * 192 + k * 64 + (lane & 7) * 8 + 4 * (lane >> 4)]) = ya[k];
      }
      count_up(F_FDONE);
    }
    STAMP(4);
  } else {
    const __amdgpu_buffer_rsrc_t rk = mk_rsrc(kg + (size_t)g * SK * 128, SK * 256);
    const __amdgpu_buffer_rsrc_t rv = mk_rsrc(vg + (size_t)g * SK * 128, SK * 256);
#define TOFF(t) ((ufirst + ((t) >> 1) * ustep) << 13)
#define tb(n, i) buf[(n) * TL + (i)]
    v4u buf[(D2 > 4 ? D2 : 4) * TL];
    if (!KKEARLY && nrole) wait_flag(F_PISS, 1);
    {
      const int t0 = nt > 0 ? TOFF(0) : OOB;
#pragma unroll
      for (int i = 0; i < 2 * TL; ++i) buf[i] = __builtin_amdgcn_raw_buffer_load_b128(rk, voff + i * 1024, t0, AUX_NT);
    }
    __builtin_amdgcn_sched_barrier(0);
    {
      const int cap = w < NSW ? 2 * K_O + (w == NSW - 1 ? 8 : 0) : 4;
#pragma unroll 1
      for (int t = nt; t < cap; t += 2) lds[L_SC + (sbase + t) * 32 + lane] = -INFINITY;
    }
    STAMP(1);
    const int hf = (c >> 2) & 1;
    v4u qf, qs;
    auto get_q = [&]() {
      wait_flag(F_QRDY, 1);
      STAMP(3);
      qf = *reinterpret_cast<const v4u*>(&ldu[L_Q + hf * 64 + 4 * c]);
      qs = *reinterpret_cast<const v4u*>(&ldu[L_Q + (hf ^ 1) * 64 + 4 * c]);
    };
    constexpr float LOG2E = 1.4426950408889634f, SCALE = 0.08838834764831845f;
    const bool b0 = (c & 1) != 0, b1 = (c & 2) != 0;
    float* const scw = &lds[L_SC + sbase * 32 + (lane >> 4) * 8 + (lane & 7)];
    float m = -INFINITY;
    float y[4];
    auto qk1 = [&](const v4u* kt) {
#pragma unroll
      for (int i = 0; i < TL; ++i) {
        float xf = 0.f, xs = 0.f;
#pragma unroll
        for (int j = 0; j < 4; ++j) { xf = dot2(kt[i][j], qf[j], xf); xs = dot2(kt[i][j], qs[j], xs); }
        y[i] = xf + dpp<XOR7>(xs);
      }
      asm volatile("" : "+v"(y[0]), "+v"(y[1]), "+v"(y[2]), "+v"(y[3]));
      __builtin_amdgcn_sched_barrier(0);
    };
    auto qk2 = [&](int t) {
      const float u0 = (b0 ? y[1] : y[0]) + dpp<XOR1>(b0 ? y[0] : y[1]);
      const float u1 = (b0 ? y[3] : y[2]) + dpp<XOR1>(b0 ? y[2] : y[3]);
      float sc = (b1 ? u1 : u0) + dpp<XOR2>(b1 ? u0 : u1);
      sc += dpp<XOR8>(sc);
      const float tl2 = rnd(rnd(sc) * SCALE) * LOG2E;
      m = fmaxf(m, tl2);
      scw[t * 32] = tl2;
    };
    int tk = 0;
    if (KOPEN && w < NSW) {
      wait_flag(F_PISS, 1);
      const int t2 = TOFF(2), t4 = TOFF(4);
#pragma unroll
      for (int i = 0; i < 2 * TL; ++i) buf[2 * TL + i] = __builtin_amdgcn_raw_buffer_load_b128(rk, voff + i * 1024, t2, AUX_NT);
      __builtin_amdgcn_sched_barrier(0);
      get_q();
#pragma unroll
      for (int n = 0; n < 2; ++n) {
        qk1(&tb(n, 0));
#pragma unroll
        for (int i = 0; i < TL; ++i) tb(n, i) = __builtin_amdgcn_raw_buffer_load_b128(rk, voff + i * 1024, t4 + n * 4096, AUX_NT);
        __builtin_amdgcn_sched_barrier(0);
        qk2(n);
      }
      TRACE(0);
      qk1(&tb(2, 0)); qk2(2);
      qk1(&tb(3, 0)); qk2(3);
      TRACE(1);
      tk = 4;
    } else get_q();
    unsigned next = (unsigned)__builtin_amdgcn_s_memrealtime() << 4, tau = KTAU;
    unsigned tbase = tend, vshift = 0;
    auto retarget = [&]() {
      const unsigned te = __builtin_amdgcn_readfirstlane(*(volatile LAS unsigned*)&ldu[L_FLAG + F_TEND]);
      if (te && te != tbase) { tend += te - tbase - vshift; tbase = te; vshift = 0; }
      return te != 0;
    };
    auto aim = [&](int left) {
      const int v = __builtin_amdgcn_readfirstlane((int)((float)(int)(tend - next) * __builtin_amdgcn_rcpf((float)left)));
      tau = (unsigned)min(KTAUHI, max(KTAULO, v));
    };
#pragma unroll 1
    for (int t = tk; t < nt; t += 2) {
      const bool lastk = t + 2 >= nt;
      const __amdgpu_buffer_rsrc_t rn = lastk ? rv : rk;
      const int nxt = TOFF(lastk ? 0 : t + 2);
      if (w == 0 && lane == 0) *(volatile LAS int*)&ldu[L_FLAG + F_KPROG] = t;
      if (t == KTCAL) { tend = tend0; tbase = tend0; }
      if (t >= KTCAL && ((t - KTCAL) & KCALM) == 0) { if (!KKDUR || retarget()) aim(4 * K_O - t); }
#pragma unroll
      for (int n = 0; n < 2; ++n) {
        qk1(&tb(n, 0));
        pace(next, tau, tend);
#pragma unroll
        for (int i = 0; i < TL; ++i) tb(n, i) = __builtin_amdgcn_raw_buffer_load_b128(rn, voff + i * 1024, nxt + n * 4096, AUX_NT);
        __builtin_amdgcn_sched_barrier(0);
        qk2(t + n);
      }
      TRACE(t >> 1);
    }
    m = fmaxf(m, dpp<XOR1>(m)); m = fmaxf(m, dpp<XOR2>(m));
    m = max_x32(max_x16(m));
    if ((lane & 59) == 0) lds[L_MW + 2 * w + (lane >> 2)] = m;
    if (w < NSW) count_up(F_P1CNT);
    f32x4 acc = {0.f, 0.f, 0.f, 0.f};
    unsigned amask[4];
    {
      const int jp = (c & 3) + 4 * (c >> 3);
#pragma unroll
      for (int r = 0; r < 4; ++r) amask[r] = (jp >> 1) == r ? ((jp & 1) ? 0xffff0000u : 0x0000ffffu) : 0u;
    }
    auto pv = [&](float sv, const v4u* vt) {
      const float p = __builtin_amdgcn_exp2f(sv - m);
      const unsigned pp = pk_bf16(p, p);
#pragma unroll
      for (int i = 0; i < TL; ++i) {
        const unsigned pb = i == 0 ? dppu<0x00>(pp) : i == 1 ? dppu<0x55>(pp) : i == 2 ? dppu<0xAA>(pp) : dppu<0xFF>(pp);
        acc = mfma16(and4(pb, amask), vt[i], acc);
      }
      asm volatile("" : "+v"(acc));
      __builtin_amdgcn_sched_barrier(0);
    };
#define VOFF(t) ((t) < nt ? TOFF(t) + ((t) & 1) * 4096 : OOB)
    int t = 0;
    vshift = tend - tbase;
    auto vrun = [&](int lim) {
#pragma unroll 1
      for (; t < lim; t += 2) {
        if (((t + 2 * K_O - KTCAL) & KCALM) == 0) {
          retarget();
          aim(2 * K_O - t);
        }
        const int nxt = TOFF(t + 2);
        const float s0 = scw[t * 32], s1 = scw[t * 32 + 32];
#pragma unroll
        for (int n = 0; n < 2; ++n) {
          pv(n ? s1 : s0, &tb(n, 0));
          pace(next, tau, tend);
#pragma unroll
          for (int i = 0; i < TL; ++i) tb(n, i) = __builtin_amdgcn_raw_buffer_load_b128(rv, voff + i * 1024, nxt + n * 4096, AUX_NT);
          __builtin_amdgcn_sched_barrier(0);
        }
        TRACE((nt + t) >> 1);
        if (w == 0 && lane == 0) *(volatile LAS int*)&ldu[L_FLAG + F_PROG] = t + 2 + 128 - nt;
      }
    };
    vrun(nt - KXMAX);
    {
      const int late = (int)(next + KXMAX * KTAUN - tend);
      vrun(nt - min(KXMAX, XMID + 2 * max(0, (late * KXC) >> 15)));
    }
#pragma unroll
    for (int k = 2; k < D2; ++k) {
      const int o = VOFF(t + k);
#pragma unroll
      for (int i = 0; i < TL; ++i) tb(k, i) = __builtin_amdgcn_raw_buffer_load_b128(rv, voff + i * 1024, o, AUX_NT);
    }
    __builtin_amdgcn_sched_barrier(0);
    float ed = 1.f;
    if (w < NSW) {
      if (KPRIO) __builtin_amdgcn_s_setprio(2);
      wait_flag(F_MRDY, 1);
      const int hh = (lane >> 4) & 1;
      ed = lds[L_MW + 2 * w + hh];
    }
    tag = ldu[L_FLAG + F_TAG];
#pragma unroll 1
    for (; t < nt; t += D2) {
      if (w == 0 && lane == 0) *(volatile LAS int*)&ldu[L_FLAG + F_PROG] = t + D2 - 2 + 128 - nt;
#pragma unroll
      for (int k = 0; k < D2; ++k) {
        const float sv = t + k < nt ? scw[(t + k) * 32] : -INFINITY;
        const int o = VOFF(t + k + D2);
        pv(sv, &tb(k, 0));
#pragma unroll
        for (int i = 0; i < TL; ++i) tb(k, i) = __builtin_amdgcn_raw_buffer_load_b128(rv, voff + i * 1024, o, AUX_NT);
        __builtin_amdgcn_sched_barrier(0);
      }
    }
    STAMP(4);
    const int fo = ((lane >> 4) & 1) * 128 + 8 * c + 4 * (lane >> 5);
    *reinterpret_cast<f32x4*>(&lds[L_ACC + w * 256 + fo]) = acc * ed;
    if (w >= NSW) count_up(F_P1CNT);
    else {
      int old = lds_inc(lds0 + (L_FLAG + F_ENDCNT) * 4);
      f32x2 o0 = *reinterpret_cast<const f32x2*>(&lds[L_ACC + NW * 256 + 2 * lane]), o1 = *reinterpret_cast<const f32x2*>(&lds[L_ACC + NW * 256 + 128 + 2 * lane]);
#pragma unroll
      for (int ww = 0; ww < NSW; ++ww) { o0 += *reinterpret_cast<const f32x2*>(&lds[L_ACC + ww * 256 + 2 * lane]); o1 += *reinterpret_cast<const f32x2*>(&lds[L_ACC + ww * 256 + 128 + 2 * lane]); }
      asm volatile("s_waitcnt lgkmcnt(0)" : "+v"(old), "+v"(o0), "+v"(o1));
      if (EXP_NOTAIL) { if (o0[0] == 1.2345f) out[1] = 2; return; }
      if (__builtin_amdgcn_readfirstlane(old) == NSW - 1) {
        const int po = WS_P1O + ((g * 16 + (lane >> 3)) * 16 + s) * 128 + (lane & 7) * 16;
        __builtin_amdgcn_raw_buffer_store_b128(v4u{__float_as_uint(o0[0]), tag, __float_as_uint(o0[1]), tag}, rws, po, 0, AUX_SC1);
        __builtin_amdgcn_raw_buffer_store_b128(v4u{__float_as_uint(o1[0]), tag, __float_as_uint(o1[1]), tag}, rws, po, 8 * 2048, AUX_SC1);
      }
      STAMP(7);
    }
  }

  if (w >= NSW) {
    const int t = (w - NSW) * 64 + lane;
    auto doze = [&](int idx, unsigned want) {
#pragma unroll 1
      for (unsigned spins = 0; spins < (SPIN_CAP << 4); ++spins) {
        if (__builtin_amdgcn_readfirstlane(*(volatile LAS unsigned*)&ldu[L_FLAG + idx]) >= want) break;
        __builtin_amdgcn_s_sleep(KWOL ? 2 : 8);
      }
    };
    if (KWOL == 1) doze(F_PROG, 128 - KWOT);
    if (KWOL == 2) doze(F_ENDCNT, KWOT);
    float wf[32];
    {
      const __amdgpu_buffer_rsrc_t rwo = mk_rsrc(wo + (size_t)b * 16 * DM, EXP_NOWO ? 0 : 16 * DM * 2);
      unsigned wod[16];
#pragma unroll
      for (int j = 0; j < 16; ++j) wod[j] = __builtin_amdgcn_raw_buffer_load_b32(rwo, t < 288 ? t * 4 : OOB, j * DM * 2, AUX_NT);
#pragma unroll
      for (int j = 0; j < 16; ++j) { wf[2 * j] = lo_f(wod[j]); wf[2 * j + 1] = hi_f(wod[j]); }
#pragma unroll
      for (int j = 0; j < 32; ++j) asm volatile("" : "+v"(wf[j]));
    }
    if (EXP_NOTAIL) { if (wf[0] + wf[31] == 1.2345f) out[2] = 2; return; }
    if (!KWOL) doze(F_PROG, 128 - NEART);
    STAMP(8);
    tag = ldu[L_FLAG + F_TAG];
    const int po = t < 288 && (!EXP_P2 || (b & 3) == 0) ? WS_P2 + ((t >> 3) * NWG + b) * 128 + (t & 7) * 16 : OOB;
    f32x4 o4[4];
#pragma unroll 1
    for (unsigned spins = 0; spins < (SPIN_CAP << 8); ++spins) {
      unsigned f = *(volatile LAS unsigned*)&ldu[L_FLAG + F_WRDY];
      asm volatile("" ::: "memory");
#pragma unroll
      for (int k = 0; k < 4; ++k) o4[k] = *reinterpret_cast<const f32x4*>(&lds[L_WT + 4 * k]);
      asm volatile("" : "+v"(o4[0]), "+v"(o4[1]), "+v"(o4[2]), "+v"(o4[3]) :: "memory");
      if (__builtin_amdgcn_readfirstlane(f) != 0u) break;
    }
    STAMP(9);
    f32x2 ya = {0.f, 0.f}, yb = {0.f, 0.f};
#pragma unroll
    for (int j = 0; j < 16; j += 2) {
      ya += o4[j >> 2][j & 3] * f32x2{wf[2 * j], wf[2 * j + 1]};
      yb += o4[j >> 2][(j & 3) + 1] * f32x2{wf[2 * j + 2], wf[2 * j + 3]};
    }
    const float y0 = ya.x + yb.x, y1 = ya.y + yb.y;
    __builtin_amdgcn_raw_buffer_store_b128(v4u{__float_as_uint(y0), tag, __float_as_uint(y1), tag}, rws, po, 0, AUX_SC1);
    STAMP(10);
  }

  if (b < NRED) {
    if (!KPFE && w == 0) gate_vals();
    if (w < NSW) wait_flag(F_WRDY, 1);
    for (int i = 0; i < KP2D; ++i) __builtin_amdgcn_s_sleep(4);
    int o4[4];
    float sv[8];
#pragma unroll
    for (int k = 0; k < 4; ++k) o4[k] = EXP_P2 && ((lane >> 3) & 3) ? OOB : WS_P2 + b * NWG * 128 + (w * 64 + lane) * 16 + k * 8192;
    v4u pl[3][4];
    gather<4>(rws, o4, tag, sv, pl);
    STAMP(2);
    float s0 = (sv[0] + sv[2]) + (sv[4] + sv[6]), s1 = (sv[1] + sv[3]) + (sv[5] + sv[7]);
    s0 += dpp<XOR8>(s0); s0 = sum_x32(sum_x16(s0));
    s1 += dpp<XOR8>(s1); s1 = sum_x32(sum_x16(s1));
    if (lane < 8) *reinterpret_cast<f32x2*>(&lds[L_FIN + w * 16 + lane * 2]) = f32x2{s0, s1};
    int old = lds_inc(lds0 + (L_FLAG + F_FINCNT) * 4);
    f32x2 t2 = *reinterpret_cast<const f32x2*>(&lds[L_FIN + lane * 2]);
    f32x2 gv = *reinterpret_cast<const f32x2*>(&lds[L_G + (lane & 7) * 2]);
    unsigned xp = ldu[L_G + 16 + (lane & 7)];
    asm volatile("s_waitcnt lgkmcnt(0)" : "+v"(old), "+v"(t2), "+v"(gv), "+v"(xp));
    if (__builtin_amdgcn_readfirstlane(old) == NW - 1) {
      t2.x += dpp<XOR8>(t2.x); t2.x = sum_x32(sum_x16(t2.x));
      t2.y += dpp<XOR8>(t2.y); t2.y = sum_x32(sum_x16(t2.y));
      const unsigned r = pk_bf16(lo_f(xp) + rnd(rnd(t2.x) * gv.x), hi_f(xp) + rnd(rnd(t2.y) * gv.y));
      __builtin_amdgcn_raw_buffer_store_b32(r, mk_rsrc(out, DM * 2), lane < 8 ? b * 32 + lane * 4 : OOB, 0, KOUTSC);
      if (b == 0 && lane == 0) __builtin_amdgcn_raw_buffer_store_b32(tag, rws, WS_EPOCH, 0, AUX_SC1);
      STAMP(11);
    }
    gather_end<4>(pl);
  }
  STAMP_FLUSH();
}

}

extern "C" void kernel_launch(void* const* d_in, const int* in_sizes, int n_in, void* d_out, int out_size, void* d_ws, size_t ws_size,
                              hipStream_t stream) {
  (void)in_sizes; (void)n_in; (void)out_size; (void)ws_size;
  fused_decode<<<NWG, NTHR, 0, stream>>>((const unsigned short*)d_in[0], (const unsigned short*)d_in[1], (const unsigned short*)d_in[2],
                                         (const unsigned short*)d_in[3], (const unsigned short*)d_in[4], (const unsigned short*)d_in[6],
                                         (const unsigned short*)d_in[7], (const unsigned short*)d_in[8], (unsigned short*)d_out,
                                         (unsigned char*)d_ws);
}
```

```cpp
#include <hip/hip_runtime.h>
#include <hip/hip_bf16.h>
#include <stdint.h>

namespace {

typedef unsigned v4u __attribute__((ext_vector_type(4)));
typedef unsigned v2u __attribute__((ext_vector_type(2)));
typedef float f32x4 __attribute__((ext_vector_type(4)));
typedef float f32x2 __attribute__((ext_vector_type(2)));
typedef __bf16 bf16x8 __attribute__((ext_vector_type(8)));
typedef __bf16 bf16x2 __attribute__((ext_vector_type(2)));
#define LAS __attribute__((address_space(3)))

constexpr int DM = 576, SK = 65536, FF = 4608, QD = 4096;
constexpr int NWG = 256;
constexpr int NSW = 4;
constexpr int NW = 9;
constexpr int NTHR = (NW + 1) * 64;
constexpr int NRED = 36;
constexpr int NFW = 72;
constexpr int TL = 4;
constexpr int NT = 64;
constexpr int OOB = 0x70000000;

constexpr int TAU0 = 884, TAU_LO = 850, TAU_HI = 907, TAU_N = 865;
constexpr int BURST = 40, STALL = 100;
constexpr int T_CAL = 12, CP_D = 6;
constexpr int K_DUR = 6430, V_DUR = 3535;
constexpr int ML_T = 54;
constexpr int X_MIN = 4, X_MAX = 20, X_C = 68;
constexpr int NEAR_T = 4;

constexpr int WS_EPOCH = 0;
constexpr int WS_QG = 4096;
constexpr int WS_ML = WS_QG + 16 * 4 * 256 * 8;
constexpr int WS_CP = WS_ML + NWG * 128;
constexpr int WS_P1 = WS_CP + NWG * 128;
constexpr int WS_P2 = WS_P1 + 16 * 16 * 16 * 128;
constexpr int WS_PF = WS_P2 + NRED * NWG * 128;
constexpr int WS_GU = WS_PF + NRED * NFW * 128;
constexpr int WS_END = WS_GU + NFW * 2 * 64 * 16;

constexpr int AUX_NT = 2, AUX_SC1 = 16;
constexpr int RSRC_W3 = 0x00020000;
constexpr unsigned SPIN_CAP = 1u << 14;

constexpr int L_FLAG = 0;
enum { F_TAG, F_QRDY, F_XRDY, F_FDONE, F_POLL, F_P1CNT, F_SRDY, F_KPROG, F_VPROG, F_ORDY, F_ENDCNT, F_FINCNT, F_S1CNT, F_TEND };
constexpr int L_MW = 16;
constexpr int L_O = L_MW + 16;
constexpr int L_G = L_O + 16;
constexpr int L_Q = L_G + 32;
constexpr int L_XN = L_Q + 128;
constexpr int L_YF = L_XN + 640;
constexpr int L_FIN = L_YF + 576;
constexpr int L_ACC = L_FIN + 128;
constexpr int L_SC = L_ACC + NSW * 256;
constexpr int L_FLOATS = 24 * 1024;
static_assert(L_SC % 4 == 0 && L_ACC % 4 == 0 && L_Q % 4 == 0 && L_O % 4 == 0 && L_SC + NSW * NT * 32 <= L_FLOATS, "LDS map");
static_assert(16 * NSW * NT * 16 == SK && X_MIN >= 4, "key split");

template <int CTRL> __device__ __forceinline__ float dpp(float x) {
  return __builtin_bit_cast(float, __builtin_amdgcn_mov_dpp(__builtin_bit_cast(int, x), CTRL, 0xf, 0xf, true));
}
template <int CTRL> __device__ __forceinline__ unsigned dppu(unsigned x) {
  return (unsigned)__builtin_amdgcn_mov_dpp((int)x, CTRL, 0xf, 0xf, true);
}
constexpr int XOR1 = 0xB1, XOR2 = 0x4E, XOR7 = 0x141, XOR8 = 0x128, ROR4 = 0x124;
__device__ __forceinline__ float sum_x16(float x) {
  auto s = __builtin_amdgcn_permlane16_swap(__float_as_uint(x), __float_as_uint(x), false, false);
  return __uint_as_float(s[0]) + __uint_as_float(s[1]);
}
__device__ __forceinline__ float sum_x32(float x) {
  auto s = __builtin_amdgcn_permlane32_swap(__float_as_uint(x), __float_as_uint(x), false, false);
  return __uint_as_float(s[0]) + __uint_as_float(s[1]);
}
__device__ __forceinline__ float max_x16(float x) {
  auto s = __builtin_amdgcn_permlane16_swap(__float_as_uint(x), __float_as_uint(x), false, false);
  return fmaxf(__uint_as_float(s[0]), __uint_as_float(s[1]));
}
__device__ __forceinline__ float max_x32(float x) {
  auto s = __builtin_amdgcn_permlane32_swap(__float_as_uint(x), __float_as_uint(x), false, false);
  return fmaxf(__uint_as_float(s[0]), __uint_as_float(s[1]));
}
__device__ __forceinline__ float wave_sum(float x) {
  x += dpp<XOR1>(x); x += dpp<XOR2>(x); x += dpp<ROR4>(x); x += dpp<XOR8>(x);
  return sum_x32(sum_x16(x));
}
__device__ __forceinline__ unsigned pk_bf16(float lo, float hi) {
  bf16x2 v = {(__bf16)lo, (__bf16)hi};
  return __builtin_bit_cast(unsigned, v);
}
__device__ __forceinline__ float rnd(float x) { return (float)(__bf16)x; }
__device__ __forceinline__ float lo_f(unsigned u) { return __uint_as_float(u << 16); }
__device__ __forceinline__ float hi_f(unsigned u) { return __uint_as_float(u & 0xffff0000u); }
__device__ __forceinline__ float dot2(unsigned a, unsigned b, float c) {
  return __builtin_amdgcn_fdot2_f32_bf16(__builtin_bit_cast(bf16x2, a), __builtin_bit_cast(bf16x2, b), c, false);
}
__device__ __forceinline__ __amdgpu_buffer_rsrc_t mk_rsrc(const void* p, int bytes) {
  return __builtin_amdgcn_make_buffer_rsrc((void*)p, 0, bytes, RSRC_W3);
}
__device__ __forceinline__ v4u and4(unsigned x, const unsigned (&m)[4]) { return v4u{x & m[0], x & m[1], x & m[2], x & m[3]}; }
__device__ __forceinline__ f32x4 mfma16(v4u a, v4u bm, f32x4 cacc) {
  return __builtin_amdgcn_mfma_f32_16x16x32_bf16(__builtin_bit_cast(bf16x8, a), __builtin_bit_cast(bf16x8, bm), cacc, 0, 0, 0);
}
__device__ __forceinline__ f32x4 gemv_fin(f32x4 d, int lane) {
  const bool valid = (lane >> 5) == ((lane >> 3) & 1);
#pragma unroll
  for (int r = 0; r < 4; ++r) { float t = valid ? d[r] : 0.f; t += dpp<XOR8>(t); d[r] = sum_x32(t); }
  return d;
}
__device__ __forceinline__ int lds_inc(unsigned addr) {
  int r; unsigned long long save;
  asm volatile("s_mov_b64 %1, exec\n\ts_mov_b64 exec, 1\n\tds_add_rtn_u32 %0, %2, %3\n\ts_mov_b64 exec, %1"
               : "=&v"(r), "=&s"(save) : "v"(addr), "v"(1) : "memory");
  return r;
}
template <int N>
__device__ __forceinline__ void gather(__amdgpu_buffer_rsrc_t rs, int (&off)[N], unsigned tag, float (&val)[2 * N], v4u (&p)[3][N]) {
  auto issue = [&](int j) {
    asm volatile("" ::: "memory");
#pragma unroll
    for (int k = 0; k < N; ++k) p[j][k] = __builtin_amdgcn_raw_buffer_load_b128(rs, off[k], 0, AUX_SC1);
    __builtin_amdgcn_sched_barrier(0);
  };
  auto check = [&](int j) {
    bool done = true;
#pragma unroll
    for (int k = 0; k < N; ++k) {
      const bool ok = off[k] != OOB && ((p[j][k].y ^ tag) | (p[j][k].w ^ tag)) == 0;
      val[2 * k] = ok ? __uint_as_float(p[j][k].x) : val[2 * k]; val[2 * k + 1] = ok ? __uint_as_float(p[j][k].z) : val[2 * k + 1];
      off[k] = ok ? OOB : off[k];
      done = done && off[k] == OOB;
    }
    return __all(done) != 0;
  };
#pragma unroll
  for (int k = 0; k < 2 * N; ++k) val[k] = 0.f;
  issue(0); __builtin_amdgcn_s_sleep(5); issue(1); __builtin_amdgcn_s_sleep(5);
#pragma unroll 1
  for (unsigned spins = 0; spins < SPIN_CAP; ++spins) {
    issue(2); if (check(0)) break;
    issue(0); if (check(1)) break;
    issue(1); if (check(2)) break;
  }
}
template <int N>
__device__ __forceinline__ void gather_end(v4u (&p)[3][N]) {
#pragma unroll
  for (int j = 0; j < 3; ++j)
#pragma unroll
    for (int k = 0; k < N; ++k) asm volatile("" :: "v"(p[j][k]));
}
__device__ __forceinline__ void pace(unsigned& next, unsigned tau, unsigned& tend) {
  int d;
  asm volatile("1:\n\ts_memrealtime vcc\n\ts_waitcnt lgkmcnt(0)\n\ts_lshl_b32 %0, vcc_lo, 4\n\ts_sub_i32 %0, %0, %1\n\ts_cmp_ge_i32 %0, 0\n\ts_cbranch_scc1 2f\n\ts_sleep 1\n\ts_branch 1b\n2:"
               : "=&s"(d) : "s"(next) : "scc", "vcc");
  next += tau + (unsigned)max(0, d - BURST * 16);
  tend += d > STALL * 16 ? d : 0;
}

__global__ void __launch_bounds__(NTHR, 3)
fused_decode(const unsigned short* __restrict__ xg, unsigned char* __restrict__ ws, const unsigned short* __restrict__ wq,
             const unsigned short* __restrict__ kg, const unsigned short* __restrict__ wgate, const unsigned short* __restrict__ wup,
             const unsigned short* __restrict__ wdown, const unsigned short* __restrict__ vg, const unsigned short* __restrict__ wo,
             unsigned short* __restrict__ out) {
  __shared__ __attribute__((aligned(16))) float lds[L_FLOATS];
  asm volatile("" :: "s"(xg), "s"(ws), "s"(wq), "s"(kg), "s"(wgate), "s"(wup), "s"(wdown));
  const int lane = threadIdx.x & 63, pw = __builtin_amdgcn_readfirstlane(threadIdx.x >> 6);
  const int w = pw == 0 ? NW : pw - 1;
  const int b = blockIdx.x, g = b >> 4, s = b & 15;
  const int c = lane & 15;
  const int sr = (s + 3 * g) & 15, ns2 = 5 - (g & 1);
  const bool is1 = sr < 9, is2 = !is1 && sr < 9 + ns2;
  const int f1 = g * 9 + sr, f2 = (g >> 1) * 9 + (g & 1) * 5 + sr - 9;
  unsigned* const ldu = reinterpret_cast<unsigned*>(lds);
  const unsigned lds0 = (unsigned)(uintptr_t)(LAS float*)lds;

  const __amdgpu_buffer_rsrc_t rws = mk_rsrc(ws, WS_END);
  const __amdgpu_buffer_rsrc_t rx = mk_rsrc(xg, DM * 2);

  auto wait_flag = [&](int idx, unsigned want) {
#pragma unroll 1
    for (unsigned spins = 0; spins < (SPIN_CAP << 6); ++spins) {
      if (__builtin_amdgcn_readfirstlane(*(volatile LAS unsigned*)&ldu[L_FLAG + idx]) >= want) break;
      __builtin_amdgcn_s_sleep(1);
    }
    asm volatile("" ::: "memory");
  };
  auto set_flag = [&](int idx) {
    asm volatile("" ::: "memory");
    if (lane == 0) *(volatile LAS unsigned*)&ldu[L_FLAG + idx] = 1u;
  };
  auto count_up = [&](int idx) {
    int r = lds_inc(lds0 + (L_FLAG + idx) * 4);
    asm volatile("s_waitcnt lgkmcnt(0)" : "+v"(r));
  };
  unsigned gm[4];
#pragma unroll
  for (int r = 0; r < 4; ++r) gm[r] = ((lane & 7) >> 1) == r ? ((lane & 1) ? 0xffff0000u : 0x0000ffffu) : 0u;
  unsigned tag;

  if (__builtin_expect(w == NW, 1)) {
    __builtin_amdgcn_s_setprio(3);
    unsigned xd[5], xq[3];
#pragma unroll
    for (int k = 0; k < 5; ++k) xd[k] = __builtin_amdgcn_raw_buffer_load_b32(rx, lane * 4 + k * 256, 0, 0);
#pragma unroll
    for (int k = 0; k < 3; ++k) xq[k] = __builtin_amdgcn_raw_buffer_load_b16(rx, (144 * (s >> 2) + lane + 64 * k) * 2, 0, 0);
    const unsigned epoch = __builtin_amdgcn_raw_buffer_load_b32(rws, lane == 0 ? WS_EPOCH : OOB, 0, AUX_SC1);
    v4u wqv[18];
    {
      const __amdgpu_buffer_rsrc_t rq = mk_rsrc(wq, DM * QD * 2);
      const int o = (144 * (s >> 2) + (lane >> 3)) * (QD * 2) + (g * 4 + (s & 3)) * 128 + (lane & 7) * 16;
#pragma unroll
      for (int i = 0; i < 18; ++i) wqv[i] = __builtin_amdgcn_raw_buffer_load_b128(rq, o, i * 8 * QD * 2, AUX_NT);
    }
    if (lane < 16) ldu[L_FLAG + lane] = 0u;
    asm volatile("s_waitcnt lgkmcnt(0)" ::: "memory");
    __builtin_amdgcn_sched_barrier(0);
    __builtin_amdgcn_s_barrier();
    __builtin_amdgcn_sched_barrier(0);
    float mb, den;
    {
      float xv[10];
#pragma unroll
      for (int k = 0; k < 5; ++k) { xv[2 * k] = lo_f(xd[k]); xv[2 * k + 1] = hi_f(xd[k]); }
      float sm = 0.f;
#pragma unroll
      for (int k = 0; k < 10; ++k) sm += xv[k];
      const float mean = wave_sum(sm) * (1.0f / DM);
      float sq = 0.f;
#pragma unroll
      for (int k = 0; k < 8; ++k) { const float d = xv[k] - mean; sq = fmaf(d, d, sq); }
      const float d8 = lane < 32 ? xv[8] - mean : 0.f, d9 = lane < 32 ? xv[9] - mean : 0.f;
      sq = fmaf(d8, d8, sq); sq = fmaf(d9, d9, sq);
      const float var = wave_sum(sq) * (1.0f / DM);
      mb = rnd(mean);
      den = rnd(sqrtf(rnd(rnd(var) + 1e-5f)));
    }
    unsigned xn[3];
#pragma unroll
    for (int k = 0; k < 3; ++k) { const float v = rnd(lo_f(xq[k]) - mb) / den; xn[k] = pk_bf16(v, v); }
    unsigned xr[18];
#pragma unroll
    for (int i = 0; i < 18; ++i) xr[i] = __shfl(xn[i >> 3], (8 * i + (lane >> 3)) & 63);
#pragma unroll
    for (int i = 0; i < 18; ++i) asm volatile("" : "+v"(xr[i]));
    __builtin_amdgcn_sched_barrier(0);
    f32x4 qa = {0.f, 0.f, 0.f, 0.f};
#pragma unroll
    for (int i = 0; i < 18; ++i) { qa = mfma16(and4(xr[i], gm), wqv[i], qa); __builtin_amdgcn_sched_barrier(0); }
    qa = gemv_fin(qa, lane);
    tag = __builtin_amdgcn_readfirstlane(epoch) + 1u;
    {
      const bool up2 = (lane & 8) != 0;
      const int col = (lane & 7) * 8 + 4 * ((lane >> 4) & 1) + (up2 ? 2 : 0);
      __builtin_amdgcn_raw_buffer_store_b128(v4u{__float_as_uint(up2 ? qa[2] : qa[0]), tag, __float_as_uint(up2 ? qa[3] : qa[1]), tag}, rws,
                                             lane < 32 ? WS_QG + ((g * 4 + (s >> 2)) * 256 + (s & 3) * 64 + col) * 8 : OOB, 0, AUX_SC1);
    }
    if (lane == 0) ldu[L_FLAG + F_TAG] = tag;
    if (is1) {
#pragma unroll
      for (int k = 0; k < 5; ++k) {
        const float v0 = rnd(lo_f(xd[k]) - mb) / den, v1 = rnd(hi_f(xd[k]) - mb) / den;
        *reinterpret_cast<v2u*>(&ldu[L_XN + 2 * (lane + 64 * k)]) = v2u{pk_bf16(v0, v0), pk_bf16(v1, v1)};
      }
      set_flag(F_XRDY);
    }
    for (int i = 0; i < 5; ++i) __builtin_amdgcn_s_sleep(8);
    float q4[4];
    for (unsigned spins = 0;; ++spins) {
      asm volatile("" ::: "memory");
      v4u a[8];
#pragma unroll
      for (int r = 0; r < 4; ++r) {
        a[2 * r] = __builtin_amdgcn_raw_buffer_load_b128(rws, WS_QG + g * 8192 + lane * 32, r * 2048, AUX_SC1);
        a[2 * r + 1] = __builtin_amdgcn_raw_buffer_load_b128(rws, WS_QG + g * 8192 + lane * 32 + 16, r * 2048, AUX_SC1);
      }
      __builtin_amdgcn_sched_barrier(0);
      set_flag(F_POLL);
      __builtin_amdgcn_sched_barrier(0);
      unsigned bad = 0;
#pragma unroll
      for (int j = 0; j < 4; ++j) q4[j] = 0.f;
#pragma unroll
      for (int r = 0; r < 4; ++r) {
        bad |= (a[2 * r].y ^ tag) | (a[2 * r].w ^ tag) | (a[2 * r + 1].y ^ tag) | (a[2 * r + 1].w ^ tag);
        q4[0] += __uint_as_float(a[2 * r].x); q4[1] += __uint_as_float(a[2 * r].z);
        q4[2] += __uint_as_float(a[2 * r + 1].x); q4[3] += __uint_as_float(a[2 * r + 1].z);
      }
      if (__all(bad == 0) || spins > SPIN_CAP) break;
      __builtin_amdgcn_s_sleep(1);
    }
    *reinterpret_cast<v2u*>(&ldu[L_Q + 2 * lane]) = v2u{pk_bf16(q4[0], q4[1]), pk_bf16(q4[2], q4[3])};
    set_flag(F_QRDY);
    __builtin_amdgcn_s_setprio(0);
    {
      wait_flag(F_KPROG, T_CAL);
      const unsigned t1 = (unsigned)__builtin_amdgcn_s_memrealtime();
      __builtin_amdgcn_raw_buffer_store_b128(v4u{t1, tag, t1, tag}, rws, lane < 8 ? WS_CP + b * 128 + lane * 16 : OOB, 0, AUX_SC1);
      wait_flag(F_KPROG, T_CAL + CP_D);
      const v2u cp = __builtin_amdgcn_raw_buffer_load_b64(rws, WS_CP + (g * 16 + c) * 128, 0, AUX_SC1);
      float dt = cp.y == tag ? (float)(int)(cp.x - t1) : (float)(CP_D * 55);
      dt += dpp<XOR1>(dt); dt += dpp<XOR2>(dt); dt += dpp<ROR4>(dt); dt += dpp<XOR8>(dt);
      if (lane == 0) ldu[L_FLAG + F_TEND] = (((t1 + K_DUR) << 4) + (unsigned)(int)dt) | 1u;
    }
    wait_flag(F_P1CNT, NSW);
    {
      const unsigned tk = (unsigned)__builtin_amdgcn_s_memrealtime();
      const float mw = lds[L_MW + (lane & 7)];
      float mx = fmaxf(mw, dpp<XOR2>(mw)); mx = fmaxf(mx, dpp<ROR4>(mx));
      float l = 0.f;
#pragma unroll 2
      for (int i = 0; i < NSW * NT / 8; ++i) {
        const f32x4 v = *reinterpret_cast<const f32x4*>(&lds[L_SC + i * 256 + lane * 4]);
        l += (__builtin_amdgcn_exp2f(v[0] - mx) + __builtin_amdgcn_exp2f(v[1] - mx)) + (__builtin_amdgcn_exp2f(v[2] - mx) + __builtin_amdgcn_exp2f(v[3] - mx));
      }
      l += dpp<XOR2>(l); l += dpp<ROR4>(l); l += dpp<XOR8>(l); l = sum_x32(sum_x16(l));
      __builtin_amdgcn_raw_buffer_store_b128(v4u{(lane & 2) ? tk : __float_as_uint(mx), tag, __float_as_uint(l), tag}, rws,
                                             lane < 8 ? WS_ML + b * 128 + lane * 16 : OOB, 0, AUX_SC1);
      wait_flag(F_VPROG, 128 - ML_T);
      v4u ml;
      for (unsigned spins = 0;; ++spins) {
        asm volatile("" ::: "memory");
        ml = __builtin_amdgcn_raw_buffer_load_b128(rws, WS_ML + (g * 16 + ((lane >> 1) & 15)) * 128 + (lane & 1) * 16 + (lane >> 5) * 32, 0, AUX_SC1);
        if (__all(((ml.y ^ tag) | (ml.w ^ tag)) == 0) || spins > SPIN_CAP) break;
        __builtin_amdgcn_s_sleep(32);
      }
      {
        float dt = (float)(int)(ml.x - tk);
        dt += dpp<XOR2>(dt); dt += dpp<ROR4>(dt); dt += dpp<XOR8>(dt); dt = sum_x16(dt);
        if (lane == 32) ldu[L_FLAG + F_TEND] = (((tk + V_DUR) << 4) + (unsigned)(int)dt) | 1u;
      }
      const float mp = __uint_as_float(ml.x);
      float M = fmaxf(mp, dpp<XOR2>(mp)); M = fmaxf(M, dpp<ROR4>(M)); M = fmaxf(M, dpp<XOR8>(M)); M = max_x16(M);
      float L = __builtin_amdgcn_exp2f(mp - M) * __uint_as_float(ml.z);
      asm volatile("" : "+v"(L));
      L += dpp<XOR2>(L); L += dpp<ROR4>(L); L += dpp<XOR8>(L); L = sum_x16(L);
      if (lane < 8) lds[L_MW + lane] = __builtin_amdgcn_exp2f(mw - M) / L;
      set_flag(F_SRDY);
    }
    if (is2) {
      wait_flag(F_VPROG, 128 - NEAR_T);
      wait_flag(F_FDONE, 3);
#pragma unroll
      for (int k = 0; k < 5; ++k) {
        const int t = k * 64 + lane;
        const f32x2 yf = *reinterpret_cast<const f32x2*>(&lds[L_YF + 2 * (t < 288 ? t : 0)]);
        __builtin_amdgcn_raw_buffer_store_b128(v4u{__float_as_uint(yf.x), tag, __float_as_uint(yf.y), tag}, rws,
                                               t < 288 ? WS_PF + ((t >> 3) * NFW + f2) * 128 + (t & 7) * 16 : OOB, 0, AUX_SC1);
      }
    }
    __builtin_amdgcn_s_setprio(3);
    wait_flag(F_ENDCNT, NSW);
    for (int i = 0; i < 3; ++i) __builtin_amdgcn_s_sleep(4);
    {
      int o2[2] = {WS_P1 + (g * 16 + s) * 2048 + lane * 16, WS_P1 + (g * 16 + s) * 2048 + 1024 + lane * 16};
      float pv4[4];
      v4u pl[3][2];
      gather<2>(rws, o2, tag, pv4, pl);
      auto h0 = __builtin_amdgcn_permlane32_swap(__float_as_uint(pv4[0] + pv4[2]), __float_as_uint(pv4[1] + pv4[3]), false, false);
      float v = sum_x16(__uint_as_float(h0[0]) + __uint_as_float(h0[1]));
      v += dpp<XOR8>(v);
      if ((lane & 24) == 0) lds[L_O + 2 * (lane & 7) + (lane >> 5)] = rnd(v);
      set_flag(F_ORDY);
      gather_end<2>(pl);
    }
    return;
  }

  const int nrole = is1 ? 4 : is2 ? 3 : 0;
  const int voff = lane * 16;
  __builtin_amdgcn_sched_barrier(0);
  __builtin_amdgcn_s_barrier();
  __builtin_amdgcn_sched_barrier(0);

  if (w >= NSW && w < NSW + nrole) {
    const int fw = w - NSW;
    v4u fr[24];
    if (is1) {
      const __amdgpu_buffer_rsrc_t rfg = mk_rsrc(wgate, DM * FF * 2);
      const __amdgpu_buffer_rsrc_t rfu = mk_rsrc(wup, DM * FF * 2);
      const int row0 = 288 * (f1 & 1) + 72 * fw + (lane >> 3);
      const int foff = row0 * (FF * 2) + (f1 >> 1) * 128 + (lane & 7) * 16;
#pragma unroll
      for (int i = 0; i < 9; ++i) {
        fr[2 * i] = __builtin_amdgcn_raw_buffer_load_b128(rfg, foff, i * 8 * FF * 2, AUX_NT);
        fr[2 * i + 1] = __builtin_amdgcn_raw_buffer_load_b128(rfu, foff, i * 8 * FF * 2, AUX_NT);
        __builtin_amdgcn_sched_barrier(0); asm volatile("s_waitcnt vmcnt(6)"); __builtin_amdgcn_sched_barrier(0);
      }
      wait_flag(F_XRDY, 1);
      tag = ldu[L_FLAG + F_TAG];
      f32x4 ag = {0.f, 0.f, 0.f, 0.f}, au = {0.f, 0.f, 0.f, 0.f};
#pragma unroll
      for (int i = 0; i < 9; ++i) {
        const v4u av = and4(ldu[L_XN + row0 + 8 * i], gm);
        ag = mfma16(av, fr[2 * i], ag); au = mfma16(av, fr[2 * i + 1], au);
      }
      ag = gemv_fin(ag, lane); au = gemv_fin(au, lane);
      if ((lane & 8) == 0) *reinterpret_cast<f32x4*>(&lds[L_YF + fw * 128 + (lane >> 5) * 64 + (lane & 7) * 8 + 4 * ((lane >> 4) & 1)]) = lane < 32 ? ag : au;
      int old = lds_inc(lds0 + (L_FLAG + F_S1CNT) * 4);
      float gs = 0.f, us = 0.f;
#pragma unroll
      for (int ww = 0; ww < 4; ++ww) { gs += lds[L_YF + ww * 128 + lane]; us += lds[L_YF + ww * 128 + 64 + lane]; }
      asm volatile("s_waitcnt lgkmcnt(0)" : "+v"(old), "+v"(gs), "+v"(us));
      if (__builtin_amdgcn_readfirstlane(old) == 3)
        __builtin_amdgcn_raw_buffer_store_b128(v4u{__float_as_uint(gs), tag, __float_as_uint(us), tag}, rws, WS_GU + (f1 * 64 + lane) * 16, 0, AUX_SC1);
    } else {
      const __amdgpu_buffer_rsrc_t rwd = mk_rsrc(wdown + (size_t)f2 * 64 * DM, 64 * DM * 2);
      const int doff = (lane >> 3) * (DM * 2) + fw * 384 + (lane & 7) * 16;
#pragma unroll
      for (int rg = 0; rg < 8; ++rg) {
#pragma unroll
        for (int k = 0; k < 3; ++k) fr[rg * 3 + k] = __builtin_amdgcn_raw_buffer_load_b128(rwd, doff + k * 128, rg * 8 * DM * 2, AUX_NT);
        __builtin_amdgcn_sched_barrier(0); asm volatile("s_waitcnt vmcnt(7)"); __builtin_amdgcn_sched_barrier(0);
      }
      wait_flag(F_QRDY, 1);
      tag = ldu[L_FLAG + F_TAG];
      for (int i = 0; i < 8; ++i) __builtin_amdgcn_s_sleep(8);
      v4u p0, p1;
      for (unsigned spins = 0;; ++spins) {
        asm volatile("" ::: "memory");
        p0 = __builtin_amdgcn_raw_buffer_load_b128(rws, WS_GU + (f2 * 128 + lane) * 16, 0, AUX_SC1);
        p1 = __builtin_amdgcn_raw_buffer_load_b128(rws, WS_GU + (f2 * 128 + lane) * 16, 1024, AUX_SC1);
        if (__all(((p0.y ^ tag) | (p0.w ^ tag) | (p1.y ^ tag) | (p1.w ^ tag)) == 0) || spins > SPIN_CAP) break;
        __builtin_amdgcn_s_sleep(16);
      }
      const float gt = rnd(__uint_as_float(p0.x) + __uint_as_float(p1.x)), up = rnd(__uint_as_float(p0.z) + __uint_as_float(p1.z));
      const float inner = 0.7978845608028654f * (gt + 0.044715f * gt * gt * gt);
      const float hv = rnd(0.5f * gt * (1.0f + tanhf(inner))) * up;
      const unsigned hp = pk_bf16(hv, hv);
      f32x4 ya[3];
#pragma unroll
      for (int k = 0; k < 3; ++k) ya[k] = f32x4{0.f, 0.f, 0.f, 0.f};
#pragma unroll
      for (int rg = 0; rg < 8; ++rg) {
        const v4u av = and4(__shfl(hp, rg * 8 + (lane >> 3)), gm);
#pragma unroll
        for (int k = 0; k < 3; ++k) ya[k] = mfma16(av, fr[rg * 3 + k], ya[k]);
      }
#pragma unroll
      for (int k = 0; k < 3; ++k) {
        ya[k] = gemv_fin(ya[k], lane);
        if (lane < 32 && (lane & 8) == 0) *reinterpret_cast<f32x4*>(&lds[L_YF + fw * 192 + k * 64 + (lane & 7) * 8 + 4 * (lane >> 4)]) = ya[k];
      }
      count_up(F_FDONE);
    }
  } else if (w < NSW) {
    const __amdgpu_buffer_rsrc_t rk = mk_rsrc(kg + (size_t)g * SK * 128, SK * 256);
    const __amdgpu_buffer_rsrc_t rv = mk_rsrc(vg + (size_t)g * SK * 128, SK * 256);
    const int ufirst = s * (NSW * NT / 2) + w;
#define TOFF(t) ((ufirst + ((t) >> 1) * NSW) << 13)
#define VOFF(t) ((t) < NT ? TOFF(t) + ((t) & 1) * 4096 : OOB)
#define tb(n, i) buf[(n) * TL + (i)]
    v4u buf[4 * TL];
#pragma unroll
    for (int i = 0; i < 2 * TL; ++i) buf[i] = __builtin_amdgcn_raw_buffer_load_b128(rk, voff + i * 1024, TOFF(0), AUX_NT);
    __builtin_amdgcn_sched_barrier(0);
    wait_flag(F_POLL, 1);
#pragma unroll
    for (int i = 0; i < 2 * TL; ++i) buf[2 * TL + i] = __builtin_amdgcn_raw_buffer_load_b128(rk, voff + i * 1024, TOFF(2), AUX_NT);
    __builtin_amdgcn_sched_barrier(0);
    wait_flag(F_QRDY, 1);
    const int hf = (c >> 2) & 1;
    const v4u qf = *reinterpret_cast<const v4u*>(&ldu[L_Q + hf * 64 + 4 * c]);
    const v4u qs = *reinterpret_cast<const v4u*>(&ldu[L_Q + (hf ^ 1) * 64 + 4 * c]);
    constexpr float LOG2E = 1.4426950408889634f, SCALE = 0.08838834764831845f;
    const bool b0 = (c & 1) != 0, b1 = (c & 2) != 0;
    float* const scw = &lds[L_SC + w * NT * 32 + (lane >> 4) * 8 + (lane & 7)];
    float m = -INFINITY;
    float y[4];
    auto qk1 = [&](const v4u* kt) {
#pragma unroll
      for (int i = 0; i < TL; ++i) {
        float xf = 0.f, xs = 0.f;
#pragma unroll
        for (int j = 0; j < 4; ++j) { xf = dot2(kt[i][j], qf[j], xf); xs = dot2(kt[i][j], qs[j], xs); }
        y[i] = xf + dpp<XOR7>(xs);
      }
      asm volatile("" : "+v"(y[0]), "+v"(y[1]), "+v"(y[2]), "+v"(y[3]));
      __builtin_amdgcn_sched_barrier(0);
    };
    auto qk2 = [&](int t) {
      const float u0 = (b0 ? y[1] : y[0]) + dpp<XOR1>(b0 ? y[0] : y[1]);
      const float u1 = (b0 ? y[3] : y[2]) + dpp<XOR1>(b0 ? y[2] : y[3]);
      float sc = (b1 ? u1 : u0) + dpp<XOR2>(b1 ? u0 : u1);
      sc += dpp<XOR8>(sc);
      const float tl2 = rnd(rnd(sc) * SCALE) * LOG2E;
      m = fmaxf(m, tl2);
      scw[t * 32] = tl2;
    };
#pragma unroll
    for (int n = 0; n < 2; ++n) {
      qk1(&tb(n, 0));
#pragma unroll
      for (int i = 0; i < TL; ++i) tb(n, i) = __builtin_amdgcn_raw_buffer_load_b128(rk, voff + i * 1024, TOFF(4) + n * 4096, AUX_NT);
      __builtin_amdgcn_sched_barrier(0);
      qk2(n);
    }
    qk1(&tb(2, 0)); qk2(2);
    qk1(&tb(3, 0)); qk2(3);
    unsigned next = (unsigned)__builtin_amdgcn_s_memrealtime() << 4, tau = TAU0;
    unsigned tend = 0, tbase = 0, vshift = 0;
    auto aim = [&](int left) {
      const unsigned te = __builtin_amdgcn_readfirstlane(*(volatile LAS unsigned*)&ldu[L_FLAG + F_TEND]);
      if (te == 0) return;
      if (te != tbase) { tend += te - tbase - vshift; tbase = te; vshift = 0; }
      const int v = __builtin_amdgcn_readfirstlane((int)((float)(int)(tend - next) * __builtin_amdgcn_rcpf((float)left)));
      tau = (unsigned)min(TAU_HI, max(TAU_LO, v));
    };
#pragma unroll 1
    for (int t = 4; t < NT; t += 2) {
      const bool lastk = t + 2 >= NT;
      const __amdgpu_buffer_rsrc_t rn = lastk ? rv : rk;
      const int nxt = TOFF(lastk ? 0 : t + 2);
      if (w == 0 && lane == 0) *(volatile LAS int*)&ldu[L_FLAG + F_KPROG] = t;
      if (t == T_CAL) tend = tbase;
      if ((t & 7) == (T_CAL & 7)) aim(2 * NT - t);
#pragma unroll
      for (int n = 0; n < 2; ++n) {
        qk1(&tb(n, 0));
        pace(next, tau, tend);
#pragma unroll
        for (int i = 0; i < TL; ++i) tb(n, i) = __builtin_amdgcn_raw_buffer_load_b128(rn, voff + i * 1024, nxt + n * 4096, AUX_NT);
        __builtin_amdgcn_sched_barrier(0);
        qk2(t + n);
      }
    }
    m = fmaxf(m, dpp<XOR1>(m)); m = fmaxf(m, dpp<XOR2>(m));
    m = max_x32(max_x16(m));
    if ((lane & 59) == 0) lds[L_MW + 2 * w + (lane >> 2)] = m;
    count_up(F_P1CNT);
    f32x4 acc = {0.f, 0.f, 0.f, 0.f};
    unsigned amask[4];
    {
      const int jp = (c & 3) + 4 * (c >> 3);
#pragma unroll
      for (int r = 0; r < 4; ++r) amask[r] = (jp >> 1) == r ? ((jp & 1) ? 0xffff0000u : 0x0000ffffu) : 0u;
    }
    auto pv = [&](float sv, const v4u* vt) {
      const float p = __builtin_amdgcn_exp2f(sv - m);
      const unsigned pp = pk_bf16(p, p);
#pragma unroll
      for (int i = 0; i < TL; ++i) {
        const unsigned pb = i == 0 ? dppu<0x00>(pp) : i == 1 ? dppu<0x55>(pp) : i == 2 ? dppu<0xAA>(pp) : dppu<0xFF>(pp);
        acc = mfma16(and4(pb, amask), vt[i], acc);
      }
      asm volatile("" : "+v"(acc));
      __builtin_amdgcn_sched_barrier(0);
    };
    int t = 0;
    vshift = tend - tbase;
    auto vrun = [&](int lim) {
#pragma unroll 1
      for (; t < lim; t += 2) {
        if ((t & 7) == (T_CAL & 7)) aim(NT - t);
        const int nxt = TOFF(t + 2);
        const float s0 = scw[t * 32], s1 = scw[t * 32 + 32];
#pragma unroll
        for (int n = 0; n < 2; ++n) {
          pv(n ? s1 : s0, &tb(n, 0));
          pace(next, tau, tend);
#pragma unroll
          for (int i = 0; i < TL; ++i) tb(n, i) = __builtin_amdgcn_raw_buffer_load_b128(rv, voff + i * 1024, nxt + n * 4096, AUX_NT);
          __builtin_amdgcn_sched_barrier(0);
        }
        if (w == 0 && lane == 0) *(volatile LAS int*)&ldu[L_FLAG + F_VPROG] = t + 2 + 128 - NT;
      }
    };
    vrun(NT - X_MAX);
    {
      const int late = min(1 << 16, max(0, (int)(next + X_MAX * TAU_N - tend)));
      vrun(NT - min(X_MAX, X_MIN + 2 * ((late * X_C) >> 15)));
    }
#pragma unroll
    for (int k = 2; k < 4; ++k) {
#pragma unroll
      for (int i = 0; i < TL; ++i) tb(k, i) = __builtin_amdgcn_raw_buffer_load_b128(rv, voff + i * 1024, TOFF(t + 2) + (k & 1) * 4096, AUX_NT);
      __builtin_amdgcn_sched_barrier(0);
    }
    __builtin_amdgcn_s_setprio(2);
    wait_flag(F_SRDY, 1);
    const float ed = lds[L_MW + 2 * w + ((lane >> 4) & 1)];
    tag = ldu[L_FLAG + F_TAG];
    const int po = WS_P1 + ((g * 16 + (lane >> 3)) * 16 + s) * 128 + (lane & 7) * 16;
#pragma unroll 1
    for (; t + 4 < NT; t += 4) {
      if (w == 0 && lane == 0) *(volatile LAS int*)&ldu[L_FLAG + F_VPROG] = t + 2 + 128 - NT;
#pragma unroll
      for (int k = 0; k < 4; ++k) {
        const int o = VOFF(t + k + 4);
        pv(scw[(t + k) * 32], &tb(k, 0));
#pragma unroll
        for (int i = 0; i < TL; ++i) tb(k, i) = __builtin_amdgcn_raw_buffer_load_b128(rv, voff + i * 1024, o, AUX_NT);
        __builtin_amdgcn_sched_barrier(0);
      }
    }
    {
      if (w == 0 && lane == 0) *(volatile LAS int*)&ldu[L_FLAG + F_VPROG] = 126;
      v4u av[4][TL];
#pragma unroll
      for (int k = 0; k < 4; ++k) {
        const float p = __builtin_amdgcn_exp2f((t + k < NT ? scw[(t + k) * 32] : -INFINITY) - m);
        const unsigned pp = pk_bf16(p, p);
        av[k][0] = and4(dppu<0x00>(pp), amask); av[k][1] = and4(dppu<0x55>(pp), amask);
        av[k][2] = and4(dppu<0xAA>(pp), amask); av[k][3] = and4(dppu<0xFF>(pp), amask);
        asm volatile("" : "+v"(av[k][0]), "+v"(av[k][1]), "+v"(av[k][2]), "+v"(av[k][3]));
      }
      __builtin_amdgcn_sched_barrier(0);
#pragma unroll
      for (int k = 0; k < 2; ++k)
#pragma unroll
        for (int i = 0; i < TL; ++i) acc = mfma16(av[k][i], tb(k, i), acc);
      if (t + 2 < NT) {
#pragma unroll
        for (int k = 2; k < 4; ++k)
#pragma unroll
          for (int i = 0; i < TL; ++i) acc = mfma16(av[k][i], tb(k, i), acc);
      }
    }
    *reinterpret_cast<f32x4*>(&lds[L_ACC + w * 256 + ((lane >> 4) & 1) * 128 + 8 * c + 4 * (lane >> 5)]) = acc * ed;
    int old = lds_inc(lds0 + (L_FLAG + F_ENDCNT) * 4);
    f32x2 o0 = {0.f, 0.f}, o1 = {0.f, 0.f};
#pragma unroll
    for (int ww = 0; ww < NSW; ++ww) { o0 += *reinterpret_cast<const f32x2*>(&lds[L_ACC + ww * 256 + 2 * lane]); o1 += *reinterpret_cast<const f32x2*>(&lds[L_ACC + ww * 256 + 128 + 2 * lane]); }
    asm volatile("s_waitcnt lgkmcnt(0)" : "+v"(old), "+v"(o0), "+v"(o1));
    if (__builtin_amdgcn_readfirstlane(old) == NSW - 1) {
      __builtin_amdgcn_raw_buffer_store_b128(v4u{__float_as_uint(o0[0]), tag, __float_as_uint(o0[1]), tag}, rws, po, 0, AUX_SC1);
      __builtin_amdgcn_raw_buffer_store_b128(v4u{__float_as_uint(o1[0]), tag, __float_as_uint(o1[1]), tag}, rws, po, 8 * 2048, AUX_SC1);
    }
  }

  if (w >= NSW) {
    const int t = (w - NSW) * 64 + lane;
    const __amdgpu_buffer_rsrc_t rwo = mk_rsrc(wo + (size_t)b * 16 * DM, 16 * DM * 2);
    if (w == NW - 1) {
      const unsigned t0 = __builtin_amdgcn_raw_buffer_load_b32(mk_rsrc(vg + (size_t)g * SK * 128, SK * 256), lane < 2 ? (s << 20) + lane * 0xffffc : OOB, 0, AUX_NT);
      const unsigned t1 = __builtin_amdgcn_raw_buffer_load_b32(rwo, lane == 0 ? 0 : OOB, 0, AUX_NT);
      const unsigned t2 = __builtin_amdgcn_raw_buffer_load_b32(mk_rsrc(out, DM * 2), lane == 0 && b < NRED ? 0 : OOB, 0, AUX_NT);
      const unsigned t3 = __builtin_amdgcn_raw_buffer_load_b32(rws, lane < 3 ? WS_P1 + lane * 0x100000 + 64 : OOB, 0, AUX_NT);
      asm volatile("" :: "v"(t0), "v"(t1), "v"(t2), "v"(t3));
    }
#pragma unroll 1
    for (unsigned spins = 0; spins < (SPIN_CAP << 5); ++spins) {
      if (__builtin_amdgcn_readfirstlane(*(volatile LAS unsigned*)&ldu[L_FLAG + F_ENDCNT]) >= 1u) break;
      __builtin_amdgcn_s_sleep(2);
    }
    float wf[32];
    {
      unsigned wod[16];
#pragma unroll
      for (int j = 0; j < 16; ++j) wod[j] = __builtin_amdgcn_raw_buffer_load_b32(rwo, t < 288 ? t * 4 : OOB, j * DM * 2, AUX_NT);
#pragma unroll
      for (int j = 0; j < 16; ++j) { wf[2 * j] = lo_f(wod[j]); wf[2 * j + 1] = hi_f(wod[j]); }
#pragma unroll
      for (int j = 0; j < 32; ++j) asm volatile("" : "+v"(wf[j]));
    }
    tag = ldu[L_FLAG + F_TAG];
    const int po = t < 288 ? WS_P2 + ((t >> 3) * NWG + b) * 128 + (t & 7) * 16 : OOB;
    f32x4 o4[4];
#pragma unroll 1
    for (unsigned spins = 0; spins < (SPIN_CAP << 8); ++spins) {
      unsigned f = *(volatile LAS unsigned*)&ldu[L_FLAG + F_ORDY];
      asm volatile("" ::: "memory");
#pragma unroll
      for (int k = 0; k < 4; ++k) o4[k] = *reinterpret_cast<const f32x4*>(&lds[L_O + 4 * k]);
      asm volatile("" : "+v"(o4[0]), "+v"(o4[1]), "+v"(o4[2]), "+v"(o4[3]) :: "memory");
      if (__builtin_amdgcn_readfirstlane(f) != 0u) break;
    }
    f32x2 ya = {0.f, 0.f}, yb = {0.f, 0.f};
#pragma unroll
    for (int j = 0; j < 16; j += 2) {
      ya += o4[j >> 2][j & 3] * f32x2{wf[2 * j], wf[2 * j + 1]};
      yb += o4[j >> 2][(j & 3) + 1] * f32x2{wf[2 * j + 2], wf[2 * j + 3]};
    }
    __builtin_amdgcn_raw_buffer_store_b128(v4u{__float_as_uint(ya.x + yb.x), tag, __float_as_uint(ya.y + yb.y), tag}, rws, po, 0, AUX_SC1);
  }

  if (b < NRED && w < 8) {
    if (w == 0) {
      const unsigned xp = __builtin_amdgcn_raw_buffer_load_b32(rx, lane < 8 ? b * 32 + lane * 4 : OOB, 0, 0);
      float f0, f1v;
      for (unsigned spins = 0;; ++spins) {
        asm volatile("" ::: "memory");
        v4u pf[9];
#pragma unroll
        for (int k = 0; k < 9; ++k) pf[k] = __builtin_amdgcn_raw_buffer_load_b128(rws, WS_PF + b * NFW * 128 + lane * 16, k * 1024, AUX_SC1);
        unsigned bad = 0; f0 = 0.f; f1v = 0.f;
#pragma unroll
        for (int k = 0; k < 9; ++k) { bad |= (pf[k].y ^ tag) | (pf[k].w ^ tag); f0 += __uint_as_float(pf[k].x); f1v += __uint_as_float(pf[k].z); }
        if (__all(bad == 0) || spins > SPIN_CAP) break;
        __builtin_amdgcn_s_sleep(8);
      }
      f0 += dpp<XOR8>(f0); f0 = rnd(sum_x32(sum_x16(f0)));
      f1v += dpp<XOR8>(f1v); f1v = rnd(sum_x32(sum_x16(f1v)));
      if (lane < 8) {
        *reinterpret_cast<f32x2*>(&lds[L_G + lane * 2]) = f32x2{rnd(f0 / (1.0f + __expf(-f0))), rnd(f1v / (1.0f + __expf(-f1v)))};
        ldu[L_G + 16 + lane] = xp;
      }
    }
    if (w < NSW) wait_flag(F_ORDY, 1);
    for (int i = 0; i < 4; ++i) __builtin_amdgcn_s_sleep(4);
    int o4[4];
    float sv[8];
#pragma unroll
    for (int k = 0; k < 4; ++k) o4[k] = WS_P2 + b * NWG * 128 + (w * 64 + lane) * 16 + k * 8192;
    v4u pl[3][4];
    gather<4>(rws, o4, tag, sv, pl);
    float s0 = (sv[0] + sv[2]) + (sv[4] + sv[6]), s1 = (sv[1] + sv[3]) + (sv[5] + sv[7]);
    s0 += dpp<XOR8>(s0); s0 = sum_x32(sum_x16(s0));
    s1 += dpp<XOR8>(s1); s1 = sum_x32(sum_x16(s1));
    if (lane < 8) *reinterpret_cast<f32x2*>(&lds[L_FIN + w * 16 + lane * 2]) = f32x2{s0, s1};
    int old = lds_inc(lds0 + (L_FLAG + F_FINCNT) * 4);
    f32x2 t2 = *reinterpret_cast<const f32x2*>(&lds[L_FIN + lane * 2]);
    f32x2 gv = *reinterpret_cast<const f32x2*>(&lds[L_G + (lane & 7) * 2]);
    unsigned xp = ldu[L_G + 16 + (lane & 7)];
    asm volatile("s_waitcnt lgkmcnt(0)" : "+v"(old), "+v"(t2), "+v"(gv), "+v"(xp));
    if (__builtin_amdgcn_readfirstlane(old) == 7) {
      t2.x += dpp<XOR8>(t2.x); t2.x = sum_x32(sum_x16(t2.x));
      t2.y += dpp<XOR8>(t2.y); t2.y = sum_x32(sum_x16(t2.y));
      const unsigned r = pk_bf16(lo_f(xp) + rnd(rnd(t2.x) * gv.x), hi_f(xp) + rnd(rnd(t2.y) * gv.y));
      __builtin_amdgcn_raw_buffer_store_b32(r, mk_rsrc(out, DM * 2), lane < 8 ? b * 32 + lane * 4 : OOB, 0, 0);
      if (b == 0 && lane == 0) __builtin_amdgcn_raw_buffer_store_b32(tag, rws, WS_EPOCH, 0, AUX_SC1);
    }
    gather_end<4>(pl);
  }
}

}

extern "C" void kernel_launch(void* const* d_in, const int* in_sizes, int n_in, void* d_out, int out_size, void* d_ws, size_t ws_size,
                              hipStream_t stream) {
  (void)in_sizes; (void)n_in; (void)out_size; (void)ws_size;
  fused_decode<<<NWG, NTHR, 0, stream>>>((const unsigned short*)d_in[0], (unsigned char*)d_ws, (const unsigned short*)d_in[3],
                                         (const unsigned short*)d_in[1], (const unsigned short*)d_in[6], (const unsigned short*)d_in[7],
                                         (const unsigned short*)d_in[8], (const unsigned short*)d_in[2], (const unsigned short*)d_in[4],
                                         (unsigned short*)d_out);
}
```

```cpp
#include <hip/hip_runtime.h>
#include <hip/hip_bf16.h>
#include <stdint.h>

#define WSPOS 64
namespace {

typedef unsigned v4u __attribute__((ext_vector_type(4)));
typedef unsigned v2u __attribute__((ext_vector_type(2)));
typedef float f32x4 __attribute__((ext_vector_type(4)));
typedef float f32x2 __attribute__((ext_vector_type(2)));
typedef __bf16 bf16x8 __attribute__((ext_vector_type(8)));
typedef __bf16 bf16x2 __attribute__((ext_vector_type(2)));
#define LAS __attribute__((address_space(3)))

constexpr int DM = 576, SK = 65536, FF = 4608, QD = 4096;
constexpr int NWG = 256;
constexpr int NSW = 4;
constexpr int NW = 9;
constexpr int NTHR = (NW + 1) * 64;
constexpr int NRED = 36;
constexpr int NFW = 72;
constexpr int TL = 4;
constexpr int NT = 64;
constexpr int OOB = 0x70000000;

constexpr int TAU0 = 884, TAU_LO = 850, TAU_HI = 907, TAU_N = 865;
constexpr int BURST = 20, STALL = 100;
constexpr int T_CAL = 12, CP_D = 6;
constexpr int K_DUR = 6430, V_DUR = 3535;
constexpr int ML_T = 54;
constexpr int X_MIN = 4, X_MAX = 20, X_C = 68;
constexpr int NEAR_T = 4;

constexpr int WS_EPOCH = 0;
constexpr int WS_QG = 4096;
constexpr int WS_ML = WS_QG + 16 * 4 * 256 * 8;
constexpr int WS_CP = WS_ML + NWG * 128;
constexpr int WS_P1 = WS_CP + NWG * 128;
constexpr int WS_P2 = WS_P1 + 16 * 16 * 16 * 128;
constexpr int WS_PF = WS_P2 + NRED * NWG * 128;
constexpr int WS_GU = WS_PF + NRED * NFW * 128;
constexpr int WS_END = WS_GU + NFW * 2 * 64 * 16;

constexpr int AUX_NT = 2, AUX_SC1 = 16;
constexpr int RSRC_W3 = 0x00020000;
constexpr unsigned SPIN_CAP = 1u << 14;

constexpr int L_FLAG = 0;
enum { F_TAG, F_QRDY, F_XRDY, F_FDONE, F_POLL, F_P1CNT, F_SRDY, F_KPROG, F_VPROG, F_ORDY, F_ENDCNT, F_FINCNT, F_S1CNT, F_TEND };
constexpr int L_MW = 16;
constexpr int L_O = L_MW + 16;
constexpr int L_G = L_O + 16;
constexpr int L_Q = L_G + 32;
constexpr int L_XN = L_Q + 128;
constexpr int L_YF = L_XN + 640;
constexpr int L_FIN = L_YF + 576;
constexpr int L_ACC = L_FIN + 128;
constexpr int L_SC = L_ACC + NSW * 256;
constexpr int L_FLOATS = 24 * 1024;
static_assert(L_SC % 4 == 0 && L_ACC % 4 == 0 && L_Q % 4 == 0 && L_O % 4 == 0 && L_SC + NSW * NT * 32 <= L_FLOATS, "LDS map");
static_assert(16 * NSW * NT * 16 == SK && X_MIN >= 4, "key split");

template <int CTRL> __device__ __forceinline__ float dpp(float x) {
  return __builtin_bit_cast(float, __builtin_amdgcn_mov_dpp(__builtin_bit_cast(int, x), CTRL, 0xf, 0xf, true));
}
template <int CTRL> __device__ __forceinline__ unsigned dppu(unsigned x) {
  return (unsigned)__builtin_amdgcn_mov_dpp((int)x, CTRL, 0xf, 0xf, true);
}
constexpr int XOR1 = 0xB1, XOR2 = 0x4E, XOR7 = 0x141, XOR8 = 0x128, ROR4 = 0x124;
__device__ __forceinline__ float sum_x16(float x) {
  auto s = __builtin_amdgcn_permlane16_swap(__float_as_uint(x), __float_as_uint(x), false, false);
  return __uint_as_float(s[0]) + __uint_as_float(s[1]);
}
__device__ __forceinline__ float sum_x32(float x) {
  auto s = __builtin_amdgcn_permlane32_swap(__float_as_uint(x), __float_as_uint(x), false, false);
  return __uint_as_float(s[0]) + __uint_as_float(s[1]);
}
__device__ __forceinline__ float max_x16(float x) {
  auto s = __builtin_amdgcn_permlane16_swap(__float_as_uint(x), __float_as_uint(x), false, false);
  return fmaxf(__uint_as_float(s[0]), __uint_as_float(s[1]));
}
__device__ __forceinline__ float max_x32(float x) {
  auto s = __builtin_amdgcn_permlane32_swap(__float_as_uint(x), __float_as_uint(x), false, false);
  return fmaxf(__uint_as_float(s[0]), __uint_as_float(s[1]));
}
__device__ __forceinline__ float wave_sum(float x) {
  x += dpp<XOR1>(x); x += dpp<XOR2>(x); x += dpp<ROR4>(x); x += dpp<XOR8>(x);
  return sum_x32(sum_x16(x));
}
__device__ __forceinline__ unsigned pk_bf16(float lo, float hi) {
  bf16x2 v = {(__bf16)lo, (__bf16)hi};
  return __builtin_bit_cast(unsigned, v);
}
__device__ __forceinline__ float rnd(float x) { return (float)(__bf16)x; }
__device__ __forceinline__ float lo_f(unsigned u) { return __uint_as_float(u << 16); }
__device__ __forceinline__ float hi_f(unsigned u) { return __uint_as_float(u & 0xffff0000u); }
__device__ __forceinline__ float dot2(unsigned a, unsigned b, float c) {
  return __builtin_amdgcn_fdot2_f32_bf16(__builtin_bit_cast(bf16x2, a), __builtin_bit_cast(bf16x2, b), c, false);
}
__device__ __forceinline__ __amdgpu_buffer_rsrc_t mk_rsrc(const void* p, int bytes) {
  return __builtin_amdgcn_make_buffer_rsrc((void*)p, 0, bytes, RSRC_W3);
}
__device__ __forceinline__ v4u and4(unsigned x, const unsigned (&m)[4]) { return v4u{x & m[0], x & m[1], x & m[2], x & m[3]}; }
__device__ __forceinline__ f32x4 mfma16(v4u a, v4u bm, f32x4 cacc) {
  return __builtin_amdgcn_mfma_f32_16x16x32_bf16(__builtin_bit_cast(bf16x8, a), __builtin_bit_cast(bf16x8, bm), cacc, 0, 0, 0);
}
__device__ __forceinline__ f32x4 gemv_fin(f32x4 d, int lane) {
  const bool valid = (lane >> 5) == ((lane >> 3) & 1);
#pragma unroll
  for (int r = 0; r < 4; ++r) { float t = valid ? d[r] : 0.f; t += dpp<XOR8>(t); d[r] = sum_x32(t); }
  return d;
}
__device__ __forceinline__ int lds_inc(unsigned addr) {
  int r; unsigned long long save;
  asm volatile("s_mov_b64 %1, exec\n\ts_mov_b64 exec, 1\n\tds_add_rtn_u32 %0, %2, %3\n\ts_mov_b64 exec, %1"
               : "=&v"(r), "=&s"(save) : "v"(addr), "v"(1) : "memory");
  return r;
}
template <int N>
__device__ __forceinline__ void gather(__amdgpu_buffer_rsrc_t rs, int (&off)[N], unsigned tag, float (&val)[2 * N], v4u (&p)[3][N]) {
  auto issue = [&](int j) {
    asm volatile("" ::: "memory");
#pragma unroll
    for (int k = 0; k < N; ++k) p[j][k] = __builtin_amdgcn_raw_buffer_load_b128(rs, off[k], 0, AUX_SC1);
    __builtin_amdgcn_sched_barrier(0);
  };
  auto check = [&](int j) {
    bool done = true;
#pragma unroll
    for (int k = 0; k < N; ++k) {
      const bool ok = off[k] != OOB && ((p[j][k].y ^ tag) | (p[j][k].w ^ tag)) == 0;
      val[2 * k] = ok ? __uint_as_float(p[j][k].x) : val[2 * k]; val[2 * k + 1] = ok ? __uint_as_float(p[j][k].z) : val[2 * k + 1];
      off[k] = ok ? OOB : off[k];
      done = done && off[k] == OOB;
    }
    return __all(done) != 0;
  };
#pragma unroll
  for (int k = 0; k < 2 * N; ++k) val[k] = 0.f;
  issue(0); __builtin_amdgcn_s_sleep(5); issue(1); __builtin_amdgcn_s_sleep(5);
#pragma unroll 1
  for (unsigned spins = 0; spins < SPIN_CAP; ++spins) {
    issue(2); if (check(0)) break;
    issue(0); if (check(1)) break;
    issue(1); if (check(2)) break;
  }
}
template <int N>
__device__ __forceinline__ void gather_end(v4u (&p)[3][N]) {
#pragma unroll
  for (int j = 0; j < 3; ++j)
#pragma unroll
    for (int k = 0; k < N; ++k) asm volatile("" :: "v"(p[j][k]));
}
__device__ __forceinline__ void pace(unsigned& next, unsigned tau, unsigned& tend) {
  int d;
  asm volatile("1:\n\ts_memrealtime vcc\n\ts_waitcnt lgkmcnt(0)\n\ts_lshl_b32 %0, vcc_lo, 4\n\ts_sub_i32 %0, %0, %1\n\ts_cmp_ge_i32 %0, 0\n\ts_cbranch_scc1 2f\n\ts_sleep 1\n\ts_branch 1b\n2:"
               : "=&s"(d) : "s"(next) : "scc", "vcc");
  next += tau + (unsigned)max(0, d - BURST * 16);
  tend += d > STALL * 16 ? d : 0;
}

__global__ void __launch_bounds__(NTHR, 3)
fused_decode(const unsigned short* __restrict__ xg, unsigned char* __restrict__ ws, const unsigned short* __restrict__ wq,
             const unsigned short* __restrict__ kg, const unsigned short* __restrict__ wgate, const unsigned short* __restrict__ wup,
             const unsigned short* __restrict__ wdown, const unsigned short* __restrict__ vg, const unsigned short* __restrict__ wo,
             unsigned short* __restrict__ out) {
  __shared__ __attribute__((aligned(16))) float lds[L_FLOATS];
  asm volatile("" :: "s"(xg), "s"(ws), "s"(wq), "s"(kg), "s"(wgate), "s"(wup), "s"(wdown));
  const int lane = threadIdx.x & 63, pw = __builtin_amdgcn_readfirstlane(threadIdx.x >> 6);
  const int w = pw == 0 ? NW : pw - 1;
  const int b = blockIdx.x, g = b >> 4, s = b & 15;
  const int c = lane & 15;
  const int sr = (s + 3 * g) & 15, ns2 = 5 - (g & 1);
  const bool is1 = sr < 9, is2 = !is1 && sr < 9 + ns2;
  const int f1 = g * 9 + sr, f2 = (g >> 1) * 9 + (g & 1) * 5 + sr - 9;
  unsigned* const ldu = reinterpret_cast<unsigned*>(lds);
  const unsigned lds0 = (unsigned)(uintptr_t)(LAS float*)lds;

  const __amdgpu_buffer_rsrc_t rws = mk_rsrc(ws, WS_END);
  const __amdgpu_buffer_rsrc_t rx = mk_rsrc(xg, DM * 2);

  auto wait_flag = [&](int idx, unsigned want) {
#pragma unroll 1
    for (unsigned spins = 0; spins < (SPIN_CAP << 6); ++spins) {
      if (__builtin_amdgcn_readfirstlane(*(volatile LAS unsigned*)&ldu[L_FLAG + idx]) >= want) break;
      __builtin_amdgcn_s_sleep(1);
    }
    asm volatile("" ::: "memory");
  };
  auto set_flag = [&](int idx) {
    asm volatile("" ::: "memory");
    if (lane == 0) *(volatile LAS unsigned*)&ldu[L_FLAG + idx] = 1u;
  };
  auto count_up = [&](int idx) {
    int r = lds_inc(lds0 + (L_FLAG + idx) * 4);
    asm volatile("s_waitcnt lgkmcnt(0)" : "+v"(r));
  };
  unsigned gm[4];
#pragma unroll
  for (int r = 0; r < 4; ++r) gm[r] = ((lane & 7) >> 1) == r ? ((lane & 1) ? 0xffff0000u : 0x0000ffffu) : 0u;
  unsigned tag;

  if (__builtin_expect(w == NW, 1)) {
    __builtin_amdgcn_s_setprio(3);
    unsigned xd[5], xq[3];
#pragma unroll
    for (int k = 0; k < 5; ++k) xd[k] = __builtin_amdgcn_raw_buffer_load_b32(rx, lane * 4 + k * 256, 0, 0);
#pragma unroll
    for (int k = 0; k < 3; ++k) xq[k] = __builtin_amdgcn_raw_buffer_load_b16(rx, (144 * (s >> 2) + lane + 64 * k) * 2, 0, 0);
    const unsigned epoch = __builtin_amdgcn_raw_buffer_load_b32(rws, lane == 0 ? WS_EPOCH : OOB, 0, AUX_SC1);
    v4u wqv[18];
    {
      const __amdgpu_buffer_rsrc_t rq = mk_rsrc(wq, DM * QD * 2);
      const int o = (144 * (s >> 2) + (lane >> 3)) * (QD * 2) + (g * 4 + (s & 3)) * 128 + (lane & 7) * 16;
#pragma unroll
      for (int i = 0; i < 18; ++i) wqv[i] = __builtin_amdgcn_raw_buffer_load_b128(rq, o, i * 8 * QD * 2, AUX_NT);
    }
    if (lane < 16) ldu[L_FLAG + lane] = 0u;
    asm volatile("s_waitcnt lgkmcnt(0)" ::: "memory");
    __builtin_amdgcn_sched_barrier(0);
    __builtin_amdgcn_s_barrier();
    __builtin_amdgcn_sched_barrier(0);
    float mb, den;
    {
      float xv[10];
#pragma unroll
      for (int k = 0; k < 5; ++k) { xv[2 * k] = lo_f(xd[k]); xv[2 * k + 1] = hi_f(xd[k]); }
      float sm = 0.f;
#pragma unroll
      for (int k = 0; k < 10; ++k) sm += xv[k];
      const float mean = wave_sum(sm) * (1.0f / DM);
      float sq = 0.f;
#pragma unroll
      for (int k = 0; k < 8; ++k) { const float d = xv[k] - mean; sq = fmaf(d, d, sq); }
      const float d8 = lane < 32 ? xv[8] - mean : 0.f, d9 = lane < 32 ? xv[9] - mean : 0.f;
      sq = fmaf(d8, d8, sq); sq = fmaf(d9, d9, sq);
      const float var = wave_sum(sq) * (1.0f / DM);
      mb = rnd(mean);
      den = rnd(sqrtf(rnd(rnd(var) + 1e-5f)));
    }
    unsigned xn[3];
#pragma unroll
    for (int k = 0; k < 3; ++k) { const float v = rnd(lo_f(xq[k]) - mb) / den; xn[k] = pk_bf16(v, v); }
    unsigned xr[18];
#pragma unroll
    for (int i = 0; i < 18; ++i) xr[i] = __shfl(xn[i >> 3], (8 * i + (lane >> 3)) & 63);
#pragma unroll
    for (int i = 0; i < 18; ++i) asm volatile("" : "+v"(xr[i]));
    __builtin_amdgcn_sched_barrier(0);
    f32x4 qa = {0.f, 0.f, 0.f, 0.f};
#pragma unroll
    for (int i = 0; i < 18; ++i) { qa = mfma16(and4(xr[i], gm), wqv[i], qa); __builtin_amdgcn_sched_barrier(0); }
    qa = gemv_fin(qa, lane);
    tag = __builtin_amdgcn_readfirstlane(epoch) + 1u;
    {
      const bool up2 = (lane & 8) != 0;
      const int col = (lane & 7) * 8 + 4 * ((lane >> 4) & 1) + (up2 ? 2 : 0);
      __builtin_amdgcn_raw_buffer_store_b128(v4u{__float_as_uint(up2 ? qa[2] : qa[0]), tag, __float_as_uint(up2 ? qa[3] : qa[1]), tag}, rws,
                                             lane < 32 ? WS_QG + ((g * 4 + (s >> 2)) * 256 + (s & 3) * 64 + col) * 8 : OOB, 0, AUX_SC1);
    }
    if (lane == 0) ldu[L_FLAG + F_TAG] = tag;
    if (is1) {
#pragma unroll
      for (int k = 0; k < 5; ++k) {
        const float v0 = rnd(lo_f(xd[k]) - mb) / den, v1 = rnd(hi_f(xd[k]) - mb) / den;
        *reinterpret_cast<v2u*>(&ldu[L_XN + 2 * (lane + 64 * k)]) = v2u{pk_bf16(v0, v0), pk_bf16(v1, v1)};
      }
      set_flag(F_XRDY);
    }
    for (int i = 0; i < 5; ++i) __builtin_amdgcn_s_sleep(8);
    float q4[4];
    for (unsigned spins = 0;; ++spins) {
      asm volatile("" ::: "memory");
      v4u a[8];
#pragma unroll
      for (int r = 0; r < 4; ++r) {
        a[2 * r] = __builtin_amdgcn_raw_buffer_load_b128(rws, WS_QG + g * 8192 + lane * 32, r * 2048, AUX_SC1);
        a[2 * r + 1] = __builtin_amdgcn_raw_buffer_load_b128(rws, WS_QG + g * 8192 + lane * 32 + 16, r * 2048, AUX_SC1);
      }
      __builtin_amdgcn_sched_barrier(0);
      set_flag(F_POLL);
      __builtin_amdgcn_sched_barrier(0);
      unsigned bad = 0;
#pragma unroll
      for (int j = 0; j < 4; ++j) q4[j] = 0.f;
#pragma unroll
      for (int r = 0; r < 4; ++r) {
        bad |= (a[2 * r].y ^ tag) | (a[2 * r].w ^ tag) | (a[2 * r + 1].y ^ tag) | (a[2 * r + 1].w ^ tag);
        q4[0] += __uint_as_float(a[2 * r].x); q4[1] += __uint_as_float(a[2 * r].z);
        q4[2] += __uint_as_float(a[2 * r + 1].x); q4[3] += __uint_as_float(a[2 * r + 1].z);
      }
      if (__all(bad == 0) || spins > SPIN_CAP) break;
      __builtin_amdgcn_s_sleep(1);
    }
    *reinterpret_cast<v2u*>(&ldu[L_Q + 2 * lane]) = v2u{pk_bf16(q4[0], q4[1]), pk_bf16(q4[2], q4[3])};
    set_flag(F_QRDY);
    __builtin_amdgcn_s_setprio(0);
    {
      wait_flag(F_KPROG, T_CAL);
      const unsigned t1 = (unsigned)__builtin_amdgcn_s_memrealtime();
      __builtin_amdgcn_raw_buffer_store_b128(v4u{t1, tag, t1, tag}, rws, lane < 8 ? WS_CP + b * 128 + lane * 16 : OOB, 0, AUX_SC1);
      wait_flag(F_KPROG, T_CAL + CP_D);
      const v2u cp = __builtin_amdgcn_raw_buffer_load_b64(rws, WS_CP + (g * 16 + c) * 128, 0, AUX_SC1);
      float dt = cp.y == tag ? (float)(int)(cp.x - t1) : (float)(CP_D * 55);
      dt += dpp<XOR1>(dt); dt += dpp<XOR2>(dt); dt += dpp<ROR4>(dt); dt += dpp<XOR8>(dt);
      if (lane == 0) ldu[L_FLAG + F_TEND] = (((t1 + K_DUR) << 4) + (unsigned)(int)dt) | 1u;
    }
    wait_flag(F_P1CNT, NSW);
    {
      const unsigned tk = (unsigned)__builtin_amdgcn_s_memrealtime();
      const float mw = lds[L_MW + (lane & 7)];
      float mx = fmaxf(mw, dpp<XOR2>(mw)); mx = fmaxf(mx, dpp<ROR4>(mx));
      float l = 0.f;
#pragma unroll 2
      for (int i = 0; i < NSW * NT / 8; ++i) {
        const f32x4 v = *reinterpret_cast<const f32x4*>(&lds[L_SC + i * 256 + lane * 4]);
        l += (__builtin_amdgcn_exp2f(v[0] - mx) + __builtin_amdgcn_exp2f(v[1] - mx)) + (__builtin_amdgcn_exp2f(v[2] - mx) + __builtin_amdgcn_exp2f(v[3] - mx));
      }
      l += dpp<XOR2>(l); l += dpp<ROR4>(l); l += dpp<XOR8>(l); l = sum_x32(sum_x16(l));
      __builtin_amdgcn_raw_buffer_store_b128(v4u{(lane & 2) ? tk : __float_as_uint(mx), tag, __float_as_uint(l), tag}, rws,
                                             lane < 8 ? WS_ML + b * 128 + lane * 16 : OOB, 0, AUX_SC1);
      wait_flag(F_VPROG, 128 - ML_T);
      v4u ml;
      for (unsigned spins = 0;; ++spins) {
        asm volatile("" ::: "memory");
        ml = __builtin_amdgcn_raw_buffer_load_b128(rws, WS_ML + (g * 16 + ((lane >> 1) & 15)) * 128 + (lane & 1) * 16 + (lane >> 5) * 32, 0, AUX_SC1);
        if (__all(((ml.y ^ tag) | (ml.w ^ tag)) == 0) || spins > SPIN_CAP) break;
        __builtin_amdgcn_s_sleep(32);
      }
      {
        float dt = (float)(int)(ml.x - tk);
        dt += dpp<XOR2>(dt); dt += dpp<ROR4>(dt); dt += dpp<XOR8>(dt); dt = sum_x16(dt);
        if (lane == 32) ldu[L_FLAG + F_TEND] = (((tk + V_DUR) << 4) + (unsigned)(int)dt) | 1u;
      }
      const float mp = __uint_as_float(ml.x);
      float M = fmaxf(mp, dpp<XOR2>(mp)); M = fmaxf(M, dpp<ROR4>(M)); M = fmaxf(M, dpp<XOR8>(M)); M = max_x16(M);
      float L = __builtin_amdgcn_exp2f(mp - M) * __uint_as_float(ml.z);
      asm volatile("" : "+v"(L));
      L += dpp<XOR2>(L); L += dpp<ROR4>(L); L += dpp<XOR8>(L); L = sum_x16(L);
      if (lane < 8) lds[L_MW + lane] = __builtin_amdgcn_exp2f(mw - M) / L;
      set_flag(F_SRDY);
    }
    if (is2) {
      wait_flag(F_VPROG, 128 - NEAR_T);
      wait_flag(F_FDONE, 3);
#pragma unroll
      for (int k = 0; k < 5; ++k) {
        const int t = k * 64 + lane;
        const f32x2 yf = *reinterpret_cast<const f32x2*>(&lds[L_YF + 2 * (t < 288 ? t : 0)]);
        __builtin_amdgcn_raw_buffer_store_b128(v4u{__float_as_uint(yf.x), tag, __float_as_uint(yf.y), tag}, rws,
                                               t < 288 ? WS_PF + ((t >> 3) * NFW + f2) * 128 + (t & 7) * 16 : OOB, 0, AUX_SC1);
      }
    }
    __builtin_amdgcn_s_setprio(3);
    wait_flag(F_ENDCNT, NSW);
    for (int i = 0; i < 3; ++i) __builtin_amdgcn_s_sleep(4);
    {
      int o2[2] = {WS_P1 + (g * 16 + s) * 2048 + lane * 16, WS_P1 + (g * 16 + s) * 2048 + 1024 + lane * 16};
      float pv4[4];
      v4u pl[3][2];
      gather<2>(rws, o2, tag, pv4, pl);
      auto h0 = __builtin_amdgcn_permlane32_swap(__float_as_uint(pv4[0] + pv4[2]), __float_as_uint(pv4[1] + pv4[3]), false, false);
      float v = sum_x16(__uint_as_float(h0[0]) + __uint_as_float(h0[1]));
      v += dpp<XOR8>(v);
      if ((lane & 24) == 0) lds[L_O + 2 * (lane & 7) + (lane >> 5)] = rnd(v);
      set_flag(F_ORDY);
      gather_end<2>(pl);
    }
    return;
  }

  const int nrole = is1 ? 4 : is2 ? 3 : 0;
  const int voff = lane * 16;
  __builtin_amdgcn_sched_barrier(0);
  __builtin_amdgcn_s_barrier();
  __builtin_amdgcn_sched_barrier(0);

  if (w >= NSW && w < NSW + nrole) {
    const int fw = w - NSW;
    v4u fr[24];
    if (is1) {
      const __amdgpu_buffer_rsrc_t rfg = mk_rsrc(wgate, DM * FF * 2);
      const __amdgpu_buffer_rsrc_t rfu = mk_rsrc(wup, DM * FF * 2);
      const int row0 = 288 * (f1 & 1) + 72 * fw + (lane >> 3);
      const int foff = row0 * (FF * 2) + (f1 >> 1) * 128 + (lane & 7) * 16;
#pragma unroll
      for (int i = 0; i < 9; ++i) {
        fr[2 * i] = __builtin_amdgcn_raw_buffer_load_b128(rfg, foff, i * 8 * FF * 2, AUX_NT);
        fr[2 * i + 1] = __builtin_amdgcn_raw_buffer_load_b128(rfu, foff, i * 8 * FF * 2, AUX_NT);
        __builtin_amdgcn_sched_barrier(0); asm volatile("s_waitcnt vmcnt(6)"); __builtin_amdgcn_sched_barrier(0);
      }
      wait_flag(F_XRDY, 1);
      tag = ldu[L_FLAG + F_TAG];
      f32x4 ag = {0.f, 0.f, 0.f, 0.f}, au = {0.f, 0.f, 0.f, 0.f};
#pragma unroll
      for (int i = 0; i < 9; ++i) {
        const v4u av = and4(ldu[L_XN + row0 + 8 * i], gm);
        ag = mfma16(av, fr[2 * i], ag); au = mfma16(av, fr[2 * i + 1], au);
      }
      ag = gemv_fin(ag, lane); au = gemv_fin(au, lane);
      if ((lane & 8) == 0) *reinterpret_cast<f32x4*>(&lds[L_YF + fw * 128 + (lane >> 5) * 64 + (lane & 7) * 8 + 4 * ((lane >> 4) & 1)]) = lane < 32 ? ag : au;
      int old = lds_inc(lds0 + (L_FLAG + F_S1CNT) * 4);
      float gs = 0.f, us = 0.f;
#pragma unroll
      for (int ww = 0; ww < 4; ++ww) { gs += lds[L_YF + ww * 128 + lane]; us += lds[L_YF + ww * 128 + 64 + lane]; }
      asm volatile("s_waitcnt lgkmcnt(0)" : "+v"(old), "+v"(gs), "+v"(us));
      if (__builtin_amdgcn_readfirstlane(old) == 3)
        __builtin_amdgcn_raw_buffer_store_b128(v4u{__float_as_uint(gs), tag, __float_as_uint(us), tag}, rws, WS_GU + (f1 * 64 + lane) * 16, 0, AUX_SC1);
    } else {
      const __amdgpu_buffer_rsrc_t rwd = mk_rsrc(wdown + (size_t)f2 * 64 * DM, 64 * DM * 2);
      const int doff = (lane >> 3) * (DM * 2) + fw * 384 + (lane & 7) * 16;
#pragma unroll
      for (int rg = 0; rg < 8; ++rg) {
#pragma unroll
        for (int k = 0; k < 3; ++k) fr[rg * 3 + k] = __builtin_amdgcn_raw_buffer_load_b128(rwd, doff + k * 128, rg * 8 * DM * 2, AUX_NT);
        __builtin_amdgcn_sched_barrier(0); asm volatile("s_waitcnt vmcnt(7)"); __builtin_amdgcn_sched_barrier(0);
      }
      wait_flag(F_QRDY, 1);
      tag = ldu[L_FLAG + F_TAG];
      for (int i = 0; i < 8; ++i) __builtin_amdgcn_s_sleep(8);
      v4u p0, p1;
      for (unsigned spins = 0;; ++spins) {
        asm volatile("" ::: "memory");
        p0 = __builtin_amdgcn_raw_buffer_load_b128(rws, WS_GU + (f2 * 128 + lane) * 16, 0, AUX_SC1);
        p1 = __builtin_amdgcn_raw_buffer_load_b128(rws, WS_GU + (f2 * 128 + lane) * 16, 1024, AUX_SC1);
        if (__all(((p0.y ^ tag) | (p0.w ^ tag) | (p1.y ^ tag) | (p1.w ^ tag)) == 0) || spins > SPIN_CAP) break;
        __builtin_amdgcn_s_sleep(16);
      }
      const float gt = rnd(__uint_as_float(p0.x) + __uint_as_float(p1.x)), up = rnd(__uint_as_float(p0.z) + __uint_as_float(p1.z));
      const float inner = 0.7978845608028654f * (gt + 0.044715f * gt * gt * gt);
      const float hv = rnd(0.5f * gt * (1.0f + tanhf(inner))) * up;
      const unsigned hp = pk_bf16(hv, hv);
      f32x4 ya[3];
#pragma unroll
      for (int k = 0; k < 3; ++k) ya[k] = f32x4{0.f, 0.f, 0.f, 0.f};
#pragma unroll
      for (int rg = 0; rg < 8; ++rg) {
        const v4u av = and4(__shfl(hp, rg * 8 + (lane >> 3)), gm);
#pragma unroll
        for (int k = 0; k < 3; ++k) ya[k] = mfma16(av, fr[rg * 3 + k], ya[k]);
      }
#pragma unroll
      for (int k = 0; k < 3; ++k) {
        ya[k] = gemv_fin(ya[k], lane);
        if (lane < 32 && (lane & 8) == 0) *reinterpret_cast<f32x4*>(&lds[L_YF + fw * 192 + k * 64 + (lane & 7) * 8 + 4 * (lane >> 4)]) = ya[k];
      }
      count_up(F_FDONE);
    }
  } else if (w < NSW) {
    const __amdgpu_buffer_rsrc_t rk = mk_rsrc(kg + (size_t)g * SK * 128, SK * 256);
    const __amdgpu_buffer_rsrc_t rv = mk_rsrc(vg + (size_t)g * SK * 128, SK * 256);
    const int ufirst = s * (NSW * NT / 2) + w;
#define TOFF(t) ((ufirst + ((t) >> 1) * NSW) << 13)
#define VOFF(t) ((t) < NT ? TOFF(t) + ((t) & 1) * 4096 : OOB)
#define tb(n, i) buf[(n) * TL + (i)]
    v4u buf[4 * TL];
#pragma unroll
    for (int i = 0; i < 2 * TL; ++i) buf[i] = __builtin_amdgcn_raw_buffer_load_b128(rk, voff + i * 1024, TOFF(0), AUX_NT);
    __builtin_amdgcn_sched_barrier(0);
    wait_flag(F_POLL, 1);
#pragma unroll
    for (int i = 0; i < 2 * TL; ++i) buf[2 * TL + i] = __builtin_amdgcn_raw_buffer_load_b128(rk, voff + i * 1024, TOFF(2), AUX_NT);
    __builtin_amdgcn_sched_barrier(0);
    wait_flag(F_QRDY, 1);
    const int hf = (c >> 2) & 1;
    const v4u qf = *reinterpret_cast<const v4u*>(&ldu[L_Q + hf * 64 + 4 * c]);
    const v4u qs = *reinterpret_cast<const v4u*>(&ldu[L_Q + (hf ^ 1) * 64 + 4 * c]);
    constexpr float LOG2E = 1.4426950408889634f, SCALE = 0.08838834764831845f;
    const bool b0 = (c & 1) != 0, b1 = (c & 2) != 0;
    float* const scw = &lds[L_SC + w * NT * 32 + (lane >> 4) * 8 + (lane & 7)];
    float m = -INFINITY;
    float y[4];
    auto qk1 = [&](const v4u* kt) {
#pragma unroll
      for (int i = 0; i < TL; ++i) {
        float xf = 0.f, xs = 0.f;
#pragma unroll
        for (int j = 0; j < 4; ++j) { xf = dot2(kt[i][j], qf[j], xf); xs = dot2(kt[i][j], qs[j], xs); }
        y[i] = xf + dpp<XOR7>(xs);
      }
      asm volatile("" : "+v"(y[0]), "+v"(y[1]), "+v"(y[2]), "+v"(y[3]));
      __builtin_amdgcn_sched_barrier(0);
    };
    auto qk2 = [&](int t) {
      const float u0 = (b0 ? y[1] : y[0]) + dpp<XOR1>(b0 ? y[0] : y[1]);
      const float u1 = (b0 ? y[3] : y[2]) + dpp<XOR1>(b0 ? y[2] : y[3]);
      float sc = (b1 ? u1 : u0) + dpp<XOR2>(b1 ? u0 : u1);
      sc += dpp<XOR8>(sc);
      const float tl2 = rnd(rnd(sc) * SCALE) * LOG2E;
      m = fmaxf(m, tl2);
      scw[t * 32] = tl2;
    };
#pragma unroll
    for (int n = 0; n < 2; ++n) {
      qk1(&tb(n, 0));
#pragma unroll
      for (int i = 0; i < TL; ++i) tb(n, i) = __builtin_amdgcn_raw_buffer_load_b128(rk, voff + i * 1024, TOFF(4) + n * 4096, AUX_NT);
      __builtin_amdgcn_sched_barrier(0);
      qk2(n);
    }
    qk1(&tb(2, 0)); qk2(2);
    qk1(&tb(3, 0)); qk2(3);
    unsigned next = (unsigned)__builtin_amdgcn_s_memrealtime() << 4, tau = TAU0;
    unsigned tend = 0, tbase = 0, vshift = 0;
    auto aim = [&](int left) {
      const unsigned te = __builtin_amdgcn_readfirstlane(*(volatile LAS unsigned*)&ldu[L_FLAG + F_TEND]);
      if (te == 0) return;
      if (te != tbase) { tend += te - tbase - vshift; tbase = te; vshift = 0; }
      const int v = __builtin_amdgcn_readfirstlane((int)((float)(int)(tend - next) * __builtin_amdgcn_rcpf((float)left)));
      tau = (unsigned)min(TAU_HI, max(TAU_LO, v));
    };
#pragma unroll 1
    for (int t = 4; t < NT; t += 2) {
      const bool lastk = t + 2 >= NT;
      const __amdgpu_buffer_rsrc_t rn = lastk ? rv : rk;
      const int nxt = TOFF(lastk ? 0 : t + 2);
      if (w == 0 && lane == 0) *(volatile LAS int*)&ldu[L_FLAG + F_KPROG] = t;
      if (t == T_CAL) tend = tbase;
      if ((t & 7) == (T_CAL & 7)) aim(2 * NT - t);
#pragma unroll
      for (int n = 0; n < 2; ++n) {
        qk1(&tb(n, 0));
        pace(next, tau, tend);
#pragma unroll
        for (int i = 0; i < TL; ++i) tb(n, i) = __builtin_amdgcn_raw_buffer_load_b128(rn, voff + i * 1024, nxt + n * 4096, AUX_NT);
        __builtin_amdgcn_sched_barrier(0);
        qk2(t + n);
      }
    }
    m = fmaxf(m, dpp<XOR1>(m)); m = fmaxf(m, dpp<XOR2>(m));
    m = max_x32(max_x16(m));
    if ((lane & 59) == 0) lds[L_MW + 2 * w + (lane >> 2)] = m;
    count_up(F_P1CNT);
    f32x4 acc = {0.f, 0.f, 0.f, 0.f};
    unsigned amask[4];
    {
      const int jp = (c & 3) + 4 * (c >> 3);
#pragma unroll
      for (int r = 0; r < 4; ++r) amask[r] = (jp >> 1) == r ? ((jp & 1) ? 0xffff0000u : 0x0000ffffu) : 0u;
    }
    auto pv = [&](float sv, const v4u* vt) {
      const float p = __builtin_amdgcn_exp2f(sv - m);
      const unsigned pp = pk_bf16(p, p);
#pragma unroll
      for (int i = 0; i < TL; ++i) {
        const unsigned pb = i == 0 ? dppu<0x00>(pp) : i == 1 ? dppu<0x55>(pp) : i == 2 ? dppu<0xAA>(pp) : dppu<0xFF>(pp);
        acc = mfma16(and4(pb, amask), vt[i], acc);
      }
      asm volatile("" : "+v"(acc));
      __builtin_amdgcn_sched_barrier(0);
    };
    int t = 0;
    vshift = tend - tbase;
    auto vrun = [&](int lim) {
#pragma unroll 1
      for (; t < lim; t += 2) {
        if ((t & 7) == (T_CAL & 7)) aim(NT - t);
        const int nxt = TOFF(t + 2);
        const float s0 = scw[t * 32], s1 = scw[t * 32 + 32];
#pragma unroll
        for (int n = 0; n < 2; ++n) {
          pv(n ? s1 : s0, &tb(n, 0));
          pace(next, tau, tend);
#pragma unroll
          for (int i = 0; i < TL; ++i) tb(n, i) = __builtin_amdgcn_raw_buffer_load_b128(rv, voff + i * 1024, nxt + n * 4096, AUX_NT);
          __builtin_amdgcn_sched_barrier(0);
        }
        if (w == 0 && lane == 0) *(volatile LAS int*)&ldu[L_FLAG + F_VPROG] = t + 2 + 128 - NT;
      }
    };
    vrun(NT - X_MAX);
    {
      const int late = min(1 << 16, max(0, (int)(next + X_MAX * TAU_N - tend)));
      vrun(NT - min(X_MAX, X_MIN + 2 * ((late * X_C) >> 15)));
    }
#pragma unroll
    for (int k = 2; k < 4; ++k) {
#pragma unroll
      for (int i = 0; i < TL; ++i) tb(k, i) = __builtin_amdgcn_raw_buffer_load_b128(rv, voff + i * 1024, TOFF(t + 2) + (k & 1) * 4096, AUX_NT);
      __builtin_amdgcn_sched_barrier(0);
    }
    __builtin_amdgcn_s_setprio(2);
    wait_flag(F_SRDY, 1);
    const float ed = lds[L_MW + 2 * w + ((lane >> 4) & 1)];
    tag = ldu[L_FLAG + F_TAG];
    const int po = WS_P1 + ((g * 16 + (lane >> 3)) * 16 + s) * 128 + (lane & 7) * 16;
#pragma unroll 1
    for (; t + 4 < NT; t += 4) {
      if (w == 0 && lane == 0) *(volatile LAS int*)&ldu[L_FLAG + F_VPROG] = t + 2 + 128 - NT;
#pragma unroll
      for (int k = 0; k < 4; ++k) {
        const int o = VOFF(t + k + 4);
        pv(scw[(t + k) * 32], &tb(k, 0));
#pragma unroll
        for (int i = 0; i < TL; ++i) tb(k, i) = __builtin_amdgcn_raw_buffer_load_b128(rv, voff + i * 1024, o, AUX_NT);
        __builtin_amdgcn_sched_barrier(0);
      }
    }
    {
      if (w == 0 && lane == 0) *(volatile LAS int*)&ldu[L_FLAG + F_VPROG] = 126;
      v4u av[4][TL];
#pragma unroll
      for (int k = 0; k < 4; ++k) {
        const float p = __builtin_amdgcn_exp2f((t + k < NT ? scw[(t + k) * 32] : -INFINITY) - m);
        const unsigned pp = pk_bf16(p, p);
        av[k][0] = and4(dppu<0x00>(pp), amask); av[k][1] = and4(dppu<0x55>(pp), amask);
        av[k][2] = and4(dppu<0xAA>(pp), amask); av[k][3] = and4(dppu<0xFF>(pp), amask);
        asm volatile("" : "+v"(av[k][0]), "+v"(av[k][1]), "+v"(av[k][2]), "+v"(av[k][3]));
      }
      __builtin_amdgcn_sched_barrier(0);
#pragma unroll
      for (int k = 0; k < 2; ++k)
#pragma unroll
        for (int i = 0; i < TL; ++i) acc = mfma16(av[k][i], tb(k, i), acc);
      if (t + 2 < NT) {
#pragma unroll
        for (int k = 2; k < 4; ++k)
#pragma unroll
          for (int i = 0; i < TL; ++i) acc = mfma16(av[k][i], tb(k, i), acc);
      }
    }
    *reinterpret_cast<f32x4*>(&lds[L_ACC + w * 256 + ((lane >> 4) & 1) * 128 + 8 * c + 4 * (lane >> 5)]) = acc * ed;
    int old = lds_inc(lds0 + (L_FLAG + F_ENDCNT) * 4);
    f32x2 o0 = {0.f, 0.f}, o1 = {0.f, 0.f};
#pragma unroll
    for (int ww = 0; ww < NSW; ++ww) { o0 += *reinterpret_cast<const f32x2*>(&lds[L_ACC + ww * 256 + 2 * lane]); o1 += *reinterpret_cast<const f32x2*>(&lds[L_ACC + ww * 256 + 128 + 2 * lane]); }
    asm volatile("s_waitcnt lgkmcnt(0)" : "+v"(old), "+v"(o0), "+v"(o1));
    if (__builtin_amdgcn_readfirstlane(old) == NSW - 1) {
      __builtin_amdgcn_raw_buffer_store_b128(v4u{__float_as_uint(o0[0]), tag, __float_as_uint(o0[1]), tag}, rws, po, 0, AUX_SC1);
      __builtin_amdgcn_raw_buffer_store_b128(v4u{__float_as_uint(o1[0]), tag, __float_as_uint(o1[1]), tag}, rws, po, 8 * 2048, AUX_SC1);
    }
  }

  if (w >= NSW) {
    const int t = (w - NSW) * 64 + lane;
    const __amdgpu_buffer_rsrc_t rwo = mk_rsrc(wo + (size_t)b * 16 * DM, 16 * DM * 2);
    if (w == NW - 1) {
      const unsigned t0 = __builtin_amdgcn_raw_buffer_load_b32(mk_rsrc(vg + (size_t)g * SK * 128, SK * 256), lane < 2 ? (s << 20) + lane * 0xffffc : OOB, 0, AUX_NT);
      const unsigned t1 = __builtin_amdgcn_raw_buffer_load_b32(rwo, lane == 0 ? 0 : OOB, 0, AUX_NT);
      const unsigned t2 = __builtin_amdgcn_raw_buffer_load_b32(mk_rsrc(out, DM * 2), lane == 0 && b < NRED ? 0 : OOB, 0, AUX_NT);
      const unsigned t3 = __builtin_amdgcn_raw_buffer_load_b32(rws, lane < 3 ? WS_P1 + lane * 0x100000 + 64 : OOB, 0, AUX_NT);
      asm volatile("" :: "v"(t0), "v"(t1), "v"(t2), "v"(t3));
    }
#pragma unroll 1
    for (unsigned spins = 0; spins < (SPIN_CAP << 5); ++spins) {
      if (__builtin_amdgcn_readfirstlane(*(volatile LAS unsigned*)&ldu[L_FLAG + F_ENDCNT]) >= 1u) break;
      __builtin_amdgcn_s_sleep(2);
    }
    float wf[32];
    {
      unsigned wod[16];
#pragma unroll
      for (int j = 0; j < 16; ++j) wod[j] = __builtin_amdgcn_raw_buffer_load_b32(rwo, t < 288 ? t * 4 : OOB, j * DM * 2, AUX_NT);
#pragma unroll
      for (int j = 0; j < 16; ++j) { wf[2 * j] = lo_f(wod[j]); wf[2 * j + 1] = hi_f(wod[j]); }
#pragma unroll
      for (int j = 0; j < 32; ++j) asm volatile("" : "+v"(wf[j]));
    }
    tag = ldu[L_FLAG + F_TAG];
    const int po = t < 288 ? WS_P2 + ((t >> 3) * NWG + b) * 128 + (t & 7) * 16 : OOB;
    f32x4 o4[4];
#pragma unroll 1
    for (unsigned spins = 0; spins < (SPIN_CAP << 8); ++spins) {
      unsigned f = *(volatile LAS unsigned*)&ldu[L_FLAG + F_ORDY];
      asm volatile("" ::: "memory");
#pragma unroll
      for (int k = 0; k < 4; ++k) o4[k] = *reinterpret_cast<const f32x4*>(&lds[L_O + 4 * k]);
      asm volatile("" : "+v"(o4[0]), "+v"(o4[1]), "+v"(o4[2]), "+v"(o4[3]) :: "memory");
      if (__builtin_amdgcn_readfirstlane(f) != 0u) break;
    }
    f32x2 ya = {0.f, 0.f}, yb = {0.f, 0.f};
#pragma unroll
    for (int j = 0; j < 16; j += 2) {
      ya += o4[j >> 2][j & 3] * f32x2{wf[2 * j], wf[2 * j + 1]};
      yb += o4[j >> 2][(j & 3) + 1] * f32x2{wf[2 * j + 2], wf[2 * j + 3]};
    }
    __builtin_amdgcn_raw_buffer_store_b128(v4u{__float_as_uint(ya.x + yb.x), tag, __float_as_uint(ya.y + yb.y), tag}, rws, po, 0, AUX_SC1);
  }

  if (b < NRED && w < 8) {
    if (w == 0) {
      const unsigned xp = __builtin_amdgcn_raw_buffer_load_b32(rx, lane < 8 ? b * 32 + lane * 4 : OOB, 0, 0);
      float f0, f1v;
      for (unsigned spins = 0;; ++spins) {
        asm volatile("" ::: "memory");
        v4u pf[9];
#pragma unroll
        for (int k = 0; k < 9; ++k) pf[k] = __builtin_amdgcn_raw_buffer_load_b128(rws, WS_PF + b * NFW * 128 + lane * 16, k * 1024, AUX_SC1);
        unsigned bad = 0; f0 = 0.f; f1v = 0.f;
#pragma unroll
        for (int k = 0; k < 9; ++k) { bad |= (pf[k].y ^ tag) | (pf[k].w ^ tag); f0 += __uint_as_float(pf[k].x); f1v += __uint_as_float(pf[k].z); }
        if (__all(bad == 0) || spins > SPIN_CAP) break;
        __builtin_amdgcn_s_sleep(8);
      }
      f0 += dpp<XOR8>(f0); f0 = rnd(sum_x32(sum_x16(f0)));
      f1v += dpp<XOR8>(f1v); f1v = rnd(sum_x32(sum_x16(f1v)));
      if (lane < 8) {
        *reinterpret_cast<f32x2*>(&lds[L_G + lane * 2]) = f32x2{rnd(f0 / (1.0f + __expf(-f0))), rnd(f1v / (1.0f + __expf(-f1v)))};
        ldu[L_G + 16 + lane] = xp;
      }
    }
    if (w < NSW) wait_flag(F_ORDY, 1);
    for (int i = 0; i < 4; ++i) __builtin_amdgcn_s_sleep(4);
    int o4[4];
    float sv[8];
#pragma unroll
    for (int k = 0; k < 4; ++k) o4[k] = WS_P2 + b * NWG * 128 + (w * 64 + lane) * 16 + k * 8192;
    v4u pl[3][4];
    gather<4>(rws, o4, tag, sv, pl);
    float s0 = (sv[0] + sv[2]) + (sv[4] + sv[6]), s1 = (sv[1] + sv[3]) + (sv[5] + sv[7]);
    s0 += dpp<XOR8>(s0); s0 = sum_x32(sum_x16(s0));
    s1 += dpp<XOR8>(s1); s1 = sum_x32(sum_x16(s1));
    if (lane < 8) *reinterpret_cast<f32x2*>(&lds[L_FIN + w * 16 + lane * 2]) = f32x2{s0, s1};
    int old = lds_inc(lds0 + (L_FLAG + F_FINCNT) * 4);
    f32x2 t2 = *reinterpret_cast<const f32x2*>(&lds[L_FIN + lane * 2]);
    f32x2 gv = *reinterpret_cast<const f32x2*>(&lds[L_G + (lane & 7) * 2]);
    unsigned xp = ldu[L_G + 16 + (lane & 7)];
    asm volatile("s_waitcnt lgkmcnt(0)" : "+v"(old), "+v"(t2), "+v"(gv), "+v"(xp));
    if (__builtin_amdgcn_readfirstlane(old) == 7) {
      t2.x += dpp<XOR8>(t2.x); t2.x = sum_x32(sum_x16(t2.x));
      t2.y += dpp<XOR8>(t2.y); t2.y = sum_x32(sum_x16(t2.y));
      const unsigned r = pk_bf16(lo_f(xp) + rnd(rnd(t2.x) * gv.x), hi_f(xp) + rnd(rnd(t2.y) * gv.y));
      __builtin_amdgcn_raw_buffer_store_b32(r, mk_rsrc(out, DM * 2), lane < 8 ? b * 32 + lane * 4 : OOB, 0, 0);
      if (b == 0 && lane == 0) __builtin_amdgcn_raw_buffer_store_b32(tag, rws, WS_EPOCH, 0, AUX_SC1);
    }
    gather_end<4>(pl);
  }
}

}

extern "C" void kernel_launch(void* const* d_in, const int* in_sizes, int n_in, void* d_out, int out_size, void* d_ws, size_t ws_size,
                              hipStream_t stream) {
  (void)in_sizes; (void)n_in; (void)out_size; (void)ws_size;
  unsigned char* wsp = (unsigned char*)d_ws + (WSPOS == 0 ? 0 : ((ws_size - ((size_t)WSPOS << 20)) & ~(size_t)0x1fffff));
  fused_decode<<<NWG, NTHR, 0, stream>>>((const unsigned short*)d_in[0], wsp, (const unsigned short*)d_in[3],
                                         (const unsigned short*)d_in[1], (const unsigned short*)d_in[6], (const unsigned short*)d_in[7],
                                         (const unsigned short*)d_in[8], (const unsigned short*)d_in[2], (const unsigned short*)d_in[4],
                                         (unsigned short*)d_out);
}
```

```cpp
#include <hip/hip_runtime.h>
#include <hip/hip_bf16.h>
#include <stdint.h>

namespace {

typedef unsigned v4u __attribute__((ext_vector_type(4)));
typedef unsigned v2u __attribute__((ext_vector_type(2)));
typedef float f32x4 __attribute__((ext_vector_type(4)));
typedef float f32x2 __attribute__((ext_vector_type(2)));
typedef __bf16 bf16x8 __attribute__((ext_vector_type(8)));
typedef __bf16 bf16x2 __attribute__((ext_vector_type(2)));
#define LAS __attribute__((address_space(3)))

constexpr int DM = 576, SK = 65536, FF = 4608, QD = 4096;
constexpr int NWG = 256;
constexpr int NSW = 4;
constexpr int NW = 9;
constexpr int NTHR = (NW + 1) * 64;
constexpr int NRED = 36;
constexpr int NFW = 72;
constexpr int TL = 4;
constexpr int NT = 64;
constexpr int OOB = 0x70000000;

constexpr int TAU0 = 884, TAU_LO = 850, TAU_HI = 907, TAU_N = 865;
constexpr int BURST = 20, STALL = 100;
constexpr int T_CAL = 12, CP_D = 6;
constexpr int K_DUR = 6430, V_DUR = 3535;
constexpr int ML_T = 54;
constexpr int X_MIN = 4, X_MAX = 20, X_C = 68;
constexpr int NEAR_T = 4;

constexpr int WS_EPOCH = 0;
constexpr int WS_QG = 4096;
constexpr int WS_ML = WS_QG + 16 * 4 * 256 * 8;
constexpr int WS_CP = WS_ML + NWG * 128;
constexpr int WS_P1 = WS_CP + NWG * 128;
constexpr int WS_P2 = WS_P1 + 16 * 16 * 16 * 128;
constexpr int WS_PF = WS_P2 + NRED * NWG * 128;
constexpr int WS_GU = WS_PF + NRED * NFW * 128;
constexpr int WS_PAD = WS_GU + NFW * 2 * 64 * 16;
constexpr int WS_END = WS_PAD + 128;

constexpr int AUX_NT = 2, AUX_SC1 = 16;
constexpr int RSRC_W3 = 0x00020000;
constexpr unsigned SPIN_CAP = 1u << 16;

constexpr int L_FLAG = 0;
enum { F_TAG, F_QRDY, F_XRDY, F_FDONE, F_POLL, F_P1CNT, F_SRDY, F_KPROG, F_VPROG, F_ORDY, F_ENDCNT, F_FINCNT, F_S1CNT, F_TEND };
constexpr int L_MW = 16;
constexpr int L_O = L_MW + 16;
constexpr int L_G = L_O + 16;
constexpr int L_Q = L_G + 32;
constexpr int L_XN = L_Q + 128;
constexpr int L_YF = L_XN + 640;
constexpr int L_FIN = L_YF + 576;
constexpr int L_ACC = L_FIN + 128;
constexpr int L_SC = L_ACC + NSW * 256;
constexpr int L_FLOATS = 24 * 1024;
static_assert(L_SC % 4 == 0 && L_ACC % 4 == 0 && L_Q % 4 == 0 && L_O % 4 == 0 && L_SC + NSW * NT * 32 <= L_FLOATS, "LDS map");
static_assert(16 * NSW * NT * 16 == SK && X_MIN >= 4, "key split");

template <int CTRL> __device__ __forceinline__ float dpp(float x) {
  return __builtin_bit_cast(float, __builtin_amdgcn_mov_dpp(__builtin_bit_cast(int, x), CTRL, 0xf, 0xf, true));
}
template <int CTRL> __device__ __forceinline__ unsigned dppu(unsigned x) {
  return (unsigned)__builtin_amdgcn_mov_dpp((int)x, CTRL, 0xf, 0xf, true);
}
constexpr int XOR1 = 0xB1, XOR2 = 0x4E, XOR7 = 0x141, XOR8 = 0x128, ROR4 = 0x124;
__device__ __forceinline__ float sum_x16(float x) {
  auto s = __builtin_amdgcn_permlane16_swap(__float_as_uint(x), __float_as_uint(x), false, false);
  return __uint_as_float(s[0]) + __uint_as_float(s[1]);
}
__device__ __forceinline__ float sum_x32(float x) {
  auto s = __builtin_amdgcn_permlane32_swap(__float_as_uint(x), __float_as_uint(x), false, false);
  return __uint_as_float(s[0]) + __uint_as_float(s[1]);
}
__device__ __forceinline__ float max_x16(float x) {
  auto s = __builtin_amdgcn_permlane16_swap(__float_as_uint(x), __float_as_uint(x), false, false);
  return fmaxf(__uint_as_float(s[0]), __uint_as_float(s[1]));
}
__device__ __forceinline__ float max_x32(float x) {
  auto s = __builtin_amdgcn_permlane32_swap(__float_as_uint(x), __float_as_uint(x), false, false);
  return fmaxf(__uint_as_float(s[0]), __uint_as_float(s[1]));
}
__device__ __forceinline__ float wave_sum(float x) {
  x += dpp<XOR1>(x); x += dpp<XOR2>(x); x += dpp<ROR4>(x); x += dpp<XOR8>(x);
  return sum_x32(sum_x16(x));
}
__device__ __forceinline__ unsigned pk_bf16(float lo, float hi) {
  bf16x2 v = {(__bf16)lo, (__bf16)hi};
  return __builtin_bit_cast(unsigned, v);
}
__device__ __forceinline__ float rnd(float x) { return (float)(__bf16)x; }
__device__ __forceinline__ float lo_f(unsigned u) { return __uint_as_float(u << 16); }
__device__ __forceinline__ float hi_f(unsigned u) { return __uint_as_float(u & 0xffff0000u); }
__device__ __forceinline__ float dot2(unsigned a, unsigned b, float c) {
  return __builtin_amdgcn_fdot2_f32_bf16(__builtin_bit_cast(bf16x2, a), __builtin_bit_cast(bf16x2, b), c, false);
}
__device__ __forceinline__ __amdgpu_buffer_rsrc_t mk_rsrc(const void* p, int bytes) {
  return __builtin_amdgcn_make_buffer_rsrc((void*)p, 0, bytes, RSRC_W3);
}
__device__ __forceinline__ v4u and4(unsigned x, const unsigned (&m)[4]) { return v4u{x & m[0], x & m[1], x & m[2], x & m[3]}; }
__device__ __forceinline__ f32x4 mfma16(v4u a, v4u bm, f32x4 cacc) {
  return __builtin_amdgcn_mfma_f32_16x16x32_bf16(__builtin_bit_cast(bf16x8, a), __builtin_bit_cast(bf16x8, bm), cacc, 0, 0, 0);
}
__device__ __forceinline__ f32x4 gemv_fin(f32x4 d, int lane) {
  const bool valid = (lane >> 5) == ((lane >> 3) & 1);
#pragma unroll
  for (int r = 0; r < 4; ++r) { float t = valid ? d[r] : 0.f; t += dpp<XOR8>(t); d[r] = sum_x32(t); }
  return d;
}
__device__ __forceinline__ int lds_inc(unsigned addr) {
  int r; unsigned long long save;
  asm volatile("s_mov_b64 %1, exec\n\ts_mov_b64 exec, 1\n\tds_add_rtn_u32 %0, %2, %3\n\ts_mov_b64 exec, %1"
               : "=&v"(r), "=&s"(save) : "v"(addr), "v"(1) : "memory");
  return r;
}
template <int N>
__device__ __forceinline__ void gather(__amdgpu_buffer_rsrc_t rs, int (&off)[N], unsigned tag, float (&val)[2 * N], v4u (&p)[3][N]) {
  auto issue = [&](int j) {
    asm volatile("" ::: "memory");
#pragma unroll
    for (int k = 0; k < N; ++k) p[j][k] = __builtin_amdgcn_raw_buffer_load_b128(rs, off[k], 0, AUX_SC1);
    __builtin_amdgcn_sched_barrier(0);
  };
  auto check = [&](int j) {
    bool done = true;
#pragma unroll
    for (int k = 0; k < N; ++k) {
      const bool ok = off[k] != OOB && ((p[j][k].y ^ tag) | (p[j][k].w ^ tag)) == 0;
      val[2 * k] = ok ? __uint_as_float(p[j][k].x) : val[2 * k]; val[2 * k + 1] = ok ? __uint_as_float(p[j][k].z) : val[2 * k + 1];
      off[k] = ok ? OOB : off[k];
      done = done && off[k] == OOB;
    }
    return __all(done) != 0;
  };
#pragma unroll
  for (int k = 0; k < 2 * N; ++k) val[k] = 0.f;
  issue(0); __builtin_amdgcn_s_sleep(5); issue(1); __builtin_amdgcn_s_sleep(5);
#pragma unroll 1
  for (unsigned spins = 0; spins < SPIN_CAP; ++spins) {
    issue(2); if (check(0)) break;
    issue(0); if (check(1)) break;
    issue(1); if (check(2)) break;
  }
}
template <int N>
__device__ __forceinline__ void gather_end(v4u (&p)[3][N]) {
#pragma unroll
  for (int j = 0; j < 3; ++j)
#pragma unroll
    for (int k = 0; k < N; ++k) asm volatile("" :: "v"(p[j][k]));
}
__device__ __forceinline__ void pace(unsigned& next, unsigned tau, unsigned& tend) {
  int d;
  asm volatile("1:\n\ts_memrealtime vcc\n\ts_waitcnt lgkmcnt(0)\n\ts_lshl_b32 %0, vcc_lo, 4\n\ts_sub_i32 %0, %0, %1\n\ts_cmp_ge_i32 %0, 0\n\ts_cbranch_scc1 2f\n\ts_sleep 1\n\ts_branch 1b\n2:"
               : "=&s"(d) : "s"(next) : "scc", "vcc");
  next += tau + (unsigned)max(0, d - BURST * 16);
  tend += d > STALL * 16 ? d : 0;
}

__global__ void __launch_bounds__(NTHR, 3)
fused_decode(const unsigned short* __restrict__ xg, unsigned char* __restrict__ ws, const unsigned short* __restrict__ wq,
             const unsigned short* __restrict__ kg, const unsigned short* __restrict__ wgate, const unsigned short* __restrict__ wup,
             const unsigned short* __restrict__ wdown, const unsigned short* __restrict__ vg, const unsigned short* __restrict__ wo,
             unsigned short* __restrict__ out) {
  __shared__ __attribute__((aligned(16))) float lds[L_FLOATS];
  asm volatile("" :: "s"(xg), "s"(ws), "s"(wq), "s"(kg), "s"(wgate), "s"(wup), "s"(wdown));
  const int lane = threadIdx.x & 63, pw = __builtin_amdgcn_readfirstlane(threadIdx.x >> 6);
  const int w = pw == 0 ? NW : pw - 1;
  const int b = blockIdx.x, g = b >> 4, s = b & 15;
  const int c = lane & 15;
  const int sr = (s + 3 * g) & 15, ns2 = 5 - (g & 1);
  const bool is1 = sr < 9, is2 = !is1 && sr < 9 + ns2;
  const int f1 = g * 9 + sr, f2 = (g >> 1) * 9 + (g & 1) * 5 + sr - 9;
  unsigned* const ldu = reinterpret_cast<unsigned*>(lds);
  const unsigned lds0 = (unsigned)(uintptr_t)(LAS float*)lds;

  const __amdgpu_buffer_rsrc_t rws = mk_rsrc(ws, WS_END);
  const __amdgpu_buffer_rsrc_t rx = mk_rsrc(xg, DM * 2);

  auto wait_flag = [&](int idx, unsigned want) {
#pragma unroll 1
    for (unsigned spins = 0; spins < (SPIN_CAP << 6); ++spins) {
      if (__builtin_amdgcn_readfirstlane(*(volatile LAS unsigned*)&ldu[L_FLAG + idx]) >= want) break;
      __builtin_amdgcn_s_sleep(1);
    }
    asm volatile("" ::: "memory");
  };
  auto set_flag = [&](int idx) {
    asm volatile("" ::: "memory");
    if (lane == 0) *(volatile LAS unsigned*)&ldu[L_FLAG + idx] = 1u;
  };
  auto count_up = [&](int idx) {
    int r = lds_inc(lds0 + (L_FLAG + idx) * 4);
    asm volatile("s_waitcnt lgkmcnt(0)" : "+v"(r));
  };
  unsigned gm[4];
#pragma unroll
  for (int r = 0; r < 4; ++r) gm[r] = ((lane & 7) >> 1) == r ? ((lane & 1) ? 0xffff0000u : 0x0000ffffu) : 0u;
  unsigned tag;

  if (__builtin_expect(w == NW, 1)) {
    __builtin_amdgcn_s_setprio(3);
    unsigned xd[5], xq[3];
#pragma unroll
    for (int k = 0; k < 5; ++k) xd[k] = __builtin_amdgcn_raw_buffer_load_b32(rx, lane * 4 + k * 256, 0, 0);
#pragma unroll
    for (int k = 0; k < 3; ++k) xq[k] = __builtin_amdgcn_raw_buffer_load_b16(rx, (144 * (s >> 2) + lane + 64 * k) * 2, 0, 0);
    const unsigned epoch = __builtin_amdgcn_raw_buffer_load_b32(rws, lane == 0 ? WS_EPOCH : OOB, 0, AUX_SC1);
    v4u wqv[18];
    {
      const __amdgpu_buffer_rsrc_t rq = mk_rsrc(wq, DM * QD * 2);
      const int o = (144 * (s >> 2) + (lane >> 3)) * (QD * 2) + (g * 4 + (s & 3)) * 128 + (lane & 7) * 16;
#pragma unroll
      for (int i = 0; i < 18; ++i) wqv[i] = __builtin_amdgcn_raw_buffer_load_b128(rq, o, i * 8 * QD * 2, AUX_NT);
    }
    if (lane < 16) ldu[L_FLAG + lane] = 0u;
    asm volatile("s_waitcnt lgkmcnt(0)" ::: "memory");
    __builtin_amdgcn_sched_barrier(0);
    __builtin_amdgcn_s_barrier();
    __builtin_amdgcn_sched_barrier(0);
    float mb, den;
    {
      float xv[10];
#pragma unroll
      for (int k = 0; k < 5; ++k) { xv[2 * k] = lo_f(xd[k]); xv[2 * k + 1] = hi_f(xd[k]); }
      float sm = 0.f;
#pragma unroll
      for (int k = 0; k < 10; ++k) sm += xv[k];
      const float mean = wave_sum(sm) * (1.0f / DM);
      float sq = 0.f;
#pragma unroll
      for (int k = 0; k < 8; ++k) { const float d = xv[k] - mean; sq = fmaf(d, d, sq); }
      const float d8 = lane < 32 ? xv[8] - mean : 0.f, d9 = lane < 32 ? xv[9] - mean : 0.f;
      sq = fmaf(d8, d8, sq); sq = fmaf(d9, d9, sq);
      const float var = wave_sum(sq) * (1.0f / DM);
      mb = rnd(mean);
      den = rnd(sqrtf(rnd(rnd(var) + 1e-5f)));
    }
    unsigned xn[3];
#pragma unroll
    for (int k = 0; k < 3; ++k) { const float v = rnd(lo_f(xq[k]) - mb) / den; xn[k] = pk_bf16(v, v); }
    unsigned xr[18];
#pragma unroll
    for (int i = 0; i < 18; ++i) xr[i] = __shfl(xn[i >> 3], (8 * i + (lane >> 3)) & 63);
#pragma unroll
    for (int i = 0; i < 18; ++i) asm volatile("" : "+v"(xr[i]));
    __builtin_amdgcn_sched_barrier(0);
    f32x4 qa = {0.f, 0.f, 0.f, 0.f};
#pragma unroll
    for (int i = 0; i < 18; ++i) { qa = mfma16(and4(xr[i], gm), wqv[i], qa); __builtin_amdgcn_sched_barrier(0); }
    qa = gemv_fin(qa, lane);
    tag = __builtin_amdgcn_readfirstlane(epoch) + 1u;
    {
      const bool up2 = (lane & 8) != 0;
      const int col = (lane & 7) * 8 + 4 * ((lane >> 4) & 1) + (up2 ? 2 : 0);
      __builtin_amdgcn_raw_buffer_store_b128(v4u{__float_as_uint(up2 ? qa[2] : qa[0]), tag, __float_as_uint(up2 ? qa[3] : qa[1]), tag}, rws,
                                             lane < 32 ? WS_QG + ((g * 4 + (s >> 2)) * 256 + (s & 3) * 64 + col) * 8 : OOB, 0, AUX_SC1);
    }
    if (lane == 0) ldu[L_FLAG + F_TAG] = tag;
    if (is1) {
#pragma unroll
      for (int k = 0; k < 5; ++k) {
        const float v0 = rnd(lo_f(xd[k]) - mb) / den, v1 = rnd(hi_f(xd[k]) - mb) / den;
        *reinterpret_cast<v2u*>(&ldu[L_XN + 2 * (lane + 64 * k)]) = v2u{pk_bf16(v0, v0), pk_bf16(v1, v1)};
      }
      set_flag(F_XRDY);
    }
    for (int i = 0; i < 5; ++i) __builtin_amdgcn_s_sleep(8);
    float q4[4];
    for (unsigned spins = 0;; ++spins) {
      asm volatile("" ::: "memory");
      v4u a[8];
#pragma unroll
      for (int r = 0; r < 4; ++r) {
        a[2 * r] = __builtin_amdgcn_raw_buffer_load_b128(rws, WS_QG + g * 8192 + lane * 32, r * 2048, AUX_SC1);
        a[2 * r + 1] = __builtin_amdgcn_raw_buffer_load_b128(rws, WS_QG + g * 8192 + lane * 32 + 16, r * 2048, AUX_SC1);
      }
      __builtin_amdgcn_sched_barrier(0);
      set_flag(F_POLL);
      __builtin_amdgcn_sched_barrier(0);
      unsigned bad = 0;
#pragma unroll
      for (int j = 0; j < 4; ++j) q4[j] = 0.f;
#pragma unroll
      for (int r = 0; r < 4; ++r) {
        bad |= (a[2 * r].y ^ tag) | (a[2 * r].w ^ tag) | (a[2 * r + 1].y ^ tag) | (a[2 * r + 1].w ^ tag);
        q4[0] += __uint_as_float(a[2 * r].x); q4[1] += __uint_as_float(a[2 * r].z);
        q4[2] += __uint_as_float(a[2 * r + 1].x); q4[3] += __uint_as_float(a[2 * r + 1].z);
      }
      if (__all(bad == 0) || spins > SPIN_CAP) break;
      __builtin_amdgcn_s_sleep(1);
    }
    *reinterpret_cast<v2u*>(&ldu[L_Q + 2 * lane]) = v2u{pk_bf16(q4[0], q4[1]), pk_bf16(q4[2], q4[3])};
    set_flag(F_QRDY);
    __builtin_amdgcn_s_setprio(0);
    {
      wait_flag(F_KPROG, T_CAL);
      const unsigned t1 = (unsigned)__builtin_amdgcn_s_memrealtime();
      __builtin_amdgcn_raw_buffer_store_b128(v4u{t1, tag, t1, tag}, rws, lane < 8 ? WS_CP + b * 128 + lane * 16 : OOB, 0, AUX_SC1);
      wait_flag(F_KPROG, T_CAL + CP_D);
      const v2u cp = __builtin_amdgcn_raw_buffer_load_b64(rws, WS_CP + (g * 16 + c) * 128, 0, AUX_SC1);
      float dt = cp.y == tag ? (float)(int)(cp.x - t1) : (float)(CP_D * 55);
      dt += dpp<XOR1>(dt); dt += dpp<XOR2>(dt); dt += dpp<ROR4>(dt); dt += dpp<XOR8>(dt);
      if (lane == 0) ldu[L_FLAG + F_TEND] = (((t1 + K_DUR) << 4) + (unsigned)(int)dt) | 1u;
    }
    wait_flag(F_P1CNT, NSW);
    {
      const unsigned tk = (unsigned)__builtin_amdgcn_s_memrealtime();
      const float mw = lds[L_MW + (lane & 7)];
      float mx = fmaxf(mw, dpp<XOR2>(mw)); mx = fmaxf(mx, dpp<ROR4>(mx));
      float l = 0.f;
#pragma unroll 2
      for (int i = 0; i < NSW * NT / 8; ++i) {
        const f32x4 v = *reinterpret_cast<const f32x4*>(&lds[L_SC + i * 256 + lane * 4]);
        l += (__builtin_amdgcn_exp2f(v[0] - mx) + __builtin_amdgcn_exp2f(v[1] - mx)) + (__builtin_amdgcn_exp2f(v[2] - mx) + __builtin_amdgcn_exp2f(v[3] - mx));
      }
      l += dpp<XOR2>(l); l += dpp<ROR4>(l); l += dpp<XOR8>(l); l = sum_x32(sum_x16(l));
      __builtin_amdgcn_raw_buffer_store_b128(v4u{(lane & 2) ? tk : __float_as_uint(mx), tag, __float_as_uint(l), tag}, rws,
                                             lane < 8 ? WS_ML + b * 128 + lane * 16 : OOB, 0, AUX_SC1);
      wait_flag(F_VPROG, 128 - ML_T);
      v4u ml;
      for (unsigned spins = 0;; ++spins) {
        asm volatile("" ::: "memory");
        ml = __builtin_amdgcn_raw_buffer_load_b128(rws, WS_ML + (g * 16 + ((lane >> 1) & 15)) * 128 + (lane & 1) * 16 + (lane >> 5) * 32, 0, AUX_SC1);
        if (__all(((ml.y ^ tag) | (ml.w ^ tag)) == 0) || spins > SPIN_CAP) break;
        __builtin_amdgcn_s_sleep(32);
      }
      {
        float dt = (float)(int)(ml.x - tk);
        dt += dpp<XOR2>(dt); dt += dpp<ROR4>(dt); dt += dpp<XOR8>(dt); dt = sum_x16(dt);
        if (lane == 32) ldu[L_FLAG + F_TEND] = (((tk + V_DUR) << 4) + (unsigned)(int)dt) | 1u;
      }
      const float mp = __uint_as_float(ml.x);
      float M = fmaxf(mp, dpp<XOR2>(mp)); M = fmaxf(M, dpp<ROR4>(M)); M = fmaxf(M, dpp<XOR8>(M)); M = max_x16(M);
      float L = __builtin_amdgcn_exp2f(mp - M) * __uint_as_float(ml.z);
      asm volatile("" : "+v"(L));
      L += dpp<XOR2>(L); L += dpp<ROR4>(L); L += dpp<XOR8>(L); L = sum_x16(L);
      if (lane < 8) lds[L_MW + lane] = __builtin_amdgcn_exp2f(mw - M) / L;
      set_flag(F_SRDY);
    }
    if (is2) {
      wait_flag(F_VPROG, 128 - NEAR_T);
      wait_flag(F_FDONE, 3);
#pragma unroll
      for (int k = 0; k < 5; ++k) {
        const int t = k * 64 + lane;
        const f32x2 yf = *reinterpret_cast<const f32x2*>(&lds[L_YF + 2 * (t < 288 ? t : 0)]);
        __builtin_amdgcn_raw_buffer_store_b128(v4u{__float_as_uint(yf.x), tag, __float_as_uint(yf.y), tag}, rws,
                                               t < 288 ? WS_PF + ((t >> 3) * NFW + f2) * 128 + (t & 7) * 16 : OOB, 0, AUX_SC1);
      }
    }
    __builtin_amdgcn_s_setprio(3);
    wait_flag(F_ENDCNT, NSW);
    for (int i = 0; i < 3; ++i) __builtin_amdgcn_s_sleep(4);
    {
      int o2[2] = {WS_P1 + (g * 16 + s) * 2048 + lane * 16, WS_P1 + (g * 16 + s) * 2048 + 1024 + lane * 16};
      float pv4[4];
      v4u pl[3][2];
      gather<2>(rws, o2, tag, pv4, pl);
      auto h0 = __builtin_amdgcn_permlane32_swap(__float_as_uint(pv4[0] + pv4[2]), __float_as_uint(pv4[1] + pv4[3]), false, false);
      float v = sum_x16(__uint_as_float(h0[0]) + __uint_as_float(h0[1]));
      v += dpp<XOR8>(v);
      if ((lane & 24) == 0) lds[L_O + 2 * (lane & 7) + (lane >> 5)] = rnd(v);
      set_flag(F_ORDY);
      gather_end<2>(pl);
    }
    return;
  }

  const int nrole = is1 ? 4 : is2 ? 3 : 0;
  const int voff = lane * 16;
  __builtin_amdgcn_sched_barrier(0);
  __builtin_amdgcn_s_barrier();
  __builtin_amdgcn_sched_barrier(0);

  if (w >= NSW && w < NSW + nrole) {
    const int fw = w - NSW;
    v4u fr[24];
    if (is1) {
      const __amdgpu_buffer_rsrc_t rfg = mk_rsrc(wgate, DM * FF * 2);
      const __amdgpu_buffer_rsrc_t rfu = mk_rsrc(wup, DM * FF * 2);
      const int row0 = 288 * (f1 & 1) + 72 * fw + (lane >> 3);
      const int foff = row0 * (FF * 2) + (f1 >> 1) * 128 + (lane & 7) * 16;
#pragma unroll
      for (int i = 0; i < 9; ++i) {
        fr[2 * i] = __builtin_amdgcn_raw_buffer_load_b128(rfg, foff, i * 8 * FF * 2, AUX_NT);
        fr[2 * i + 1] = __builtin_amdgcn_raw_buffer_load_b128(rfu, foff, i * 8 * FF * 2, AUX_NT);
        __builtin_amdgcn_sched_barrier(0); asm volatile("s_waitcnt vmcnt(6)"); __builtin_amdgcn_sched_barrier(0);
      }
      wait_flag(F_XRDY, 1);
      tag = ldu[L_FLAG + F_TAG];
      f32x4 ag = {0.f, 0.f, 0.f, 0.f}, au = {0.f, 0.f, 0.f, 0.f};
#pragma unroll
      for (int i = 0; i < 9; ++i) {
        const v4u av = and4(ldu[L_XN + row0 + 8 * i], gm);
        ag = mfma16(av, fr[2 * i], ag); au = mfma16(av, fr[2 * i + 1], au);
      }
      ag = gemv_fin(ag, lane); au = gemv_fin(au, lane);
      if ((lane & 8) == 0) *reinterpret_cast<f32x4*>(&lds[L_YF + fw * 128 + (lane >> 5) * 64 + (lane & 7) * 8 + 4 * ((lane >> 4) & 1)]) = lane < 32 ? ag : au;
      int old = lds_inc(lds0 + (L_FLAG + F_S1CNT) * 4);
      float gs = 0.f, us = 0.f;
#pragma unroll
      for (int ww = 0; ww < 4; ++ww) { gs += lds[L_YF + ww * 128 + lane]; us += lds[L_YF + ww * 128 + 64 + lane]; }
      asm volatile("s_waitcnt lgkmcnt(0)" : "+v"(old), "+v"(gs), "+v"(us));
      if (__builtin_amdgcn_readfirstlane(old) == 3)
        __builtin_amdgcn_raw_buffer_store_b128(v4u{__float_as_uint(gs), tag, __float_as_uint(us), tag}, rws, WS_GU + (f1 * 64 + lane) * 16, 0, AUX_SC1);
    } else {
      const __amdgpu_buffer_rsrc_t rwd = mk_rsrc(wdown + (size_t)f2 * 64 * DM, 64 * DM * 2);
      const int doff = (lane >> 3) * (DM * 2) + fw * 384 + (lane & 7) * 16;
#pragma unroll
      for (int rg = 0; rg < 8; ++rg) {
#pragma unroll
        for (int k = 0; k < 3; ++k) fr[rg * 3 + k] = __builtin_amdgcn_raw_buffer_load_b128(rwd, doff + k * 128, rg * 8 * DM * 2, AUX_NT);
        __builtin_amdgcn_sched_barrier(0); asm volatile("s_waitcnt vmcnt(7)"); __builtin_amdgcn_sched_barrier(0);
      }
      wait_flag(F_QRDY, 1);
      tag = ldu[L_FLAG + F_TAG];
      for (int i = 0; i < 8; ++i) __builtin_amdgcn_s_sleep(8);
      v4u p0, p1;
      for (unsigned spins = 0;; ++spins) {
        asm volatile("" ::: "memory");
        p0 = __builtin_amdgcn_raw_buffer_load_b128(rws, WS_GU + (f2 * 128 + lane) * 16, 0, AUX_SC1);
        p1 = __builtin_amdgcn_raw_buffer_load_b128(rws, WS_GU + (f2 * 128 + lane) * 16, 1024, AUX_SC1);
        if (__all(((p0.y ^ tag) | (p0.w ^ tag) | (p1.y ^ tag) | (p1.w ^ tag)) == 0) || spins > SPIN_CAP) break;
        __builtin_amdgcn_s_sleep(16);
      }
      const float gt = rnd(__uint_as_float(p0.x) + __uint_as_float(p1.x)), up = rnd(__uint_as_float(p0.z) + __uint_as_float(p1.z));
      const float inner = 0.7978845608028654f * (gt + 0.044715f * gt * gt * gt);
      const float hv = rnd(0.5f * gt * (1.0f + tanhf(inner))) * up;
      const unsigned hp = pk_bf16(hv, hv);
      f32x4 ya[3];
#pragma unroll
      for (int k = 0; k < 3; ++k) ya[k] = f32x4{0.f, 0.f, 0.f, 0.f};
#pragma unroll
      for (int rg = 0; rg < 8; ++rg) {
        const v4u av = and4(__shfl(hp, rg * 8 + (lane >> 3)), gm);
#pragma unroll
        for (int k = 0; k < 3; ++k) ya[k] = mfma16(av, fr[rg * 3 + k], ya[k]);
      }
#pragma unroll
      for (int k = 0; k < 3; ++k) {
        ya[k] = gemv_fin(ya[k], lane);
        if (lane < 32 && (lane & 8) == 0) *reinterpret_cast<f32x4*>(&lds[L_YF + fw * 192 + k * 64 + (lane & 7) * 8 + 4 * (lane >> 4)]) = ya[k];
      }
      count_up(F_FDONE);
    }
  } else if (w < NSW) {
    const __amdgpu_buffer_rsrc_t rk = mk_rsrc(kg + (size_t)g * SK * 128, SK * 256);
    const __amdgpu_buffer_rsrc_t rv = mk_rsrc(vg + (size_t)g * SK * 128, SK * 256);
    const int ufirst = s * (NSW * NT / 2) + w;
#define TOFF(t) ((ufirst + ((t) >> 1) * NSW) << 13)
#define VOFF(t) ((t) < NT ? TOFF(t) + ((t) & 1) * 4096 : OOB)
#define tb(n, i) buf[(n) * TL + (i)]
    v4u buf[4 * TL];
#pragma unroll
    for (int i = 0; i < 2 * TL; ++i) buf[i] = __builtin_amdgcn_raw_buffer_load_b128(rk, voff + i * 1024, TOFF(0), AUX_NT);
    __builtin_amdgcn_sched_barrier(0);
    wait_flag(F_POLL, 1);
#pragma unroll
    for (int i = 0; i < 2 * TL; ++i) buf[2 * TL + i] = __builtin_amdgcn_raw_buffer_load_b128(rk, voff + i * 1024, TOFF(2), AUX_NT);
    __builtin_amdgcn_sched_barrier(0);
    wait_flag(F_QRDY, 1);
    const int hf = (c >> 2) & 1;
    const v4u qf = *reinterpret_cast<const v4u*>(&ldu[L_Q + hf * 64 + 4 * c]);
    const v4u qs = *reinterpret_cast<const v4u*>(&ldu[L_Q + (hf ^ 1) * 64 + 4 * c]);
    constexpr float LOG2E = 1.4426950408889634f, SCALE = 0.08838834764831845f;
    const bool b0 = (c & 1) != 0, b1 = (c & 2) != 0;
    float* const scw = &lds[L_SC + w * NT * 32 + (lane >> 4) * 8 + (lane & 7)];
    float m = -INFINITY;
    float y[4];
    auto qk1 = [&](const v4u* kt) {
#pragma unroll
      for (int i = 0; i < TL; ++i) {
        float xf = 0.f, xs = 0.f;
#pragma unroll
        for (int j = 0; j < 4; ++j) { xf = dot2(kt[i][j], qf[j], xf); xs = dot2(kt[i][j], qs[j], xs); }
        y[i] = xf + dpp<XOR7>(xs);
      }
      asm volatile("" : "+v"(y[0]), "+v"(y[1]), "+v"(y[2]), "+v"(y[3]));
      __builtin_amdgcn_sched_barrier(0);
    };
    auto qk2 = [&](int t) {
      const float u0 = (b0 ? y[1] : y[0]) + dpp<XOR1>(b0 ? y[0] : y[1]);
      const float u1 = (b0 ? y[3] : y[2]) + dpp<XOR1>(b0 ? y[2] : y[3]);
      float sc = (b1 ? u1 : u0) + dpp<XOR2>(b1 ? u0 : u1);
      sc += dpp<XOR8>(sc);
      const float tl2 = rnd(rnd(sc) * SCALE) * LOG2E;
      m = fmaxf(m, tl2);
      scw[t * 32] = tl2;
    };
#pragma unroll
    for (int n = 0; n < 2; ++n) {
      qk1(&tb(n, 0));
#pragma unroll
      for (int i = 0; i < TL; ++i) tb(n, i) = __builtin_amdgcn_raw_buffer_load_b128(rk, voff + i * 1024, TOFF(4) + n * 4096, AUX_NT);
      __builtin_amdgcn_sched_barrier(0);
      qk2(n);
    }
    qk1(&tb(2, 0)); qk2(2);
    qk1(&tb(3, 0)); qk2(3);
    unsigned next = (unsigned)__builtin_amdgcn_s_memrealtime() << 4, tau = TAU0;
    unsigned tend = 0, tbase = 0, vshift = 0;
    auto aim = [&](int left) {
      const unsigned te = __builtin_amdgcn_readfirstlane(*(volatile LAS unsigned*)&ldu[L_FLAG + F_TEND]);
      if (te == 0) return;
      if (te != tbase) { tend += te - tbase - vshift; tbase = te; vshift = 0; }
      const int v = __builtin_amdgcn_readfirstlane((int)((float)(int)(tend - next) * __builtin_amdgcn_rcpf((float)left)));
      tau = (unsigned)min(TAU_HI, max(TAU_LO, v));
    };
#pragma unroll 1
    for (int t = 4; t < NT; t += 2) {
      const bool lastk = t + 2 >= NT;
      const __amdgpu_buffer_rsrc_t rn = lastk ? rv : rk;
      const int nxt = TOFF(lastk ? 0 : t + 2);
      if (w == 0 && lane == 0) *(volatile LAS int*)&ldu[L_FLAG + F_KPROG] = t;
      if (t == T_CAL) tend = tbase;
      if ((t & 7) == (T_CAL & 7)) aim(2 * NT - t);
#pragma unroll
      for (int n = 0; n < 2; ++n) {
        qk1(&tb(n, 0));
        pace(next, tau, tend);
#pragma unroll
        for (int i = 0; i < TL; ++i) tb(n, i) = __builtin_amdgcn_raw_buffer_load_b128(rn, voff + i * 1024, nxt + n * 4096, AUX_NT);
        __builtin_amdgcn_sched_barrier(0);
        qk2(t + n);
      }
    }
    m = fmaxf(m, dpp<XOR1>(m)); m = fmaxf(m, dpp<XOR2>(m));
    m = max_x32(max_x16(m));
    if ((lane & 59) == 0) lds[L_MW + 2 * w + (lane >> 2)] = m;
    count_up(F_P1CNT);
    f32x4 acc = {0.f, 0.f, 0.f, 0.f};
    unsigned amask[4];
    {
      const int jp = (c & 3) + 4 * (c >> 3);
#pragma unroll
      for (int r = 0; r < 4; ++r) amask[r] = (jp >> 1) == r ? ((jp & 1) ? 0xffff0000u : 0x0000ffffu) : 0u;
    }
    auto pv = [&](float sv, const v4u* vt) {
      const float p = __builtin_amdgcn_exp2f(sv - m);
      const unsigned pp = pk_bf16(p, p);
#pragma unroll
      for (int i = 0; i < TL; ++i) {
        const unsigned pb = i == 0 ? dppu<0x00>(pp) : i == 1 ? dppu<0x55>(pp) : i == 2 ? dppu<0xAA>(pp) : dppu<0xFF>(pp);
        acc = mfma16(and4(pb, amask), vt[i], acc);
      }
      asm volatile("" : "+v"(acc));
      __builtin_amdgcn_sched_barrier(0);
    };
    int t = 0;
    vshift = tend - tbase;
    auto vrun = [&](int lim) {
#pragma unroll 1
      for (; t < lim; t += 2) {
        if ((t & 7) == (T_CAL & 7)) aim(NT - t);
        const int nxt = TOFF(t + 2);
        const float s0 = scw[t * 32], s1 = scw[t * 32 + 32];
#pragma unroll
        for (int n = 0; n < 2; ++n) {
          pv(n ? s1 : s0, &tb(n, 0));
          pace(next, tau, tend);
#pragma unroll
          for (int i = 0; i < TL; ++i) tb(n, i) = __builtin_amdgcn_raw_buffer_load_b128(rv, voff + i * 1024, nxt + n * 4096, AUX_NT);
          __builtin_amdgcn_sched_barrier(0);
        }
        if (w == 0 && lane == 0) *(volatile LAS int*)&ldu[L_FLAG + F_VPROG] = t + 2 + 128 - NT;
      }
    };
    vrun(NT - X_MAX);
    {
      const int late = min(1 << 16, max(0, (int)(next + X_MAX * TAU_N - tend)));
      vrun(NT - min(X_MAX, X_MIN + 2 * ((late * X_C) >> 15)));
    }
#pragma unroll
    for (int k = 2; k < 4; ++k) {
#pragma unroll
      for (int i = 0; i < TL; ++i) tb(k, i) = __builtin_amdgcn_raw_buffer_load_b128(rv, voff + i * 1024, TOFF(t + 2) + (k & 1) * 4096, AUX_NT);
      __builtin_amdgcn_sched_barrier(0);
    }
    __builtin_amdgcn_s_setprio(2);
    wait_flag(F_SRDY, 1);
    const float ed = lds[L_MW + 2 * w + ((lane >> 4) & 1)];
    tag = ldu[L_FLAG + F_TAG];
    const int po = WS_P1 + ((g * 16 + (lane >> 3)) * 16 + s) * 128 + (lane & 7) * 16;
#pragma unroll 1
    for (; t + 4 < NT; t += 4) {
      if (w == 0 && lane == 0) *(volatile LAS int*)&ldu[L_FLAG + F_VPROG] = t + 2 + 128 - NT;
#pragma unroll
      for (int k = 0; k < 4; ++k) {
        const int o = VOFF(t + k + 4);
        pv(scw[(t + k) * 32], &tb(k, 0));
#pragma unroll
        for (int i = 0; i < TL; ++i) tb(k, i) = __builtin_amdgcn_raw_buffer_load_b128(rv, voff + i * 1024, o, AUX_NT);
        __builtin_amdgcn_sched_barrier(0);
      }
    }
    {
      if (w == 0 && lane == 0) *(volatile LAS int*)&ldu[L_FLAG + F_VPROG] = 126;
      v4u av[4][TL];
#pragma unroll
      for (int k = 0; k < 4; ++k) {
        const float p = __builtin_amdgcn_exp2f((t + k < NT ? scw[(t + k) * 32] : -INFINITY) - m);
        const unsigned pp = pk_bf16(p, p);
        av[k][0] = and4(dppu<0x00>(pp), amask); av[k][1] = and4(dppu<0x55>(pp), amask);
        av[k][2] = and4(dppu<0xAA>(pp), amask); av[k][3] = and4(dppu<0xFF>(pp), amask);
        asm volatile("" : "+v"(av[k][0]), "+v"(av[k][1]), "+v"(av[k][2]), "+v"(av[k][3]));
      }
      __builtin_amdgcn_sched_barrier(0);
#pragma unroll
      for (int k = 0; k < 2; ++k)
#pragma unroll
        for (int i = 0; i < TL; ++i) acc = mfma16(av[k][i], tb(k, i), acc);
      if (t + 2 < NT) {
#pragma unroll
        for (int k = 2; k < 4; ++k)
#pragma unroll
          for (int i = 0; i < TL; ++i) acc = mfma16(av[k][i], tb(k, i), acc);
      }
    }
    *reinterpret_cast<f32x4*>(&lds[L_ACC + w * 256 + ((lane >> 4) & 1) * 128 + 8 * c + 4 * (lane >> 5)]) = acc * ed;
    int old = lds_inc(lds0 + (L_FLAG + F_ENDCNT) * 4);
    f32x2 o0 = {0.f, 0.f}, o1 = {0.f, 0.f};
#pragma unroll
    for (int ww = 0; ww < NSW; ++ww) { o0 += *reinterpret_cast<const f32x2*>(&lds[L_ACC + ww * 256 + 2 * lane]); o1 += *reinterpret_cast<const f32x2*>(&lds[L_ACC + ww * 256 + 128 + 2 * lane]); }
    asm volatile("s_waitcnt lgkmcnt(0)" : "+v"(old), "+v"(o0), "+v"(o1));
    if (__builtin_amdgcn_readfirstlane(old) == NSW - 1) {
      __builtin_amdgcn_raw_buffer_store_b128(v4u{__float_as_uint(o0[0]), tag, __float_as_uint(o0[1]), tag}, rws, po, 0, AUX_SC1);
      __builtin_amdgcn_raw_buffer_store_b128(v4u{__float_as_uint(o1[0]), tag, __float_as_uint(o1[1]), tag}, rws, po, 8 * 2048, AUX_SC1);
    }
  }

  if (w >= NSW) {
    const int t = (w - NSW) * 64 + lane;
    const __amdgpu_buffer_rsrc_t rwo = mk_rsrc(wo + (size_t)b * 16 * DM, 16 * DM * 2);
    if (w == NW - 1) {
      const unsigned t0 = __builtin_amdgcn_raw_buffer_load_b32(mk_rsrc(vg + (size_t)g * SK * 128, SK * 256), lane < 2 ? (s << 20) + lane * 0xffffc : OOB, 0, AUX_NT);
      const unsigned t1 = __builtin_amdgcn_raw_buffer_load_b32(rwo, lane == 0 ? 0 : OOB, 0, AUX_NT);
      const unsigned t2 = __builtin_amdgcn_raw_buffer_load_b32(mk_rsrc(out, DM * 2), lane == 0 && b < NRED ? b * 32 : OOB, 0, AUX_SC1);
      const unsigned t3 = __builtin_amdgcn_raw_buffer_load_b32(rws, lane == 0 ? WS_PAD : OOB, 0, AUX_NT);
      asm volatile("" :: "v"(t0), "v"(t1), "v"(t2), "v"(t3));
    }
#pragma unroll 1
    for (unsigned spins = 0; spins < (SPIN_CAP << 5); ++spins) {
      if (__builtin_amdgcn_readfirstlane(*(volatile LAS unsigned*)&ldu[L_FLAG + F_ENDCNT]) >= 1u) break;
      __builtin_amdgcn_s_sleep(2);
    }
    float wf[32];
    {
      unsigned wod[16];
#pragma unroll
      for (int j = 0; j < 16; ++j) wod[j] = __builtin_amdgcn_raw_buffer_load_b32(rwo, t < 288 ? t * 4 : OOB, j * DM * 2, AUX_NT);
#pragma unroll
      for (int j = 0; j < 16; ++j) { wf[2 * j] = lo_f(wod[j]); wf[2 * j + 1] = hi_f(wod[j]); }
#pragma unroll
      for (int j = 0; j < 32; ++j) asm volatile("" : "+v"(wf[j]));
    }
    tag = ldu[L_FLAG + F_TAG];
    const int po = t < 288 ? WS_P2 + ((t >> 3) * NWG + b) * 128 + (t & 7) * 16 : OOB;
    f32x4 o4[4];
#pragma unroll 1
    for (unsigned spins = 0; spins < (SPIN_CAP << 8); ++spins) {
      unsigned f = *(volatile LAS unsigned*)&ldu[L_FLAG + F_ORDY];
      asm volatile("" ::: "memory");
#pragma unroll
      for (int k = 0; k < 4; ++k) o4[k] = *reinterpret_cast<const f32x4*>(&lds[L_O + 4 * k]);
      asm volatile("" : "+v"(o4[0]), "+v"(o4[1]), "+v"(o4[2]), "+v"(o4[3]) :: "memory");
      if (__builtin_amdgcn_readfirstlane(f) != 0u) break;
    }
    f32x2 ya = {0.f, 0.f}, yb = {0.f, 0.f};
#pragma unroll
    for (int j = 0; j < 16; j += 2) {
      ya += o4[j >> 2][j & 3] * f32x2{wf[2 * j], wf[2 * j + 1]};
      yb += o4[j >> 2][(j & 3) + 1] * f32x2{wf[2 * j + 2], wf[2 * j + 3]};
    }
    __builtin_amdgcn_raw_buffer_store_b128(v4u{__float_as_uint(ya.x + yb.x), tag, __float_as_uint(ya.y + yb.y), tag}, rws, po, 0, AUX_SC1);
  }

  if (b < NRED && w < 8) {
    if (w == 0) {
      const unsigned xp = __builtin_amdgcn_raw_buffer_load_b32(rx, lane < 8 ? b * 32 + lane * 4 : OOB, 0, 0);
      float f0, f1v;
      for (unsigned spins = 0;; ++spins) {
        asm volatile("" ::: "memory");
        v4u pf[9];
#pragma unroll
        for (int k = 0; k < 9; ++k) pf[k] = __builtin_amdgcn_raw_buffer_load_b128(rws, WS_PF + b * NFW * 128 + lane * 16, k * 1024, AUX_SC1);
        unsigned bad = 0; f0 = 0.f; f1v = 0.f;
#pragma unroll
        for (int k = 0; k < 9; ++k) { bad |= (pf[k].y ^ tag) | (pf[k].w ^ tag); f0 += __uint_as_float(pf[k].x); f1v += __uint_as_float(pf[k].z); }
        if (__all(bad == 0) || spins > SPIN_CAP) break;
        __builtin_amdgcn_s_sleep(8);
      }
      f0 += dpp<XOR8>(f0); f0 = rnd(sum_x32(sum_x16(f0)));
      f1v += dpp<XOR8>(f1v); f1v = rnd(sum_x32(sum_x16(f1v)));
      if (lane < 8) {
        *reinterpret_cast<f32x2*>(&lds[L_G + lane * 2]) = f32x2{rnd(f0 / (1.0f + __expf(-f0))), rnd(f1v / (1.0f + __expf(-f1v)))};
        ldu[L_G + 16 + lane] = xp;
      }
    }
    if (w < NSW) wait_flag(F_ORDY, 1);
    for (int i = 0; i < 4; ++i) __builtin_amdgcn_s_sleep(4);
    int o4[4];
    float sv[8];
#pragma unroll
    for (int k = 0; k < 4; ++k) o4[k] = WS_P2 + b * NWG * 128 + (w * 64 + lane) * 16 + k * 8192;
    v4u pl[3][4];
    gather<4>(rws, o4, tag, sv, pl);
    float s0 = (sv[0] + sv[2]) + (sv[4] + sv[6]), s1 = (sv[1] + sv[3]) + (sv[5] + sv[7]);
    s0 += dpp<XOR8>(s0); s0 = sum_x32(sum_x16(s0));
    s1 += dpp<XOR8>(s1); s1 = sum_x32(sum_x16(s1));
    if (lane < 8) *reinterpret_cast<f32x2*>(&lds[L_FIN + w * 16 + lane * 2]) = f32x2{s0, s1};
    int old = lds_inc(lds0 + (L_FLAG + F_FINCNT) * 4);
    f32x2 t2 = *reinterpret_cast<const f32x2*>(&lds[L_FIN + lane * 2]);
    f32x2 gv = *reinterpret_cast<const f32x2*>(&lds[L_G + (lane & 7) * 2]);
    unsigned xp = ldu[L_G + 16 + (lane & 7)];
    asm volatile("s_waitcnt lgkmcnt(0)" : "+v"(old), "+v"(t2), "+v"(gv), "+v"(xp));
    if (__builtin_amdgcn_readfirstlane(old) == 7) {
      t2.x += dpp<XOR8>(t2.x); t2.x = sum_x32(sum_x16(t2.x));
      t2.y += dpp<XOR8>(t2.y); t2.y = sum_x32(sum_x16(t2.y));
      const unsigned r = pk_bf16(lo_f(xp) + rnd(rnd(t2.x) * gv.x), hi_f(xp) + rnd(rnd(t2.y) * gv.y));
      __builtin_amdgcn_raw_buffer_store_b32(r, mk_rsrc(out, DM * 2), lane < 8 ? b * 32 + lane * 4 : OOB, 0, 0);
      if (b == 0 && lane == 0) __builtin_amdgcn_raw_buffer_store_b32(tag, rws, WS_EPOCH, 0, AUX_SC1);
    }
    gather_end<4>(pl);
  }
}

}

extern "C" void kernel_launch(void* const* d_in, const int* in_sizes, int n_in, void* d_out, int out_size, void* d_ws, size_t ws_size,
                              hipStream_t stream) {
  (void)in_sizes; (void)n_in; (void)out_size; (void)ws_size;
  fused_decode<<<NWG, NTHR, 0, stream>>>((const unsigned short*)d_in[0], (unsigned char*)d_ws, (const unsigned short*)d_in[3],
                                         (const unsigned short*)d_in[1], (const unsigned short*)d_in[6], (const unsigned short*)d_in[7],
                                         (const unsigned short*)d_in[8], (const unsigned short*)d_in[2], (const unsigned short*)d_in[4],
                                         (unsigned short*)d_out);
}
```

```cpp
#include <hip/hip_runtime.h>
#include <hip/hip_bf16.h>
#include <stdint.h>

namespace {

typedef unsigned v4u __attribute__((ext_vector_type(4)));
typedef unsigned v2u __attribute__((ext_vector_type(2)));
typedef float f32x4 __attribute__((ext_vector_type(4)));
typedef float f32x2 __attribute__((ext_vector_type(2)));
typedef __bf16 bf16x8 __attribute__((ext_vector_type(8)));
typedef __bf16 bf16x2 __attribute__((ext_vector_type(2)));
#define LAS __attribute__((address_space(3)))

constexpr int DM = 576, SK = 65536, FF = 4608, QD = 4096;
constexpr int NWG = 256;
constexpr int NSW = 4;
constexpr int NW = 9;
constexpr int NTHR = (NW + 1) * 64;
constexpr int NRED = 36;
constexpr int NFW = 72;
constexpr int TL = 4;
constexpr int NT = 64;
constexpr int OOB = 0x70000000;

#define KNOM 890
#define KCPS 4
#define KUP 1.010f
#define KDN 0.995f
#define KTH 1.004f
constexpr int TAU_NOM = KNOM;
constexpr int TAU_DN = 35, TAU_UP = 22, TAU_FAST = 20;
constexpr int BURST = 20, STALL = 100;
constexpr int CP_D = 6;
constexpr int ML_AT = 74;
constexpr int X_MIN = 4, X_MAX = 20, X_C = 68;
constexpr int NEAR_AT = 124;

constexpr int WS_EPOCH = 0;
constexpr int WS_QG = 4096;
constexpr int WS_ML = WS_QG + 16 * 4 * 256 * 8;
constexpr int WS_CP = WS_ML + NWG * 128;
constexpr int WS_P1 = WS_CP + NWG * 128;
constexpr int WS_P2 = WS_P1 + 16 * 16 * 16 * 128;
constexpr int WS_PF = WS_P2 + NRED * NWG * 128;
constexpr int WS_GU = WS_PF + NRED * NFW * 128;
constexpr int WS_PAD = WS_GU + NFW * 2 * 64 * 16;
constexpr int WS_END = WS_PAD + 128;

constexpr int AUX_NT = 2, AUX_SC1 = 16;
constexpr int RSRC_W3 = 0x00020000;
constexpr unsigned SPIN_CAP = 1u << 16;

constexpr int L_FLAG = 0;
enum { F_TAG, F_QRDY, F_XRDY, F_FDONE, F_POLL, F_P1CNT, F_SRDY, F_ORDY, F_PROG, F_STALL, F_TEND, F_SLOPE, F_ENDCNT, F_FINCNT, F_S1CNT };
constexpr int L_MW = 16;
constexpr int L_O = L_MW + 16;
constexpr int L_G = L_O + 16;
constexpr int L_Q = L_G + 32;
constexpr int L_XN = L_Q + 128;
constexpr int L_YF = L_XN + 640;
constexpr int L_FIN = L_YF + 576;
constexpr int L_ACC = L_FIN + 128;
constexpr int L_SC = L_ACC + NSW * 256;
constexpr int L_FLOATS = 24 * 1024;
static_assert(L_SC % 4 == 0 && L_ACC % 4 == 0 && L_Q % 4 == 0 && L_O % 4 == 0 && L_SC + NSW * NT * 32 <= L_FLOATS, "LDS map");
static_assert(16 * NSW * NT * 16 == SK && X_MIN >= 4, "key split");

template <int CTRL> __device__ __forceinline__ float dpp(float x) {
  return __builtin_bit_cast(float, __builtin_amdgcn_mov_dpp(__builtin_bit_cast(int, x), CTRL, 0xf, 0xf, true));
}
template <int CTRL> __device__ __forceinline__ unsigned dppu(unsigned x) {
  return (unsigned)__builtin_amdgcn_mov_dpp((int)x, CTRL, 0xf, 0xf, true);
}
constexpr int XOR1 = 0xB1, XOR2 = 0x4E, XOR7 = 0x141, XOR8 = 0x128, ROR4 = 0x124;
__device__ __forceinline__ float sum_x16(float x) {
  auto s = __builtin_amdgcn_permlane16_swap(__float_as_uint(x), __float_as_uint(x), false, false);
  return __uint_as_float(s[0]) + __uint_as_float(s[1]);
}
__device__ __forceinline__ float sum_x32(float x) {
  auto s = __builtin_amdgcn_permlane32_swap(__float_as_uint(x), __float_as_uint(x), false, false);
  return __uint_as_float(s[0]) + __uint_as_float(s[1]);
}
__device__ __forceinline__ float max_x16(float x) {
  auto s = __builtin_amdgcn_permlane16_swap(__float_as_uint(x), __float_as_uint(x), false, false);
  return fmaxf(__uint_as_float(s[0]), __uint_as_float(s[1]));
}
__device__ __forceinline__ float max_x32(float x) {
  auto s = __builtin_amdgcn_permlane32_swap(__float_as_uint(x), __float_as_uint(x), false, false);
  return fmaxf(__uint_as_float(s[0]), __uint_as_float(s[1]));
}
__device__ __forceinline__ float wave_sum(float x) {
  x += dpp<XOR1>(x); x += dpp<XOR2>(x); x += dpp<ROR4>(x); x += dpp<XOR8>(x);
  return sum_x32(sum_x16(x));
}
__device__ __forceinline__ unsigned pk_bf16(float lo, float hi) {
  bf16x2 v = {(__bf16)lo, (__bf16)hi};
  return __builtin_bit_cast(unsigned, v);
}
__device__ __forceinline__ float rnd(float x) { return (float)(__bf16)x; }
__device__ __forceinline__ float lo_f(unsigned u) { return __uint_as_float(u << 16); }
__device__ __forceinline__ float hi_f(unsigned u) { return __uint_as_float(u & 0xffff0000u); }
__device__ __forceinline__ float dot2(unsigned a, unsigned b, float c) {
  return __builtin_amdgcn_fdot2_f32_bf16(__builtin_bit_cast(bf16x2, a), __builtin_bit_cast(bf16x2, b), c, false);
}
__device__ __forceinline__ __amdgpu_buffer_rsrc_t mk_rsrc(const void* p, int bytes) {
  return __builtin_amdgcn_make_buffer_rsrc((void*)p, 0, bytes, RSRC_W3);
}
__device__ __forceinline__ v4u and4(unsigned x, const unsigned (&m)[4]) { return v4u{x & m[0], x & m[1], x & m[2], x & m[3]}; }
__device__ __forceinline__ f32x4 mfma16(v4u a, v4u bm, f32x4 cacc) {
  return __builtin_amdgcn_mfma_f32_16x16x32_bf16(__builtin_bit_cast(bf16x8, a), __builtin_bit_cast(bf16x8, bm), cacc, 0, 0, 0);
}
__device__ __forceinline__ f32x4 gemv_fin(f32x4 d, int lane) {
  const bool valid = (lane >> 5) == ((lane >> 3) & 1);
#pragma unroll
  for (int r = 0; r < 4; ++r) { float t = valid ? d[r] : 0.f; t += dpp<XOR8>(t); d[r] = sum_x32(t); }
  return d;
}
__device__ __forceinline__ int lds_inc(unsigned addr) {
  int r; unsigned long long save;
  asm volatile("s_mov_b64 %1, exec\n\ts_mov_b64 exec, 1\n\tds_add_rtn_u32 %0, %2, %3\n\ts_mov_b64 exec, %1"
               : "=&v"(r), "=&s"(save) : "v"(addr), "v"(1) : "memory");
  return r;
}
template <int N>
__device__ __forceinline__ void gather(__amdgpu_buffer_rsrc_t rs, int (&off)[N], unsigned tag, float (&val)[2 * N], v4u (&p)[3][N]) {
  auto issue = [&](int j) {
    asm volatile("" ::: "memory");
#pragma unroll
    for (int k = 0; k < N; ++k) p[j][k] = __builtin_amdgcn_raw_buffer_load_b128(rs, off[k], 0, AUX_SC1);
    __builtin_amdgcn_sched_barrier(0);
  };
  auto check = [&](int j) {
    bool done = true;
#pragma unroll
    for (int k = 0; k < N; ++k) {
      const bool ok = off[k] != OOB && ((p[j][k].y ^ tag) | (p[j][k].w ^ tag)) == 0;
      val[2 * k] = ok ? __uint_as_float(p[j][k].x) : val[2 * k]; val[2 * k + 1] = ok ? __uint_as_float(p[j][k].z) : val[2 * k + 1];
      off[k] = ok ? OOB : off[k];
      done = done && off[k] == OOB;
    }
    return __all(done) != 0;
  };
#pragma unroll
  for (int k = 0; k < 2 * N; ++k) val[k] = 0.f;
  issue(0); __builtin_amdgcn_s_sleep(5); issue(1); __builtin_amdgcn_s_sleep(5);
#pragma unroll 1
  for (unsigned spins = 0; spins < SPIN_CAP; ++spins) {
    issue(2); if (check(0)) break;
    issue(0); if (check(1)) break;
    issue(1); if (check(2)) break;
  }
}
template <int N>
__device__ __forceinline__ void gather_end(v4u (&p)[3][N]) {
#pragma unroll
  for (int j = 0; j < 3; ++j)
#pragma unroll
    for (int k = 0; k < N; ++k) asm volatile("" :: "v"(p[j][k]));
}
__device__ __forceinline__ void pace(unsigned& next, unsigned tau, unsigned& stall) {
  int d;
  asm volatile("1:\n\ts_memrealtime vcc\n\ts_waitcnt lgkmcnt(0)\n\ts_lshl_b32 %0, vcc_lo, 4\n\ts_sub_i32 %0, %0, %1\n\ts_cmp_ge_i32 %0, 0\n\ts_cbranch_scc1 2f\n\ts_sleep 1\n\ts_branch 1b\n2:"
               : "=&s"(d) : "s"(next) : "scc", "vcc");
  next += tau + (unsigned)max(0, d - BURST * 16);
  stall += d > STALL * 16 ? d : 0;
}

__global__ void __launch_bounds__(NTHR, 3)
fused_decode(const unsigned short* __restrict__ xg, unsigned char* __restrict__ ws, const unsigned short* __restrict__ wq,
             const unsigned short* __restrict__ kg, const unsigned short* __restrict__ wgate, const unsigned short* __restrict__ wup,
             const unsigned short* __restrict__ wdown, const unsigned short* __restrict__ vg, const unsigned short* __restrict__ wo,
             unsigned short* __restrict__ out) {
  __shared__ __attribute__((aligned(16))) float lds[L_FLOATS];
  asm volatile("" :: "s"(xg), "s"(ws), "s"(wq), "s"(kg), "s"(wgate), "s"(wup), "s"(wdown));
  const int lane = threadIdx.x & 63, pw = __builtin_amdgcn_readfirstlane(threadIdx.x >> 6);
  const int w = pw == 0 ? NW : pw - 1;
  const int b = blockIdx.x, g = b >> 4, s = b & 15;
  const int c = lane & 15;
  const int sr = (s + 3 * g) & 15, ns2 = 5 - (g & 1);
  const bool is1 = sr < 9, is2 = !is1 && sr < 9 + ns2;
  const int f1 = g * 9 + sr, f2 = (g >> 1) * 9 + (g & 1) * 5 + sr - 9;
  unsigned* const ldu = reinterpret_cast<unsigned*>(lds);
  const unsigned lds0 = (unsigned)(uintptr_t)(LAS float*)lds;

  const __amdgpu_buffer_rsrc_t rws = mk_rsrc(ws, WS_END);
  const __amdgpu_buffer_rsrc_t rx = mk_rsrc(xg, DM * 2);

  auto wait_flag = [&](int idx, unsigned want) {
#pragma unroll 1
    for (unsigned spins = 0; spins < (SPIN_CAP << 6); ++spins) {
      if (__builtin_amdgcn_readfirstlane(*(volatile LAS unsigned*)&ldu[L_FLAG + idx]) >= want) break;
      __builtin_amdgcn_s_sleep(1);
    }
    asm volatile("" ::: "memory");
  };
  auto set_flag = [&](int idx) {
    asm volatile("" ::: "memory");
    if (lane == 0) *(volatile LAS unsigned*)&ldu[L_FLAG + idx] = 1u;
  };
  auto count_up = [&](int idx) {
    int r = lds_inc(lds0 + (L_FLAG + idx) * 4);
    asm volatile("s_waitcnt lgkmcnt(0)" : "+v"(r));
  };
  unsigned gm[4];
#pragma unroll
  for (int r = 0; r < 4; ++r) gm[r] = ((lane & 7) >> 1) == r ? ((lane & 1) ? 0xffff0000u : 0x0000ffffu) : 0u;
  unsigned tag;

  if (__builtin_expect(w == NW, 1)) {
    __builtin_amdgcn_s_setprio(3);
    unsigned xd[5], xq[3];
#pragma unroll
    for (int k = 0; k < 5; ++k) xd[k] = __builtin_amdgcn_raw_buffer_load_b32(rx, lane * 4 + k * 256, 0, 0);
#pragma unroll
    for (int k = 0; k < 3; ++k) xq[k] = __builtin_amdgcn_raw_buffer_load_b16(rx, (144 * (s >> 2) + lane + 64 * k) * 2, 0, 0);
    const unsigned epoch = __builtin_amdgcn_raw_buffer_load_b32(rws, lane == 0 ? WS_EPOCH : OOB, 0, AUX_SC1);
    v4u wqv[18];
    {
      const __amdgpu_buffer_rsrc_t rq = mk_rsrc(wq, DM * QD * 2);
      const int o = (144 * (s >> 2) + (lane >> 3)) * (QD * 2) + (g * 4 + (s & 3)) * 128 + (lane & 7) * 16;
#pragma unroll
      for (int i = 0; i < 18; ++i) wqv[i] = __builtin_amdgcn_raw_buffer_load_b128(rq, o, i * 8 * QD * 2, AUX_NT);
    }
    if (lane < 16) ldu[L_FLAG + lane] = 0u;
    asm volatile("s_waitcnt lgkmcnt(0)" ::: "memory");
    __builtin_amdgcn_sched_barrier(0);
    __builtin_amdgcn_s_barrier();
    __builtin_amdgcn_sched_barrier(0);
    float mb, den;
    {
      float xv[10];
#pragma unroll
      for (int k = 0; k < 5; ++k) { xv[2 * k] = lo_f(xd[k]); xv[2 * k + 1] = hi_f(xd[k]); }
      float sm = 0.f;
#pragma unroll
      for (int k = 0; k < 10; ++k) sm += xv[k];
      const float mean = wave_sum(sm) * (1.0f / DM);
      float sq = 0.f;
#pragma unroll
      for (int k = 0; k < 8; ++k) { const float d = xv[k] - mean; sq = fmaf(d, d, sq); }
      const float d8 = lane < 32 ? xv[8] - mean : 0.f, d9 = lane < 32 ? xv[9] - mean : 0.f;
      sq = fmaf(d8, d8, sq); sq = fmaf(d9, d9, sq);
      const float var = wave_sum(sq) * (1.0f / DM);
      mb = rnd(mean);
      den = rnd(sqrtf(rnd(rnd(var) + 1e-5f)));
    }
    unsigned xn[3];
#pragma unroll
    for (int k = 0; k < 3; ++k) { const float v = rnd(lo_f(xq[k]) - mb) / den; xn[k] = pk_bf16(v, v); }
    unsigned xr[18];
#pragma unroll
    for (int i = 0; i < 18; ++i) xr[i] = __shfl(xn[i >> 3], (8 * i + (lane >> 3)) & 63);
#pragma unroll
    for (int i = 0; i < 18; ++i) asm volatile("" : "+v"(xr[i]));
    __builtin_amdgcn_sched_barrier(0);
    f32x4 qa = {0.f, 0.f, 0.f, 0.f};
#pragma unroll
    for (int i = 0; i < 18; ++i) { qa = mfma16(and4(xr[i], gm), wqv[i], qa); __builtin_amdgcn_sched_barrier(0); }
    qa = gemv_fin(qa, lane);
    tag = __builtin_amdgcn_readfirstlane(epoch) + 1u;
    {
      const bool up2 = (lane & 8) != 0;
      const int col = (lane & 7) * 8 + 4 * ((lane >> 4) & 1) + (up2 ? 2 : 0);
      __builtin_amdgcn_raw_buffer_store_b128(v4u{__float_as_uint(up2 ? qa[2] : qa[0]), tag, __float_as_uint(up2 ? qa[3] : qa[1]), tag}, rws,
                                             lane < 32 ? WS_QG + ((g * 4 + (s >> 2)) * 256 + (s & 3) * 64 + col) * 8 : OOB, 0, AUX_SC1);
    }
    if (lane == 0) ldu[L_FLAG + F_TAG] = tag;
    if (is1) {
#pragma unroll
      for (int k = 0; k < 5; ++k) {
        const float v0 = rnd(lo_f(xd[k]) - mb) / den, v1 = rnd(hi_f(xd[k]) - mb) / den;
        *reinterpret_cast<v2u*>(&ldu[L_XN + 2 * (lane + 64 * k)]) = v2u{pk_bf16(v0, v0), pk_bf16(v1, v1)};
      }
      set_flag(F_XRDY);
    }
    for (int i = 0; i < 5; ++i) __builtin_amdgcn_s_sleep(8);
    float q4[4];
    for (unsigned spins = 0;; ++spins) {
      asm volatile("" ::: "memory");
      v4u a[8];
#pragma unroll
      for (int r = 0; r < 4; ++r) {
        a[2 * r] = __builtin_amdgcn_raw_buffer_load_b128(rws, WS_QG + g * 8192 + lane * 32, r * 2048, AUX_SC1);
        a[2 * r + 1] = __builtin_amdgcn_raw_buffer_load_b128(rws, WS_QG + g * 8192 + lane * 32 + 16, r * 2048, AUX_SC1);
      }
      __builtin_amdgcn_sched_barrier(0);
      set_flag(F_POLL);
      __builtin_amdgcn_sched_barrier(0);
      unsigned bad = 0;
#pragma unroll
      for (int j = 0; j < 4; ++j) q4[j] = 0.f;
#pragma unroll
      for (int r = 0; r < 4; ++r) {
        bad |= (a[2 * r].y ^ tag) | (a[2 * r].w ^ tag) | (a[2 * r + 1].y ^ tag) | (a[2 * r + 1].w ^ tag);
        q4[0] += __uint_as_float(a[2 * r].x); q4[1] += __uint_as_float(a[2 * r].z);
        q4[2] += __uint_as_float(a[2 * r + 1].x); q4[3] += __uint_as_float(a[2 * r + 1].z);
      }
      if (__all(bad == 0) || spins > SPIN_CAP) break;
      __builtin_amdgcn_s_sleep(1);
    }
    *reinterpret_cast<v2u*>(&ldu[L_Q + 2 * lane]) = v2u{pk_bf16(q4[0], q4[1]), pk_bf16(q4[2], q4[3])};
    set_flag(F_QRDY);
    __builtin_amdgcn_s_setprio(0);
    float slope = (float)TAU_NOM;
    unsigned hist = 0, prev16 = 0, pstall = 0;
    auto checkpoint = [&](int k, int idx, int span) {
      wait_flag(F_PROG, idx);
      const unsigned t1 = (unsigned)__builtin_amdgcn_s_memrealtime();
      const unsigned stall = __builtin_amdgcn_readfirstlane(*(volatile LAS unsigned*)&ldu[L_FLAG + F_STALL]);
      hist = lane == k ? t1 : hist;
      const unsigned tg = lane <= k ? tag : ~tag;
      __builtin_amdgcn_raw_buffer_store_b128(v4u{hist, tg, hist, tg}, rws, lane < 8 ? WS_CP + b * 128 + lane * 16 : OOB, 0, AUX_SC1);
      wait_flag(F_PROG, idx + CP_D);
      const v2u cp = __builtin_amdgcn_raw_buffer_load_b64(rws, WS_CP + (g * 16 + c) * 128 + k * 16, 0, AUX_SC1);
      float dt = cp.y == tag ? (float)(int)(cp.x - t1) : (float)(CP_D * TAU_NOM / 16);
      dt += dpp<XOR1>(dt); dt += dpp<XOR2>(dt); dt += dpp<ROR4>(dt); dt += dpp<XOR8>(dt);
      const unsigned t16 = (t1 << 4) + (unsigned)(int)dt;
      if (k > 0) {
        const float meas = (float)(int)(t16 - prev16 - (stall - pstall)) / (float)span;
        slope = fminf(meas > slope * KTH ? meas * KUP : fmaxf((float)TAU_NOM, slope * KDN), 2.0f * TAU_NOM);
      }
      prev16 = t16; pstall = stall;
      if (lane == 0) *reinterpret_cast<v2u*>(&ldu[L_FLAG + F_TEND]) = v2u{(t16 + (unsigned)(int)(slope * (float)(2 * NT - idx)) - stall) | 1u, (unsigned)(int)slope};
    };
    checkpoint(0, 12, 0); checkpoint(1, 32, 20); checkpoint(2, 56, 24);
    wait_flag(F_P1CNT, NSW);
    {
      const float mw = lds[L_MW + (lane & 7)];
      float mx = fmaxf(mw, dpp<XOR2>(mw)); mx = fmaxf(mx, dpp<ROR4>(mx));
      float l = 0.f;
#pragma unroll 2
      for (int i = 0; i < NSW * NT / 8; ++i) {
        const f32x4 v = *reinterpret_cast<const f32x4*>(&lds[L_SC + i * 256 + lane * 4]);
        l += (__builtin_amdgcn_exp2f(v[0] - mx) + __builtin_amdgcn_exp2f(v[1] - mx)) + (__builtin_amdgcn_exp2f(v[2] - mx) + __builtin_amdgcn_exp2f(v[3] - mx));
      }
      l += dpp<XOR2>(l); l += dpp<ROR4>(l); l += dpp<XOR8>(l); l = sum_x32(sum_x16(l));
      __builtin_amdgcn_raw_buffer_store_b128(v4u{__float_as_uint(mx), tag, __float_as_uint(l), tag}, rws,
                                             lane < 8 ? WS_ML + b * 128 + lane * 16 : OOB, 0, AUX_SC1);
      wait_flag(F_PROG, ML_AT);
      v4u ml;
      for (unsigned spins = 0;; ++spins) {
        asm volatile("" ::: "memory");
        ml = __builtin_amdgcn_raw_buffer_load_b128(rws, WS_ML + (g * 16 + ((lane >> 1) & 15)) * 128 + (lane & 1) * 16, 0, AUX_SC1);
        if (__all(((ml.y ^ tag) | (ml.w ^ tag)) == 0) || spins > SPIN_CAP) break;
        __builtin_amdgcn_s_sleep(32);
      }
      const float mp = __uint_as_float(ml.x);
      float M = fmaxf(mp, dpp<XOR2>(mp)); M = fmaxf(M, dpp<ROR4>(M)); M = fmaxf(M, dpp<XOR8>(M)); M = max_x16(M);
      float L = __builtin_amdgcn_exp2f(mp - M) * __uint_as_float(ml.z);
      asm volatile("" : "+v"(L));
      L += dpp<XOR2>(L); L += dpp<ROR4>(L); L += dpp<XOR8>(L); L = sum_x16(L);
      if (lane < 8) lds[L_MW + lane] = __builtin_amdgcn_exp2f(mw - M) / L;
      set_flag(F_SRDY);
    }
    checkpoint(3, 84, 28);
    if (is2) {
      wait_flag(F_PROG, NEAR_AT);
      wait_flag(F_FDONE, 3);
#pragma unroll
      for (int k = 0; k < 5; ++k) {
        const int t = k * 64 + lane;
        const f32x2 yf = *reinterpret_cast<const f32x2*>(&lds[L_YF + 2 * (t < 288 ? t : 0)]);
        __builtin_amdgcn_raw_buffer_store_b128(v4u{__float_as_uint(yf.x), tag, __float_as_uint(yf.y), tag}, rws,
                                               t < 288 ? WS_PF + ((t >> 3) * NFW + f2) * 128 + (t & 7) * 16 : OOB, 0, AUX_SC1);
      }
    }
    __builtin_amdgcn_s_setprio(3);
    wait_flag(F_ENDCNT, NSW);
    for (int i = 0; i < 3; ++i) __builtin_amdgcn_s_sleep(4);
    {
      int o2[2] = {WS_P1 + (g * 16 + s) * 2048 + lane * 16, WS_P1 + (g * 16 + s) * 2048 + 1024 + lane * 16};
      float pv4[4];
      v4u pl[3][2];
      gather<2>(rws, o2, tag, pv4, pl);
      auto h0 = __builtin_amdgcn_permlane32_swap(__float_as_uint(pv4[0] + pv4[2]), __float_as_uint(pv4[1] + pv4[3]), false, false);
      float v = sum_x16(__uint_as_float(h0[0]) + __uint_as_float(h0[1]));
      v += dpp<XOR8>(v);
      if ((lane & 24) == 0) lds[L_O + 2 * (lane & 7) + (lane >> 5)] = rnd(v);
      set_flag(F_ORDY);
      gather_end<2>(pl);
    }
    return;
  }

  const int nrole = is1 ? 4 : is2 ? 3 : 0;
  const int voff = lane * 16;
  __builtin_amdgcn_sched_barrier(0);
  __builtin_amdgcn_s_barrier();
  __builtin_amdgcn_sched_barrier(0);

  if (w >= NSW && w < NSW + nrole) {
    const int fw = w - NSW;
    v4u fr[24];
    if (is1) {
      const __amdgpu_buffer_rsrc_t rfg = mk_rsrc(wgate, DM * FF * 2);
      const __amdgpu_buffer_rsrc_t rfu = mk_rsrc(wup, DM * FF * 2);
      const int row0 = 288 * (f1 & 1) + 72 * fw + (lane >> 3);
      const int foff = row0 * (FF * 2) + (f1 >> 1) * 128 + (lane & 7) * 16;
#pragma unroll
      for (int i = 0; i < 9; ++i) {
        fr[2 * i] = __builtin_amdgcn_raw_buffer_load_b128(rfg, foff, i * 8 * FF * 2, AUX_NT);
        fr[2 * i + 1] = __builtin_amdgcn_raw_buffer_load_b128(rfu, foff, i * 8 * FF * 2, AUX_NT);
        __builtin_amdgcn_sched_barrier(0); asm volatile("s_waitcnt vmcnt(6)"); __builtin_amdgcn_sched_barrier(0);
      }
      wait_flag(F_XRDY, 1);
      tag = ldu[L_FLAG + F_TAG];
      f32x4 ag = {0.f, 0.f, 0.f, 0.f}, au = {0.f, 0.f, 0.f, 0.f};
#pragma unroll
      for (int i = 0; i < 9; ++i) {
        const v4u av = and4(ldu[L_XN + row0 + 8 * i], gm);
        ag = mfma16(av, fr[2 * i], ag); au = mfma16(av, fr[2 * i + 1], au);
      }
      ag = gemv_fin(ag, lane); au = gemv_fin(au, lane);
      if ((lane & 8) == 0) *reinterpret_cast<f32x4*>(&lds[L_YF + fw * 128 + (lane >> 5) * 64 + (lane & 7) * 8 + 4 * ((lane >> 4) & 1)]) = lane < 32 ? ag : au;
      int old = lds_inc(lds0 + (L_FLAG + F_S1CNT) * 4);
      float gs = 0.f, us = 0.f;
#pragma unroll
      for (int ww = 0; ww < 4; ++ww) { gs += lds[L_YF + ww * 128 + lane]; us += lds[L_YF + ww * 128 + 64 + lane]; }
      asm volatile("s_waitcnt lgkmcnt(0)" : "+v"(old), "+v"(gs), "+v"(us));
      if (__builtin_amdgcn_readfirstlane(old) == 3)
        __builtin_amdgcn_raw_buffer_store_b128(v4u{__float_as_uint(gs), tag, __float_as_uint(us), tag}, rws, WS_GU + (f1 * 64 + lane) * 16, 0, AUX_SC1);
    } else {
      const __amdgpu_buffer_rsrc_t rwd = mk_rsrc(wdown + (size_t)f2 * 64 * DM, 64 * DM * 2);
      const int doff = (lane >> 3) * (DM * 2) + fw * 384 + (lane & 7) * 16;
#pragma unroll
      for (int rg = 0; rg < 8; ++rg) {
#pragma unroll
        for (int k = 0; k < 3; ++k) fr[rg * 3 + k] = __builtin_amdgcn_raw_buffer_load_b128(rwd, doff + k * 128, rg * 8 * DM * 2, AUX_NT);
        __builtin_amdgcn_sched_barrier(0); asm volatile("s_waitcnt vmcnt(7)"); __builtin_amdgcn_sched_barrier(0);
      }
      wait_flag(F_QRDY, 1);
      tag = ldu[L_FLAG + F_TAG];
      for (int i = 0; i < 8; ++i) __builtin_amdgcn_s_sleep(8);
      v4u p0, p1;
      for (unsigned spins = 0;; ++spins) {
        asm volatile("" ::: "memory");
        p0 = __builtin_amdgcn_raw_buffer_load_b128(rws, WS_GU + (f2 * 128 + lane) * 16, 0, AUX_SC1);
        p1 = __builtin_amdgcn_raw_buffer_load_b128(rws, WS_GU + (f2 * 128 + lane) * 16, 1024, AUX_SC1);
        if (__all(((p0.y ^ tag) | (p0.w ^ tag) | (p1.y ^ tag) | (p1.w ^ tag)) == 0) || spins > SPIN_CAP) break;
        __builtin_amdgcn_s_sleep(16);
      }
      const float gt = rnd(__uint_as_float(p0.x) + __uint_as_float(p1.x)), up = rnd(__uint_as_float(p0.z) + __uint_as_float(p1.z));
      const float inner = 0.7978845608028654f * (gt + 0.044715f * gt * gt * gt);
      const float hv = rnd(0.5f * gt * (1.0f + tanhf(inner))) * up;
      const unsigned hp = pk_bf16(hv, hv);
      f32x4 ya[3];
#pragma unroll
      for (int k = 0; k < 3; ++k) ya[k] = f32x4{0.f, 0.f, 0.f, 0.f};
#pragma unroll
      for (int rg = 0; rg < 8; ++rg) {
        const v4u av = and4(__shfl(hp, rg * 8 + (lane >> 3)), gm);
#pragma unroll
        for (int k = 0; k < 3; ++k) ya[k] = mfma16(av, fr[rg * 3 + k], ya[k]);
      }
#pragma unroll
      for (int k = 0; k < 3; ++k) {
        ya[k] = gemv_fin(ya[k], lane);
        if (lane < 32 && (lane & 8) == 0) *reinterpret_cast<f32x4*>(&lds[L_YF + fw * 192 + k * 64 + (lane & 7) * 8 + 4 * (lane >> 4)]) = ya[k];
      }
      count_up(F_FDONE);
    }
  } else if (w < NSW) {
    const __amdgpu_buffer_rsrc_t rk = mk_rsrc(kg + (size_t)g * SK * 128, SK * 256);
    const __amdgpu_buffer_rsrc_t rv = mk_rsrc(vg + (size_t)g * SK * 128, SK * 256);
    const int ufirst = s * (NSW * NT / 2) + w;
#define TOFF(t) ((ufirst + ((t) >> 1) * NSW) << 13)
#define VOFF(t) ((t) < NT ? TOFF(t) + ((t) & 1) * 4096 : OOB)
#define tb(n, i) buf[(n) * TL + (i)]
    v4u buf[4 * TL];
#pragma unroll
    for (int i = 0; i < 2 * TL; ++i) buf[i] = __builtin_amdgcn_raw_buffer_load_b128(rk, voff + i * 1024, TOFF(0), AUX_NT);
    __builtin_amdgcn_sched_barrier(0);
    wait_flag(F_POLL, 1);
#pragma unroll
    for (int i = 0; i < 2 * TL; ++i) buf[2 * TL + i] = __builtin_amdgcn_raw_buffer_load_b128(rk, voff + i * 1024, TOFF(2), AUX_NT);
    __builtin_amdgcn_sched_barrier(0);
    wait_flag(F_QRDY, 1);
    const int hf = (c >> 2) & 1;
    const v4u qf = *reinterpret_cast<const v4u*>(&ldu[L_Q + hf * 64 + 4 * c]);
    const v4u qs = *reinterpret_cast<const v4u*>(&ldu[L_Q + (hf ^ 1) * 64 + 4 * c]);
    constexpr float LOG2E = 1.4426950408889634f, SCALE = 0.08838834764831845f;
    const bool b0 = (c & 1) != 0, b1 = (c & 2) != 0;
    float* const scw = &lds[L_SC + w * NT * 32 + (lane >> 4) * 8 + (lane & 7)];
    float m = -INFINITY;
    float y[4];
    auto qk1 = [&](const v4u* kt) {
#pragma unroll
      for (int i = 0; i < TL; ++i) {
        float xf = 0.f, xs = 0.f;
#pragma unroll
        for (int j = 0; j < 4; ++j) { xf = dot2(kt[i][j], qf[j], xf); xs = dot2(kt[i][j], qs[j], xs); }
        y[i] = xf + dpp<XOR7>(xs);
      }
      asm volatile("" : "+v"(y[0]), "+v"(y[1]), "+v"(y[2]), "+v"(y[3]));
      __builtin_amdgcn_sched_barrier(0);
    };
    auto qk2 = [&](int t) {
      const float u0 = (b0 ? y[1] : y[0]) + dpp<XOR1>(b0 ? y[0] : y[1]);
      const float u1 = (b0 ? y[3] : y[2]) + dpp<XOR1>(b0 ? y[2] : y[3]);
      float sc = (b1 ? u1 : u0) + dpp<XOR2>(b1 ? u0 : u1);
      sc += dpp<XOR8>(sc);
      const float tl2 = rnd(rnd(sc) * SCALE) * LOG2E;
      m = fmaxf(m, tl2);
      scw[t * 32] = tl2;
    };
#pragma unroll
    for (int n = 0; n < 2; ++n) {
      qk1(&tb(n, 0));
#pragma unroll
      for (int i = 0; i < TL; ++i) tb(n, i) = __builtin_amdgcn_raw_buffer_load_b128(rk, voff + i * 1024, TOFF(4) + n * 4096, AUX_NT);
      __builtin_amdgcn_sched_barrier(0);
      qk2(n);
    }
    qk1(&tb(2, 0)); qk2(2);
    qk1(&tb(3, 0)); qk2(3);
    unsigned next = (unsigned)__builtin_amdgcn_s_memrealtime() << 4, tau = TAU_NOM;
    unsigned stall = 0, tend = 0;
    int slope = TAU_NOM;
    auto aim = [&](int done) {
      if (w == 0 && lane == 0) *(volatile LAS v2u*)&ldu[L_FLAG + F_PROG] = v2u{(unsigned)done, stall};
      if (done & 2) return;
      const v2u te = *(volatile LAS v2u*)&ldu[L_FLAG + F_TEND];
      const unsigned t0 = __builtin_amdgcn_readfirstlane(te.x);
      if (t0 == 0) return;
      tend = t0; slope = __builtin_amdgcn_readfirstlane(te.y);
      const int v = __builtin_amdgcn_readfirstlane((int)((float)(int)(tend + stall - next) * __builtin_amdgcn_rcpf((float)(2 * NT - done))));
      tau = (unsigned)min(slope + TAU_UP, max(slope - TAU_DN, v));
    };
#pragma unroll 1
    for (int t = 4; t < NT; t += 2) {
      const bool lastk = t + 2 >= NT;
      const __amdgpu_buffer_rsrc_t rn = lastk ? rv : rk;
      const int nxt = TOFF(lastk ? 0 : t + 2);
      if (t <= 12) stall = 0;
      aim(t);
#pragma unroll
      for (int n = 0; n < 2; ++n) {
        qk1(&tb(n, 0));
        pace(next, tau, stall);
#pragma unroll
        for (int i = 0; i < TL; ++i) tb(n, i) = __builtin_amdgcn_raw_buffer_load_b128(rn, voff + i * 1024, nxt + n * 4096, AUX_NT);
        __builtin_amdgcn_sched_barrier(0);
        qk2(t + n);
      }
    }
    m = fmaxf(m, dpp<XOR1>(m)); m = fmaxf(m, dpp<XOR2>(m));
    m = max_x32(max_x16(m));
    if ((lane & 59) == 0) lds[L_MW + 2 * w + (lane >> 2)] = m;
    count_up(F_P1CNT);
    f32x4 acc = {0.f, 0.f, 0.f, 0.f};
    unsigned amask[4];
    {
      const int jp = (c & 3) + 4 * (c >> 3);
#pragma unroll
      for (int r = 0; r < 4; ++r) amask[r] = (jp >> 1) == r ? ((jp & 1) ? 0xffff0000u : 0x0000ffffu) : 0u;
    }
    auto pv = [&](float sv, const v4u* vt) {
      const float p = __builtin_amdgcn_exp2f(sv - m);
      const unsigned pp = pk_bf16(p, p);
#pragma unroll
      for (int i = 0; i < TL; ++i) {
        const unsigned pb = i == 0 ? dppu<0x00>(pp) : i == 1 ? dppu<0x55>(pp) : i == 2 ? dppu<0xAA>(pp) : dppu<0xFF>(pp);
        acc = mfma16(and4(pb, amask), vt[i], acc);
      }
      asm volatile("" : "+v"(acc));
      __builtin_amdgcn_sched_barrier(0);
    };
    int t = 0;
    auto vrun = [&](int lim) {
#pragma unroll 1
      for (; t < lim; t += 2) {
        aim(NT + t);
        const int nxt = TOFF(t + 2);
        const float s0 = scw[t * 32], s1 = scw[t * 32 + 32];
#pragma unroll
        for (int n = 0; n < 2; ++n) {
          pv(n ? s1 : s0, &tb(n, 0));
          pace(next, tau, stall);
#pragma unroll
          for (int i = 0; i < TL; ++i) tb(n, i) = __builtin_amdgcn_raw_buffer_load_b128(rv, voff + i * 1024, nxt + n * 4096, AUX_NT);
          __builtin_amdgcn_sched_barrier(0);
        }
      }
    };
    vrun(NT - X_MAX);
    {
      const int late = min(1 << 16, max(0, (int)(next + X_MAX * (slope - TAU_FAST) - tend - stall)));
      vrun(NT - min(X_MAX, X_MIN + 2 * ((late * X_C) >> 15)));
    }
#pragma unroll
    for (int k = 2; k < 4; ++k) {
#pragma unroll
      for (int i = 0; i < TL; ++i) tb(k, i) = __builtin_amdgcn_raw_buffer_load_b128(rv, voff + i * 1024, TOFF(t + 2) + (k & 1) * 4096, AUX_NT);
      __builtin_amdgcn_sched_barrier(0);
    }
    __builtin_amdgcn_s_setprio(2);
    wait_flag(F_SRDY, 1);
    const float ed = lds[L_MW + 2 * w + ((lane >> 4) & 1)];
    tag = ldu[L_FLAG + F_TAG];
    const int po = WS_P1 + ((g * 16 + (lane >> 3)) * 16 + s) * 128 + (lane & 7) * 16;
#pragma unroll 1
    for (; t + 4 < NT; t += 4) {
      if (w == 0 && lane == 0) *(volatile LAS int*)&ldu[L_FLAG + F_PROG] = NT + t + 2;
#pragma unroll
      for (int k = 0; k < 4; ++k) {
        const int o = VOFF(t + k + 4);
        pv(scw[(t + k) * 32], &tb(k, 0));
#pragma unroll
        for (int i = 0; i < TL; ++i) tb(k, i) = __builtin_amdgcn_raw_buffer_load_b128(rv, voff + i * 1024, o, AUX_NT);
        __builtin_amdgcn_sched_barrier(0);
      }
    }
    {
      if (w == 0 && lane == 0) *(volatile LAS int*)&ldu[L_FLAG + F_PROG] = 126;
      v4u av[4][TL];
#pragma unroll
      for (int k = 0; k < 4; ++k) {
        const float p = __builtin_amdgcn_exp2f((t + k < NT ? scw[(t + k) * 32] : -INFINITY) - m);
        const unsigned pp = pk_bf16(p, p);
        av[k][0] = and4(dppu<0x00>(pp), amask); av[k][1] = and4(dppu<0x55>(pp), amask);
        av[k][2] = and4(dppu<0xAA>(pp), amask); av[k][3] = and4(dppu<0xFF>(pp), amask);
        asm volatile("" : "+v"(av[k][0]), "+v"(av[k][1]), "+v"(av[k][2]), "+v"(av[k][3]));
      }
      __builtin_amdgcn_sched_barrier(0);
#pragma unroll
      for (int k = 0; k < 2; ++k)
#pragma unroll
        for (int i = 0; i < TL; ++i) acc = mfma16(av[k][i], tb(k, i), acc);
      if (t + 2 < NT) {
#pragma unroll
        for (int k = 2; k < 4; ++k)
#pragma unroll
          for (int i = 0; i < TL; ++i) acc = mfma16(av[k][i], tb(k, i), acc);
      }
    }
    *reinterpret_cast<f32x4*>(&lds[L_ACC + w * 256 + ((lane >> 4) & 1) * 128 + 8 * c + 4 * (lane >> 5)]) = acc * ed;
    int old = lds_inc(lds0 + (L_FLAG + F_ENDCNT) * 4);
    f32x2 o0 = {0.f, 0.f}, o1 = {0.f, 0.f};
#pragma unroll
    for (int ww = 0; ww < NSW; ++ww) { o0 += *reinterpret_cast<const f32x2*>(&lds[L_ACC + ww * 256 + 2 * lane]); o1 += *reinterpret_cast<const f32x2*>(&lds[L_ACC + ww * 256 + 128 + 2 * lane]); }
    asm volatile("s_waitcnt lgkmcnt(0)" : "+v"(old), "+v"(o0), "+v"(o1));
    if (__builtin_amdgcn_readfirstlane(old) == NSW - 1) {
      __builtin_amdgcn_raw_buffer_store_b128(v4u{__float_as_uint(o0[0]), tag, __float_as_uint(o0[1]), tag}, rws, po, 0, AUX_SC1);
      __builtin_amdgcn_raw_buffer_store_b128(v4u{__float_as_uint(o1[0]), tag, __float_as_uint(o1[1]), tag}, rws, po, 8 * 2048, AUX_SC1);
    }
  }

  if (w >= NSW) {
    const int t = (w - NSW) * 64 + lane;
    const __amdgpu_buffer_rsrc_t rwo = mk_rsrc(wo + (size_t)b * 16 * DM, 16 * DM * 2);
    if (w == NW - 1) {
      const unsigned t0 = __builtin_amdgcn_raw_buffer_load_b32(mk_rsrc(vg + (size_t)g * SK * 128, SK * 256), lane < 2 ? (s << 20) + lane * 0xffffc : OOB, 0, AUX_NT);
      const unsigned t1 = __builtin_amdgcn_raw_buffer_load_b32(rwo, lane == 0 ? 0 : OOB, 0, AUX_NT);
      const unsigned t2 = __builtin_amdgcn_raw_buffer_load_b32(mk_rsrc(out, DM * 2), lane == 0 && b < NRED ? b * 32 : OOB, 0, AUX_SC1);
      const unsigned t3 = __builtin_amdgcn_raw_buffer_load_b32(rws, lane == 0 ? WS_PAD : OOB, 0, AUX_NT);
      asm volatile("" :: "v"(t0), "v"(t1), "v"(t2), "v"(t3));
    }
#pragma unroll 1
    for (unsigned spins = 0; spins < (SPIN_CAP << 5); ++spins) {
      if (__builtin_amdgcn_readfirstlane(*(volatile LAS unsigned*)&ldu[L_FLAG + F_ENDCNT]) >= 1u) break;
      __builtin_amdgcn_s_sleep(2);
    }
    float wf[32];
    {
      unsigned wod[16];
#pragma unroll
      for (int j = 0; j < 16; ++j) wod[j] = __builtin_amdgcn_raw_buffer_load_b32(rwo, t < 288 ? t * 4 : OOB, j * DM * 2, AUX_NT);
#pragma unroll
      for (int j = 0; j < 16; ++j) { wf[2 * j] = lo_f(wod[j]); wf[2 * j + 1] = hi_f(wod[j]); }
#pragma unroll
      for (int j = 0; j < 32; ++j) asm volatile("" : "+v"(wf[j]));
    }
    tag = ldu[L_FLAG + F_TAG];
    const int po = t < 288 ? WS_P2 + ((t >> 3) * NWG + b) * 128 + (t & 7) * 16 : OOB;
    f32x4 o4[4];
#pragma unroll 1
    for (unsigned spins = 0; spins < (SPIN_CAP << 8); ++spins) {
      unsigned f = *(volatile LAS unsigned*)&ldu[L_FLAG + F_ORDY];
      asm volatile("" ::: "memory");
#pragma unroll
      for (int k = 0; k < 4; ++k) o4[k] = *reinterpret_cast<const f32x4*>(&lds[L_O + 4 * k]);
      asm volatile("" : "+v"(o4[0]), "+v"(o4[1]), "+v"(o4[2]), "+v"(o4[3]) :: "memory");
      if (__builtin_amdgcn_readfirstlane(f) != 0u) break;
    }
    f32x2 ya = {0.f, 0.f}, yb = {0.f, 0.f};
#pragma unroll
    for (int j = 0; j < 16; j += 2) {
      ya += o4[j >> 2][j & 3] * f32x2{wf[2 * j], wf[2 * j + 1]};
      yb += o4[j >> 2][(j & 3) + 1] * f32x2{wf[2 * j + 2], wf[2 * j + 3]};
    }
    __builtin_amdgcn_raw_buffer_store_b128(v4u{__float_as_uint(ya.x + yb.x), tag, __float_as_uint(ya.y + yb.y), tag}, rws, po, 0, AUX_SC1);
  }

  if (b < NRED && w < 8) {
    if (w == 0) {
      const unsigned xp = __builtin_amdgcn_raw_buffer_load_b32(rx, lane < 8 ? b * 32 + lane * 4 : OOB, 0, 0);
      float f0, f1v;
      for (unsigned spins = 0;; ++spins) {
        asm volatile("" ::: "memory");
        v4u pf[9];
#pragma unroll
        for (int k = 0; k < 9; ++k) pf[k] = __builtin_amdgcn_raw_buffer_load_b128(rws, WS_PF + b * NFW * 128 + lane * 16, k * 1024, AUX_SC1);
        unsigned bad = 0; f0 = 0.f; f1v = 0.f;
#pragma unroll
        for (int k = 0; k < 9; ++k) { bad |= (pf[k].y ^ tag) | (pf[k].w ^ tag); f0 += __uint_as_float(pf[k].x); f1v += __uint_as_float(pf[k].z); }
        if (__all(bad == 0) || spins > SPIN_CAP) break;
        __builtin_amdgcn_s_sleep(8);
      }
      f0 += dpp<XOR8>(f0); f0 = rnd(sum_x32(sum_x16(f0)));
      f1v += dpp<XOR8>(f1v); f1v = rnd(sum_x32(sum_x16(f1v)));
      if (lane < 8) {
        *reinterpret_cast<f32x2*>(&lds[L_G + lane * 2]) = f32x2{rnd(f0 / (1.0f + __expf(-f0))), rnd(f1v / (1.0f + __expf(-f1v)))};
        ldu[L_G + 16 + lane] = xp;
      }
    }
    if (w < NSW) wait_flag(F_ORDY, 1);
    for (int i = 0; i < 4; ++i) __builtin_amdgcn_s_sleep(4);
    int o4[4];
    float sv[8];
#pragma unroll
    for (int k = 0; k < 4; ++k) o4[k] = WS_P2 + b * NWG * 128 + (w * 64 + lane) * 16 + k * 8192;
    v4u pl[3][4];
    gather<4>(rws, o4, tag, sv, pl);
    float s0 = (sv[0] + sv[2]) + (sv[4] + sv[6]), s1 = (sv[1] + sv[3]) + (sv[5] + sv[7]);
    s0 += dpp<XOR8>(s0); s0 = sum_x32(sum_x16(s0));
    s1 += dpp<XOR8>(s1); s1 = sum_x32(sum_x16(s1));
    if (lane < 8) *reinterpret_cast<f32x2*>(&lds[L_FIN + w * 16 + lane * 2]) = f32x2{s0, s1};
    int old = lds_inc(lds0 + (L_FLAG + F_FINCNT) * 4);
    f32x2 t2 = *reinterpret_cast<const f32x2*>(&lds[L_FIN + lane * 2]);
    f32x2 gv = *reinterpret_cast<const f32x2*>(&lds[L_G + (lane & 7) * 2]);
    unsigned xp = ldu[L_G + 16 + (lane & 7)];
    asm volatile("s_waitcnt lgkmcnt(0)" : "+v"(old), "+v"(t2), "+v"(gv), "+v"(xp));
    if (__builtin_amdgcn_readfirstlane(old) == 7) {
      t2.x += dpp<XOR8>(t2.x); t2.x = sum_x32(sum_x16(t2.x));
      t2.y += dpp<XOR8>(t2.y); t2.y = sum_x32(sum_x16(t2.y));
      const unsigned r = pk_bf16(lo_f(xp) + rnd(rnd(t2.x) * gv.x), hi_f(xp) + rnd(rnd(t2.y) * gv.y));
      __builtin_amdgcn_raw_buffer_store_b32(r, mk_rsrc(out, DM * 2), lane < 8 ? b * 32 + lane * 4 : OOB, 0, 0);
      if (b == 0 && lane == 0) __builtin_amdgcn_raw_buffer_store_b32(tag, rws, WS_EPOCH, 0, AUX_SC1);
    }
    gather_end<4>(pl);
  }
}

}

extern "C" void kernel_launch(void* const* d_in, const int* in_sizes, int n_in, void* d_out, int out_size, void* d_ws, size_t ws_size,
                              hipStream_t stream) {
  (void)in_sizes; (void)n_in; (void)out_size; (void)ws_size;
  fused_decode<<<NWG, NTHR, 0, stream>>>((const unsigned short*)d_in[0], (unsigned char*)d_ws, (const unsigned short*)d_in[3],
                                         (const unsigned short*)d_in[1], (const unsigned short*)d_in[6], (const unsigned short*)d_in[7],
                                         (const unsigned short*)d_in[8], (const unsigned short*)d_in[2], (const unsigned short*)d_in[4],
                                         (unsigned short*)d_out);
}
```

```cpp
#include <hip/hip_runtime.h>
#include <hip/hip_bf16.h>
#include <stdint.h>

namespace {

typedef unsigned v4u __attribute__((ext_vector_type(4)));
typedef unsigned v2u __attribute__((ext_vector_type(2)));
typedef float f32x4 __attribute__((ext_vector_type(4)));
typedef float f32x2 __attribute__((ext_vector_type(2)));
typedef __bf16 bf16x8 __attribute__((ext_vector_type(8)));
typedef __bf16 bf16x2 __attribute__((ext_vector_type(2)));
#define LAS __attribute__((address_space(3)))

constexpr int DM = 576, SK = 65536, FF = 4608, QD = 4096;
constexpr int NWG = 256;
constexpr int NSW = 4;
constexpr int NW = 9;
constexpr int NTHR = (NW + 1) * 64;
constexpr int NRED = 36;
constexpr int NFW = 72;
constexpr int TL = 4;
constexpr int NT = 64;
constexpr int OOB = 0x70000000;

#define KNOM 882
#define KCPS 4
#define KPAT 1
#define KUP 1.010f
#define KDN 0.995f
#define KTH 1.004f
constexpr int TAU_NOM = KNOM;
constexpr int TAU_DN = 35, TAU_UP = 22, TAU_FAST = 20;
constexpr int BURST = 20, STALL = 100;
constexpr int CP_D = 6;
constexpr int ML_AT = 74;
constexpr int X_MIN = 4, X_MAX = 20, X_C = 68;
constexpr int NEAR_AT = 124;

constexpr int WS_EPOCH = 0;
constexpr int WS_QG = 4096;
constexpr int WS_ML = WS_QG + 16 * 4 * 256 * 8;
constexpr int WS_CP = WS_ML + NWG * 128;
constexpr int WS_P1 = WS_CP + NWG * 128;
constexpr int WS_P2 = WS_P1 + 16 * 16 * 16 * 128;
constexpr int WS_PF = WS_P2 + NRED * NWG * 128;
constexpr int WS_GU = WS_PF + NRED * NFW * 128;
constexpr int WS_PAD = WS_GU + NFW * 2 * 64 * 16;
constexpr int WS_END = WS_PAD + 128;

constexpr int AUX_NT = 2, AUX_SC1 = 16;
constexpr int RSRC_W3 = 0x00020000;
constexpr unsigned SPIN_CAP = 1u << 16;

constexpr int L_FLAG = 0;
enum { F_TAG, F_QRDY, F_XRDY, F_FDONE, F_POLL, F_P1CNT, F_SRDY, F_ORDY, F_PROG, F_STALL, F_TEND, F_SLOPE, F_ENDCNT, F_FINCNT, F_S1CNT };
constexpr int L_MW = 16;
constexpr int L_O = L_MW + 16;
constexpr int L_G = L_O + 16;
constexpr int L_Q = L_G + 32;
constexpr int L_XN = L_Q + 128;
constexpr int L_YF = L_XN + 640;
constexpr int L_FIN = L_YF + 576;
constexpr int L_ACC = L_FIN + 128;
constexpr int L_SC = L_ACC + NSW * 256;
constexpr int L_FLOATS = 24 * 1024;
static_assert(L_SC % 4 == 0 && L_ACC % 4 == 0 && L_Q % 4 == 0 && L_O % 4 == 0 && L_SC + NSW * NT * 32 <= L_FLOATS, "LDS map");
static_assert(16 * NSW * NT * 16 == SK && X_MIN >= 4, "key split");

template <int CTRL> __device__ __forceinline__ float dpp(float x) {
  return __builtin_bit_cast(float, __builtin_amdgcn_mov_dpp(__builtin_bit_cast(int, x), CTRL, 0xf, 0xf, true));
}
template <int CTRL> __device__ __forceinline__ unsigned dppu(unsigned x) {
  return (unsigned)__builtin_amdgcn_mov_dpp((int)x, CTRL, 0xf, 0xf, true);
}
constexpr int XOR1 = 0xB1, XOR2 = 0x4E, XOR7 = 0x141, XOR8 = 0x128, ROR4 = 0x124;
__device__ __forceinline__ float sum_x16(float x) {
  auto s = __builtin_amdgcn_permlane16_swap(__float_as_uint(x), __float_as_uint(x), false, false);
  return __uint_as_float(s[0]) + __uint_as_float(s[1]);
}
__device__ __forceinline__ float sum_x32(float x) {
  auto s = __builtin_amdgcn_permlane32_swap(__float_as_uint(x), __float_as_uint(x), false, false);
  return __uint_as_float(s[0]) + __uint_as_float(s[1]);
}
__device__ __forceinline__ float max_x16(float x) {
  auto s = __builtin_amdgcn_permlane16_swap(__float_as_uint(x), __float_as_uint(x), false, false);
  return fmaxf(__uint_as_float(s[0]), __uint_as_float(s[1]));
}
__device__ __forceinline__ float max_x32(float x) {
  auto s = __builtin_amdgcn_permlane32_swap(__float_as_uint(x), __float_as_uint(x), false, false);
  return fmaxf(__uint_as_float(s[0]), __uint_as_float(s[1]));
}
__device__ __forceinline__ float wave_sum(float x) {
  x += dpp<XOR1>(x); x += dpp<XOR2>(x); x += dpp<ROR4>(x); x += dpp<XOR8>(x);
  return sum_x32(sum_x16(x));
}
__device__ __forceinline__ unsigned pk_bf16(float lo, float hi) {
  bf16x2 v = {(__bf16)lo, (__bf16)hi};
  return __builtin_bit_cast(unsigned, v);
}
__device__ __forceinline__ float rnd(float x) { return (float)(__bf16)x; }
__device__ __forceinline__ float lo_f(unsigned u) { return __uint_as_float(u << 16); }
__device__ __forceinline__ float hi_f(unsigned u) { return __uint_as_float(u & 0xffff0000u); }
__device__ __forceinline__ float dot2(unsigned a, unsigned b, float c) {
  return __builtin_amdgcn_fdot2_f32_bf16(__builtin_bit_cast(bf16x2, a), __builtin_bit_cast(bf16x2, b), c, false);
}
__device__ __forceinline__ __amdgpu_buffer_rsrc_t mk_rsrc(const void* p, int bytes) {
  return __builtin_amdgcn_make_buffer_rsrc((void*)p, 0, bytes, RSRC_W3);
}
__device__ __forceinline__ v4u and4(unsigned x, const unsigned (&m)[4]) { return v4u{x & m[0], x & m[1], x & m[2], x & m[3]}; }
__device__ __forceinline__ f32x4 mfma16(v4u a, v4u bm, f32x4 cacc) {
  return __builtin_amdgcn_mfma_f32_16x16x32_bf16(__builtin_bit_cast(bf16x8, a), __builtin_bit_cast(bf16x8, bm), cacc, 0, 0, 0);
}
__device__ __forceinline__ f32x4 gemv_fin(f32x4 d, int lane) {
  const bool valid = (lane >> 5) == ((lane >> 3) & 1);
#pragma unroll
  for (int r = 0; r < 4; ++r) { float t = valid ? d[r] : 0.f; t += dpp<XOR8>(t); d[r] = sum_x32(t); }
  return d;
}
__device__ __forceinline__ int lds_inc(unsigned addr) {
  int r; unsigned long long save;
  asm volatile("s_mov_b64 %1, exec\n\ts_mov_b64 exec, 1\n\tds_add_rtn_u32 %0, %2, %3\n\ts_mov_b64 exec, %1"
               : "=&v"(r), "=&s"(save) : "v"(addr), "v"(1) : "memory");
  return r;
}
template <int N>
__device__ __forceinline__ void gather(__amdgpu_buffer_rsrc_t rs, int (&off)[N], unsigned tag, float (&val)[2 * N], v4u (&p)[3][N]) {
  auto issue = [&](int j) {
    asm volatile("" ::: "memory");
#pragma unroll
    for (int k = 0; k < N; ++k) p[j][k] = __builtin_amdgcn_raw_buffer_load_b128(rs, off[k], 0, AUX_SC1);
    __builtin_amdgcn_sched_barrier(0);
  };
  auto check = [&](int j) {
    bool done = true;
#pragma unroll
    for (int k = 0; k < N; ++k) {
      const bool ok = off[k] != OOB && ((p[j][k].y ^ tag) | (p[j][k].w ^ tag)) == 0;
      val[2 * k] = ok ? __uint_as_float(p[j][k].x) : val[2 * k]; val[2 * k + 1] = ok ? __uint_as_float(p[j][k].z) : val[2 * k + 1];
      off[k] = ok ? OOB : off[k];
      done = done && off[k] == OOB;
    }
    return __all(done) != 0;
  };
#pragma unroll
  for (int k = 0; k < 2 * N; ++k) val[k] = 0.f;
  issue(0); __builtin_amdgcn_s_sleep(5); issue(1); __builtin_amdgcn_s_sleep(5);
#pragma unroll 1
  for (unsigned spins = 0; spins < SPIN_CAP; ++spins) {
    issue(2); if (check(0)) break;
    issue(0); if (check(1)) break;
    issue(1); if (check(2)) break;
  }
}
template <int N>
__device__ __forceinline__ void gather_end(v4u (&p)[3][N]) {
#pragma unroll
  for (int j = 0; j < 3; ++j)
#pragma unroll
    for (int k = 0; k < N; ++k) asm volatile("" :: "v"(p[j][k]));
}
__device__ __forceinline__ void pace(unsigned& next, unsigned tau, unsigned& stall) {
  int d;
  asm volatile("1:\n\ts_memrealtime vcc\n\ts_waitcnt lgkmcnt(0)\n\ts_lshl_b32 %0, vcc_lo, 4\n\ts_sub_i32 %0, %0, %1\n\ts_cmp_ge_i32 %0, 0\n\ts_cbranch_scc1 2f\n\ts_sleep 1\n\ts_branch 1b\n2:"
               : "=&s"(d) : "s"(next) : "scc", "vcc");
  next += tau + (unsigned)max(0, d - BURST * 16);
  stall += d > STALL * 16 ? d : 0;
}

__global__ void __launch_bounds__(NTHR, 3)
fused_decode(const unsigned short* __restrict__ xg, unsigned char* __restrict__ ws, const unsigned short* __restrict__ wq,
             const unsigned short* __restrict__ kg, const unsigned short* __restrict__ wgate, const unsigned short* __restrict__ wup,
             const unsigned short* __restrict__ wdown, const unsigned short* __restrict__ vg, const unsigned short* __restrict__ wo,
             unsigned short* __restrict__ out) {
  __shared__ __attribute__((aligned(16))) float lds[L_FLOATS];
  asm volatile("" :: "s"(xg), "s"(ws), "s"(wq), "s"(kg), "s"(wgate), "s"(wup), "s"(wdown));
  const int lane = threadIdx.x & 63, pw = __builtin_amdgcn_readfirstlane(threadIdx.x >> 6);
  const int w = pw == 0 ? NW : pw - 1;
  const int b = blockIdx.x, g = b >> 4, s = b & 15;
  const int c = lane & 15;
  const int sr = (s + 3 * g) & 15, ns2 = 5 - (g & 1);
  const bool is1 = sr < 9, is2 = !is1 && sr < 9 + ns2;
  const int f1 = g * 9 + sr, f2 = (g >> 1) * 9 + (g & 1) * 5 + sr - 9;
  unsigned* const ldu = reinterpret_cast<unsigned*>(lds);
  const unsigned lds0 = (unsigned)(uintptr_t)(LAS float*)lds;

  const __amdgpu_buffer_rsrc_t rws = mk_rsrc(ws, WS_END);
  const __amdgpu_buffer_rsrc_t rx = mk_rsrc(xg, DM * 2);

  auto wait_flag = [&](int idx, unsigned want) {
#pragma unroll 1
    for (unsigned spins = 0; spins < (SPIN_CAP << 6); ++spins) {
      if (__builtin_amdgcn_readfirstlane(*(volatile LAS unsigned*)&ldu[L_FLAG + idx]) >= want) break;
      __builtin_amdgcn_s_sleep(1);
    }
    asm volatile("" ::: "memory");
  };
  auto set_flag = [&](int idx) {
    asm volatile("" ::: "memory");
    if (lane == 0) *(volatile LAS unsigned*)&ldu[L_FLAG + idx] = 1u;
  };
  auto count_up = [&](int idx) {
    int r = lds_inc(lds0 + (L_FLAG + idx) * 4);
    asm volatile("s_waitcnt lgkmcnt(0)" : "+v"(r));
  };
  unsigned gm[4];
#pragma unroll
  for (int r = 0; r < 4; ++r) gm[r] = ((lane & 7) >> 1) == r ? ((lane & 1) ? 0xffff0000u : 0x0000ffffu) : 0u;
  unsigned tag;

  if (__builtin_expect(w == NW, 1)) {
    __builtin_amdgcn_s_setprio(3);
    unsigned xd[5], xq[3];
#pragma unroll
    for (int k = 0; k < 5; ++k) xd[k] = __builtin_amdgcn_raw_buffer_load_b32(rx, lane * 4 + k * 256, 0, 0);
#pragma unroll
    for (int k = 0; k < 3; ++k) xq[k] = __builtin_amdgcn_raw_buffer_load_b16(rx, (144 * (s >> 2) + lane + 64 * k) * 2, 0, 0);
    const unsigned epoch = __builtin_amdgcn_raw_buffer_load_b32(rws, lane == 0 ? WS_EPOCH : OOB, 0, AUX_SC1);
    v4u wqv[18];
    {
      const __amdgpu_buffer_rsrc_t rq = mk_rsrc(wq, DM * QD * 2);
      const int o = (144 * (s >> 2) + (lane >> 3)) * (QD * 2) + (g * 4 + (s & 3)) * 128 + (lane & 7) * 16;
#pragma unroll
      for (int i = 0; i < 18; ++i) wqv[i] = __builtin_amdgcn_raw_buffer_load_b128(rq, o, i * 8 * QD * 2, AUX_NT);
    }
    if (lane < 16) ldu[L_FLAG + lane] = 0u;
    asm volatile("s_waitcnt lgkmcnt(0)" ::: "memory");
    __builtin_amdgcn_sched_barrier(0);
    __builtin_amdgcn_s_barrier();
    __builtin_amdgcn_sched_barrier(0);
    float mb, den;
    {
      float xv[10];
#pragma unroll
      for (int k = 0; k < 5; ++k) { xv[2 * k] = lo_f(xd[k]); xv[2 * k + 1] = hi_f(xd[k]); }
      float sm = 0.f;
#pragma unroll
      for (int k = 0; k < 10; ++k) sm += xv[k];
      const float mean = wave_sum(sm) * (1.0f / DM);
      float sq = 0.f;
#pragma unroll
      for (int k = 0; k < 8; ++k) { const float d = xv[k] - mean; sq = fmaf(d, d, sq); }
      const float d8 = lane < 32 ? xv[8] - mean : 0.f, d9 = lane < 32 ? xv[9] - mean : 0.f;
      sq = fmaf(d8, d8, sq); sq = fmaf(d9, d9, sq);
      const float var = wave_sum(sq) * (1.0f / DM);
      mb = rnd(mean);
      den = rnd(sqrtf(rnd(rnd(var) + 1e-5f)));
    }
    unsigned xn[3];
#pragma unroll
    for (int k = 0; k < 3; ++k) { const float v = rnd(lo_f(xq[k]) - mb) / den; xn[k] = pk_bf16(v, v); }
    unsigned xr[18];
#pragma unroll
    for (int i = 0; i < 18; ++i) xr[i] = __shfl(xn[i >> 3], (8 * i + (lane >> 3)) & 63);
#pragma unroll
    for (int i = 0; i < 18; ++i) asm volatile("" : "+v"(xr[i]));
    __builtin_amdgcn_sched_barrier(0);
    f32x4 qa = {0.f, 0.f, 0.f, 0.f};
#pragma unroll
    for (int i = 0; i < 18; ++i) { qa = mfma16(and4(xr[i], gm), wqv[i], qa); __builtin_amdgcn_sched_barrier(0); }
    qa = gemv_fin(qa, lane);
    tag = __builtin_amdgcn_readfirstlane(epoch) + 1u;
    {
      const bool up2 = (lane & 8) != 0;
      const int col = (lane & 7) * 8 + 4 * ((lane >> 4) & 1) + (up2 ? 2 : 0);
      __builtin_amdgcn_raw_buffer_store_b128(v4u{__float_as_uint(up2 ? qa[2] : qa[0]), tag, __float_as_uint(up2 ? qa[3] : qa[1]), tag}, rws,
                                             lane < 32 ? WS_QG + ((g * 4 + (s >> 2)) * 256 + (s & 3) * 64 + col) * 8 : OOB, 0, AUX_SC1);
    }
    if (lane == 0) ldu[L_FLAG + F_TAG] = tag;
    if (is1) {
#pragma unroll
      for (int k = 0; k < 5; ++k) {
        const float v0 = rnd(lo_f(xd[k]) - mb) / den, v1 = rnd(hi_f(xd[k]) - mb) / den;
        *reinterpret_cast<v2u*>(&ldu[L_XN + 2 * (lane + 64 * k)]) = v2u{pk_bf16(v0, v0), pk_bf16(v1, v1)};
      }
      set_flag(F_XRDY);
    }
    for (int i = 0; i < 5; ++i) __builtin_amdgcn_s_sleep(8);
    float q4[4];
    for (unsigned spins = 0;; ++spins) {
      asm volatile("" ::: "memory");
      v4u a[8];
#pragma unroll
      for (int r = 0; r < 4; ++r) {
        a[2 * r] = __builtin_amdgcn_raw_buffer_load_b128(rws, WS_QG + g * 8192 + lane * 32, r * 2048, AUX_SC1);
        a[2 * r + 1] = __builtin_amdgcn_raw_buffer_load_b128(rws, WS_QG + g * 8192 + lane * 32 + 16, r * 2048, AUX_SC1);
      }
      __builtin_amdgcn_sched_barrier(0);
      set_flag(F_POLL);
      __builtin_amdgcn_sched_barrier(0);
      unsigned bad = 0;
#pragma unroll
      for (int j = 0; j < 4; ++j) q4[j] = 0.f;
#pragma unroll
      for (int r = 0; r < 4; ++r) {
        bad |= (a[2 * r].y ^ tag) | (a[2 * r].w ^ tag) | (a[2 * r + 1].y ^ tag) | (a[2 * r + 1].w ^ tag);
        q4[0] += __uint_as_float(a[2 * r].x); q4[1] += __uint_as_float(a[2 * r].z);
        q4[2] += __uint_as_float(a[2 * r + 1].x); q4[3] += __uint_as_float(a[2 * r + 1].z);
      }
      if (__all(bad == 0) || spins > SPIN_CAP) break;
      __builtin_amdgcn_s_sleep(1);
    }
    *reinterpret_cast<v2u*>(&ldu[L_Q + 2 * lane]) = v2u{pk_bf16(q4[0], q4[1]), pk_bf16(q4[2], q4[3])};
    set_flag(F_QRDY);
    __builtin_amdgcn_s_setprio(0);
    float slope = (float)TAU_NOM;
    unsigned hist = 0, prev16 = 0, pstall = 0;
    auto checkpoint = [&](int k, int idx, int span) {
      wait_flag(F_PROG, idx);
      const unsigned t1 = (unsigned)__builtin_amdgcn_s_memrealtime();
      const unsigned stall = __builtin_amdgcn_readfirstlane(*(volatile LAS unsigned*)&ldu[L_FLAG + F_STALL]);
      hist = lane == k ? t1 : hist;
      const unsigned tg = lane <= k ? tag : ~tag;
      __builtin_amdgcn_raw_buffer_store_b128(v4u{hist, tg, hist, tg}, rws, lane < 8 ? WS_CP + b * 128 + lane * 16 : OOB, 0, AUX_SC1);
      wait_flag(F_PROG, idx + CP_D);
      const v2u cp = __builtin_amdgcn_raw_buffer_load_b64(rws, WS_CP + (g * 16 + c) * 128 + k * 16, 0, AUX_SC1);
      float dt = cp.y == tag ? (float)(int)(cp.x - t1) : (float)(CP_D * TAU_NOM / 16);
      dt += dpp<XOR1>(dt); dt += dpp<XOR2>(dt); dt += dpp<ROR4>(dt); dt += dpp<XOR8>(dt);
      const unsigned t16 = (t1 << 4) + (unsigned)(int)dt;
      if (k > 0) {
        const float meas = (float)(int)(t16 - prev16 - (stall - pstall)) / (float)span;
        slope = fminf(meas > slope * KTH ? meas * KUP : fmaxf((float)TAU_NOM, slope * KDN), 2.0f * TAU_NOM);
      }
      prev16 = t16; pstall = stall;
      if (lane == 0) *reinterpret_cast<v2u*>(&ldu[L_FLAG + F_TEND]) = v2u{(t16 + (unsigned)(int)(slope * (float)(2 * NT - idx)) - stall) | 1u, (unsigned)(int)slope};
    };
    checkpoint(0, 12, 0); checkpoint(1, 32, 20); checkpoint(2, 56, 24);
    wait_flag(F_P1CNT, NSW);
    {
      const float mw = lds[L_MW + (lane & 7)];
      float mx = fmaxf(mw, dpp<XOR2>(mw)); mx = fmaxf(mx, dpp<ROR4>(mx));
      float l = 0.f;
#pragma unroll 2
      for (int i = 0; i < NSW * NT / 8; ++i) {
        const f32x4 v = *reinterpret_cast<const f32x4*>(&lds[L_SC + i * 256 + lane * 4]);
        l += (__builtin_amdgcn_exp2f(v[0] - mx) + __builtin_amdgcn_exp2f(v[1] - mx)) + (__builtin_amdgcn_exp2f(v[2] - mx) + __builtin_amdgcn_exp2f(v[3] - mx));
      }
      l += dpp<XOR2>(l); l += dpp<ROR4>(l); l += dpp<XOR8>(l); l = sum_x32(sum_x16(l));
      __builtin_amdgcn_raw_buffer_store_b128(v4u{__float_as_uint(mx), tag, __float_as_uint(l), tag}, rws,
                                             lane < 8 ? WS_ML + b * 128 + lane * 16 : OOB, 0, AUX_SC1);
      wait_flag(F_PROG, ML_AT);
      v4u ml;
      for (unsigned spins = 0;; ++spins) {
        asm volatile("" ::: "memory");
        ml = __builtin_amdgcn_raw_buffer_load_b128(rws, WS_ML + (g * 16 + ((lane >> 1) & 15)) * 128 + (lane & 1) * 16, 0, AUX_SC1);
        if (__all(((ml.y ^ tag) | (ml.w ^ tag)) == 0) || spins > SPIN_CAP) break;
        __builtin_amdgcn_s_sleep(32);
      }
      const float mp = __uint_as_float(ml.x);
      float M = fmaxf(mp, dpp<XOR2>(mp)); M = fmaxf(M, dpp<ROR4>(M)); M = fmaxf(M, dpp<XOR8>(M)); M = max_x16(M);
      float L = __builtin_amdgcn_exp2f(mp - M) * __uint_as_float(ml.z);
      asm volatile("" : "+v"(L));
      L += dpp<XOR2>(L); L += dpp<ROR4>(L); L += dpp<XOR8>(L); L = sum_x16(L);
      if (lane < 8) lds[L_MW + lane] = __builtin_amdgcn_exp2f(mw - M) / L;
      set_flag(F_SRDY);
    }
    checkpoint(3, 84, 28);
    if (is2) {
      wait_flag(F_PROG, NEAR_AT);
      wait_flag(F_FDONE, 3);
#pragma unroll
      for (int k = 0; k < 5; ++k) {
        const int t = k * 64 + lane;
        const f32x2 yf = *reinterpret_cast<const f32x2*>(&lds[L_YF + 2 * (t < 288 ? t : 0)]);
        __builtin_amdgcn_raw_buffer_store_b128(v4u{__float_as_uint(yf.x), tag, __float_as_uint(yf.y), tag}, rws,
                                               t < 288 ? WS_PF + ((t >> 3) * NFW + f2) * 128 + (t & 7) * 16 : OOB, 0, AUX_SC1);
      }
    }
    __builtin_amdgcn_s_setprio(3);
    wait_flag(F_ENDCNT, NSW);
    for (int i = 0; i < 3; ++i) __builtin_amdgcn_s_sleep(4);
    {
      int o2[2] = {WS_P1 + (g * 16 + s) * 2048 + lane * 16, WS_P1 + (g * 16 + s) * 2048 + 1024 + lane * 16};
      float pv4[4];
      v4u pl[3][2];
      gather<2>(rws, o2, tag, pv4, pl);
      auto h0 = __builtin_amdgcn_permlane32_swap(__float_as_uint(pv4[0] + pv4[2]), __float_as_uint(pv4[1] + pv4[3]), false, false);
      float v = sum_x16(__uint_as_float(h0[0]) + __uint_as_float(h0[1]));
      v += dpp<XOR8>(v);
      if ((lane & 24) == 0) lds[L_O + 2 * (lane & 7) + (lane >> 5)] = rnd(v);
      set_flag(F_ORDY);
      gather_end<2>(pl);
    }
    return;
  }

  const int nrole = is1 ? 4 : is2 ? 3 : 0;
  const int voff = lane * 16;
  __builtin_amdgcn_sched_barrier(0);
  __builtin_amdgcn_s_barrier();
  __builtin_amdgcn_sched_barrier(0);

  if (w >= NSW && w < NSW + nrole) {
    const int fw = w - NSW;
    v4u fr[24];
    if (is1) {
      const __amdgpu_buffer_rsrc_t rfg = mk_rsrc(wgate, DM * FF * 2);
      const __amdgpu_buffer_rsrc_t rfu = mk_rsrc(wup, DM * FF * 2);
      const int row0 = 288 * (f1 & 1) + 72 * fw + (lane >> 3);
      const int foff = row0 * (FF * 2) + (f1 >> 1) * 128 + (lane & 7) * 16;
#pragma unroll
      for (int i = 0; i < 9; ++i) {
        fr[2 * i] = __builtin_amdgcn_raw_buffer_load_b128(rfg, foff, i * 8 * FF * 2, AUX_NT);
        fr[2 * i + 1] = __builtin_amdgcn_raw_buffer_load_b128(rfu, foff, i * 8 * FF * 2, AUX_NT);
        __builtin_amdgcn_sched_barrier(0); asm volatile("s_waitcnt vmcnt(6)"); __builtin_amdgcn_sched_barrier(0);
      }
      wait_flag(F_XRDY, 1);
      tag = ldu[L_FLAG + F_TAG];
      f32x4 ag = {0.f, 0.f, 0.f, 0.f}, au = {0.f, 0.f, 0.f, 0.f};
#pragma unroll
      for (int i = 0; i < 9; ++i) {
        const v4u av = and4(ldu[L_XN + row0 + 8 * i], gm);
        ag = mfma16(av, fr[2 * i], ag); au = mfma16(av, fr[2 * i + 1], au);
      }
      ag = gemv_fin(ag, lane); au = gemv_fin(au, lane);
      if ((lane & 8) == 0) *reinterpret_cast<f32x4*>(&lds[L_YF + fw * 128 + (lane >> 5) * 64 + (lane & 7) * 8 + 4 * ((lane >> 4) & 1)]) = lane < 32 ? ag : au;
      int old = lds_inc(lds0 + (L_FLAG + F_S1CNT) * 4);
      float gs = 0.f, us = 0.f;
#pragma unroll
      for (int ww = 0; ww < 4; ++ww) { gs += lds[L_YF + ww * 128 + lane]; us += lds[L_YF + ww * 128 + 64 + lane]; }
      asm volatile("s_waitcnt lgkmcnt(0)" : "+v"(old), "+v"(gs), "+v"(us));
      if (__builtin_amdgcn_readfirstlane(old) == 3)
        __builtin_amdgcn_raw_buffer_store_b128(v4u{__float_as_uint(gs), tag, __float_as_uint(us), tag}, rws, WS_GU + (f1 * 64 + lane) * 16, 0, AUX_SC1);
    } else {
      const __amdgpu_buffer_rsrc_t rwd = mk_rsrc(wdown + (size_t)f2 * 64 * DM, 64 * DM * 2);
      const int doff = (lane >> 3) * (DM * 2) + fw * 384 + (lane & 7) * 16;
#pragma unroll
      for (int rg = 0; rg < 8; ++rg) {
#pragma unroll
        for (int k = 0; k < 3; ++k) fr[rg * 3 + k] = __builtin_amdgcn_raw_buffer_load_b128(rwd, doff + k * 128, rg * 8 * DM * 2, AUX_NT);
        __builtin_amdgcn_sched_barrier(0); asm volatile("s_waitcnt vmcnt(7)"); __builtin_amdgcn_sched_barrier(0);
      }
      wait_flag(F_QRDY, 1);
      tag = ldu[L_FLAG + F_TAG];
      for (int i = 0; i < 8; ++i) __builtin_amdgcn_s_sleep(8);
      v4u p0, p1;
      for (unsigned spins = 0;; ++spins) {
        asm volatile("" ::: "memory");
        p0 = __builtin_amdgcn_raw_buffer_load_b128(rws, WS_GU + (f2 * 128 + lane) * 16, 0, AUX_SC1);
        p1 = __builtin_amdgcn_raw_buffer_load_b128(rws, WS_GU + (f2 * 128 + lane) * 16, 1024, AUX_SC1);
        if (__all(((p0.y ^ tag) | (p0.w ^ tag) | (p1.y ^ tag) | (p1.w ^ tag)) == 0) || spins > SPIN_CAP) break;
        __builtin_amdgcn_s_sleep(16);
      }
      const float gt = rnd(__uint_as_float(p0.x) + __uint_as_float(p1.x)), up = rnd(__uint_as_float(p0.z) + __uint_as_float(p1.z));
      const float inner = 0.7978845608028654f * (gt + 0.044715f * gt * gt * gt);
      const float hv = rnd(0.5f * gt * (1.0f + tanhf(inner))) * up;
      const unsigned hp = pk_bf16(hv, hv);
      f32x4 ya[3];
#pragma unroll
      for (int k = 0; k < 3; ++k) ya[k] = f32x4{0.f, 0.f, 0.f, 0.f};
#pragma unroll
      for (int rg = 0; rg < 8; ++rg) {
        const v4u av = and4(__shfl(hp, rg * 8 + (lane >> 3)), gm);
#pragma unroll
        for (int k = 0; k < 3; ++k) ya[k] = mfma16(av, fr[rg * 3 + k], ya[k]);
      }
#pragma unroll
      for (int k = 0; k < 3; ++k) {
        ya[k] = gemv_fin(ya[k], lane);
        if (lane < 32 && (lane & 8) == 0) *reinterpret_cast<f32x4*>(&lds[L_YF + fw * 192 + k * 64 + (lane & 7) * 8 + 4 * (lane >> 4)]) = ya[k];
      }
      count_up(F_FDONE);
    }
  } else if (w < NSW) {
    const __amdgpu_buffer_rsrc_t rk = mk_rsrc(kg + (size_t)g * SK * 128, SK * 256);
    const __amdgpu_buffer_rsrc_t rv = mk_rsrc(vg + (size_t)g * SK * 128, SK * 256);
    const int ufirst = (s >> 1) * 256 + (s & 1) * 4 + w;
#define TOFF(t) ((ufirst + ((t) >> 1) * 8) << 13)
#define TSUB 4096
#define VOFF(t) ((t) < NT ? TOFF(t) + ((t) & 1) * TSUB : OOB)
#define tb(n, i) buf[(n) * TL + (i)]
    v4u buf[4 * TL];
#pragma unroll
    for (int i = 0; i < 2 * TL; ++i) buf[i] = __builtin_amdgcn_raw_buffer_load_b128(rk, voff + (i & 3) * 1024, TOFF(0) + (i >> 2) * TSUB, AUX_NT);
    __builtin_amdgcn_sched_barrier(0);
    wait_flag(F_POLL, 1);
#pragma unroll
    for (int i = 0; i < 2 * TL; ++i) buf[2 * TL + i] = __builtin_amdgcn_raw_buffer_load_b128(rk, voff + (i & 3) * 1024, TOFF(2) + (i >> 2) * TSUB, AUX_NT);
    __builtin_amdgcn_sched_barrier(0);
    wait_flag(F_QRDY, 1);
    const int hf = (c >> 2) & 1;
    const v4u qf = *reinterpret_cast<const v4u*>(&ldu[L_Q + hf * 64 + 4 * c]);
    const v4u qs = *reinterpret_cast<const v4u*>(&ldu[L_Q + (hf ^ 1) * 64 + 4 * c]);
    constexpr float LOG2E = 1.4426950408889634f, SCALE = 0.08838834764831845f;
    const bool b0 = (c & 1) != 0, b1 = (c & 2) != 0;
    float* const scw = &lds[L_SC + w * NT * 32 + (lane >> 4) * 8 + (lane & 7)];
    float m = -INFINITY;
    float y[4];
    auto qk1 = [&](const v4u* kt) {
#pragma unroll
      for (int i = 0; i < TL; ++i) {
        float xf = 0.f, xs = 0.f;
#pragma unroll
        for (int j = 0; j < 4; ++j) { xf = dot2(kt[i][j], qf[j], xf); xs = dot2(kt[i][j], qs[j], xs); }
        y[i] = xf + dpp<XOR7>(xs);
      }
      asm volatile("" : "+v"(y[0]), "+v"(y[1]), "+v"(y[2]), "+v"(y[3]));
      __builtin_amdgcn_sched_barrier(0);
    };
    auto qk2 = [&](int t) {
      const float u0 = (b0 ? y[1] : y[0]) + dpp<XOR1>(b0 ? y[0] : y[1]);
      const float u1 = (b0 ? y[3] : y[2]) + dpp<XOR1>(b0 ? y[2] : y[3]);
      float sc = (b1 ? u1 : u0) + dpp<XOR2>(b1 ? u0 : u1);
      sc += dpp<XOR8>(sc);
      const float tl2 = rnd(rnd(sc) * SCALE) * LOG2E;
      m = fmaxf(m, tl2);
      scw[t * 32] = tl2;
    };
#pragma unroll
    for (int n = 0; n < 2; ++n) {
      qk1(&tb(n, 0));
#pragma unroll
      for (int i = 0; i < TL; ++i) tb(n, i) = __builtin_amdgcn_raw_buffer_load_b128(rk, voff + i * 1024, TOFF(4) + n * TSUB, AUX_NT);
      __builtin_amdgcn_sched_barrier(0);
      qk2(n);
    }
    qk1(&tb(2, 0)); qk2(2);
    qk1(&tb(3, 0)); qk2(3);
    unsigned next = (unsigned)__builtin_amdgcn_s_memrealtime() << 4, tau = TAU_NOM;
    unsigned stall = 0, tend = 0;
    int slope = TAU_NOM;
    auto aim = [&](int done) {
      if (w == 0 && lane == 0) *(volatile LAS v2u*)&ldu[L_FLAG + F_PROG] = v2u{(unsigned)done, stall};
      if (done & 2) return;
      const v2u te = *(volatile LAS v2u*)&ldu[L_FLAG + F_TEND];
      const unsigned t0 = __builtin_amdgcn_readfirstlane(te.x);
      if (t0 == 0) return;
      tend = t0; slope = __builtin_amdgcn_readfirstlane(te.y);
      const int v = __builtin_amdgcn_readfirstlane((int)((float)(int)(tend + stall - next) * __builtin_amdgcn_rcpf((float)(2 * NT - done))));
      tau = (unsigned)min(slope + TAU_UP, max(slope - TAU_DN, v));
    };
#pragma unroll 1
    for (int t = 4; t < NT; t += 2) {
      const bool lastk = t + 2 >= NT;
      const __amdgpu_buffer_rsrc_t rn = lastk ? rv : rk;
      const int nxt = TOFF(lastk ? 0 : t + 2);
      if (t <= 12) stall = 0;
      aim(t);
#pragma unroll
      for (int n = 0; n < 2; ++n) {
        qk1(&tb(n, 0));
        pace(next, tau, stall);
#pragma unroll
        for (int i = 0; i < TL; ++i) tb(n, i) = __builtin_amdgcn_raw_buffer_load_b128(rn, voff + i * 1024, nxt + n * TSUB, AUX_NT);
        __builtin_amdgcn_sched_barrier(0);
        qk2(t + n);
      }
    }
    m = fmaxf(m, dpp<XOR1>(m)); m = fmaxf(m, dpp<XOR2>(m));
    m = max_x32(max_x16(m));
    if ((lane & 59) == 0) lds[L_MW + 2 * w + (lane >> 2)] = m;
    count_up(F_P1CNT);
    f32x4 acc = {0.f, 0.f, 0.f, 0.f};
    unsigned amask[4];
    {
      const int jp = (c & 3) + 4 * (c >> 3);
#pragma unroll
      for (int r = 0; r < 4; ++r) amask[r] = (jp >> 1) == r ? ((jp & 1) ? 0xffff0000u : 0x0000ffffu) : 0u;
    }
    auto pv = [&](float sv, const v4u* vt) {
      const float p = __builtin_amdgcn_exp2f(sv - m);
      const unsigned pp = pk_bf16(p, p);
#pragma unroll
      for (int i = 0; i < TL; ++i) {
        const unsigned pb = i == 0 ? dppu<0x00>(pp) : i == 1 ? dppu<0x55>(pp) : i == 2 ? dppu<0xAA>(pp) : dppu<0xFF>(pp);
        acc = mfma16(and4(pb, amask), vt[i], acc);
      }
      asm volatile("" : "+v"(acc));
      __builtin_amdgcn_sched_barrier(0);
    };
    int t = 0;
    auto vrun = [&](int lim) {
#pragma unroll 1
      for (; t < lim; t += 2) {
        aim(NT + t);
        const int nxt = TOFF(t + 2);
        const float s0 = scw[t * 32], s1 = scw[t * 32 + 32];
#pragma unroll
        for (int n = 0; n < 2; ++n) {
          pv(n ? s1 : s0, &tb(n, 0));
          pace(next, tau, stall);
#pragma unroll
          for (int i = 0; i < TL; ++i) tb(n, i) = __builtin_amdgcn_raw_buffer_load_b128(rv, voff + i * 1024, nxt + n * TSUB, AUX_NT);
          __builtin_amdgcn_sched_barrier(0);
        }
      }
    };
    vrun(NT - X_MAX);
    {
      const int late = min(1 << 16, max(0, (int)(next + X_MAX * (slope - TAU_FAST) - tend - stall)));
      vrun(NT - min(X_MAX, X_MIN + 2 * ((late * X_C) >> 15)));
    }
#pragma unroll
    for (int k = 2; k < 4; ++k) {
#pragma unroll
      for (int i = 0; i < TL; ++i) tb(k, i) = __builtin_amdgcn_raw_buffer_load_b128(rv, voff + i * 1024, TOFF(t + 2) + (k & 1) * TSUB, AUX_NT);
      __builtin_amdgcn_sched_barrier(0);
    }
    __builtin_amdgcn_s_setprio(2);
    wait_flag(F_SRDY, 1);
    const float ed = lds[L_MW + 2 * w + ((lane >> 4) & 1)];
    tag = ldu[L_FLAG + F_TAG];
    const int po = WS_P1 + ((g * 16 + (lane >> 3)) * 16 + s) * 128 + (lane & 7) * 16;
#pragma unroll 1
    for (; t + 4 < NT; t += 4) {
      if (w == 0 && lane == 0) *(volatile LAS int*)&ldu[L_FLAG + F_PROG] = NT + t + 2;
#pragma unroll
      for (int k = 0; k < 4; ++k) {
        const int o = VOFF(t + k + 4);
        pv(scw[(t + k) * 32], &tb(k, 0));
#pragma unroll
        for (int i = 0; i < TL; ++i) tb(k, i) = __builtin_amdgcn_raw_buffer_load_b128(rv, voff + i * 1024, o, AUX_NT);
        __builtin_amdgcn_sched_barrier(0);
      }
    }
    {
      if (w == 0 && lane == 0) *(volatile LAS int*)&ldu[L_FLAG + F_PROG] = 126;
      v4u av[4][TL];
#pragma unroll
      for (int k = 0; k < 4; ++k) {
        const float p = __builtin_amdgcn_exp2f((t + k < NT ? scw[(t + k) * 32] : -INFINITY) - m);
        const unsigned pp = pk_bf16(p, p);
        av[k][0] = and4(dppu<0x00>(pp), amask); av[k][1] = and4(dppu<0x55>(pp), amask);
        av[k][2] = and4(dppu<0xAA>(pp), amask); av[k][3] = and4(dppu<0xFF>(pp), amask);
        asm volatile("" : "+v"(av[k][0]), "+v"(av[k][1]), "+v"(av[k][2]), "+v"(av[k][3]));
      }
      __builtin_amdgcn_sched_barrier(0);
#pragma unroll
      for (int k = 0; k < 2; ++k)
#pragma unroll
        for (int i = 0; i < TL; ++i) acc = mfma16(av[k][i], tb(k, i), acc);
      if (t + 2 < NT) {
#pragma unroll
        for (int k = 2; k < 4; ++k)
#pragma unroll
          for (int i = 0; i < TL; ++i) acc = mfma16(av[k][i], tb(k, i), acc);
      }
    }
    *reinterpret_cast<f32x4*>(&lds[L_ACC + w * 256 + ((lane >> 4) & 1) * 128 + 8 * c + 4 * (lane >> 5)]) = acc * ed;
    int old = lds_inc(lds0 + (L_FLAG + F_ENDCNT) * 4);
    f32x2 o0 = {0.f, 0.f}, o1 = {0.f, 0.f};
#pragma unroll
    for (int ww = 0; ww < NSW; ++ww) { o0 += *reinterpret_cast<const f32x2*>(&lds[L_ACC + ww * 256 + 2 * lane]); o1 += *reinterpret_cast<const f32x2*>(&lds[L_ACC + ww * 256 + 128 + 2 * lane]); }
    asm volatile("s_waitcnt lgkmcnt(0)" : "+v"(old), "+v"(o0), "+v"(o1));
    if (__builtin_amdgcn_readfirstlane(old) == NSW - 1) {
      __builtin_amdgcn_raw_buffer_store_b128(v4u{__float_as_uint(o0[0]), tag, __float_as_uint(o0[1]), tag}, rws, po, 0, AUX_SC1);
      __builtin_amdgcn_raw_buffer_store_b128(v4u{__float_as_uint(o1[0]), tag, __float_as_uint(o1[1]), tag}, rws, po, 8 * 2048, AUX_SC1);
    }
  }

  if (w >= NSW) {
    const int t = (w - NSW) * 64 + lane;
    const __amdgpu_buffer_rsrc_t rwo = mk_rsrc(wo + (size_t)b * 16 * DM, 16 * DM * 2);
    if (w == NW - 1) {
      const unsigned t0 = __builtin_amdgcn_raw_buffer_load_b32(mk_rsrc(vg + (size_t)g * SK * 128, SK * 256), lane < 2 ? ((KPAT == 2 ? (s & 7) : (s >> 1)) << 21) + lane * 0x1ffffc : OOB, 0, AUX_NT);
      const unsigned t1 = __builtin_amdgcn_raw_buffer_load_b32(rwo, lane == 0 ? 0 : OOB, 0, AUX_NT);
      const unsigned t2 = __builtin_amdgcn_raw_buffer_load_b32(mk_rsrc(out, DM * 2), lane == 0 && b < NRED ? b * 32 : OOB, 0, AUX_SC1);
      const unsigned t3 = __builtin_amdgcn_raw_buffer_load_b32(rws, lane == 0 ? WS_PAD : OOB, 0, AUX_NT);
      asm volatile("" :: "v"(t0), "v"(t1), "v"(t2), "v"(t3));
    }
#pragma unroll 1
    for (unsigned spins = 0; spins < (SPIN_CAP << 5); ++spins) {
      if (__builtin_amdgcn_readfirstlane(*(volatile LAS unsigned*)&ldu[L_FLAG + F_ENDCNT]) >= 1u) break;
      __builtin_amdgcn_s_sleep(2);
    }
    float wf[32];
    {
      unsigned wod[16];
#pragma unroll
      for (int j = 0; j < 16; ++j) wod[j] = __builtin_amdgcn_raw_buffer_load_b32(rwo, t < 288 ? t * 4 : OOB, j * DM * 2, AUX_NT);
#pragma unroll
      for (int j = 0; j < 16; ++j) { wf[2 * j] = lo_f(wod[j]); wf[2 * j + 1] = hi_f(wod[j]); }
#pragma unroll
      for (int j = 0; j < 32; ++j) asm volatile("" : "+v"(wf[j]));
    }
    tag = ldu[L_FLAG + F_TAG];
    const int po = t < 288 ? WS_P2 + ((t >> 3) * NWG + b) * 128 + (t & 7) * 16 : OOB;
    f32x4 o4[4];
#pragma unroll 1
    for (unsigned spins = 0; spins < (SPIN_CAP << 8); ++spins) {
      unsigned f = *(volatile LAS unsigned*)&ldu[L_FLAG + F_ORDY];
      asm volatile("" ::: "memory");
#pragma unroll
      for (int k = 0; k < 4; ++k) o4[k] = *reinterpret_cast<const f32x4*>(&lds[L_O + 4 * k]);
      asm volatile("" : "+v"(o4[0]), "+v"(o4[1]), "+v"(o4[2]), "+v"(o4[3]) :: "memory");
      if (__builtin_amdgcn_readfirstlane(f) != 0u) break;
    }
    f32x2 ya = {0.f, 0.f}, yb = {0.f, 0.f};
#pragma unroll
    for (int j = 0; j < 16; j += 2) {
      ya += o4[j >> 2][j & 3] * f32x2{wf[2 * j], wf[2 * j + 1]};
      yb += o4[j >> 2][(j & 3) + 1] * f32x2{wf[2 * j + 2], wf[2 * j + 3]};
    }
    __builtin_amdgcn_raw_buffer_store_b128(v4u{__float_as_uint(ya.x + yb.x), tag, __float_as_uint(ya.y + yb.y), tag}, rws, po, 0, AUX_SC1);
  }

  if (b < NRED && w < 8) {
    if (w == 0) {
      const unsigned xp = __builtin_amdgcn_raw_buffer_load_b32(rx, lane < 8 ? b * 32 + lane * 4 : OOB, 0, 0);
      float f0, f1v;
      for (unsigned spins = 0;; ++spins) {
        asm volatile("" ::: "memory");
        v4u pf[9];
#pragma unroll
        for (int k = 0; k < 9; ++k) pf[k] = __builtin_amdgcn_raw_buffer_load_b128(rws, WS_PF + b * NFW * 128 + lane * 16, k * 1024, AUX_SC1);
        unsigned bad = 0; f0 = 0.f; f1v = 0.f;
#pragma unroll
        for (int k = 0; k < 9; ++k) { bad |= (pf[k].y ^ tag) | (pf[k].w ^ tag); f0 += __uint_as_float(pf[k].x); f1v += __uint_as_float(pf[k].z); }
        if (__all(bad == 0) || spins > SPIN_CAP) break;
        __builtin_amdgcn_s_sleep(8);
      }
      f0 += dpp<XOR8>(f0); f0 = rnd(sum_x32(sum_x16(f0)));
      f1v += dpp<XOR8>(f1v); f1v = rnd(sum_x32(sum_x16(f1v)));
      if (lane < 8) {
        *reinterpret_cast<f32x2*>(&lds[L_G + lane * 2]) = f32x2{rnd(f0 / (1.0f + __expf(-f0))), rnd(f1v / (1.0f + __expf(-f1v)))};
        ldu[L_G + 16 + lane] = xp;
      }
    }
    if (w < NSW) wait_flag(F_ORDY, 1);
    for (int i = 0; i < 4; ++i) __builtin_amdgcn_s_sleep(4);
    int o4[4];
    float sv[8];
#pragma unroll
    for (int k = 0; k < 4; ++k) o4[k] = WS_P2 + b * NWG * 128 + (w * 64 + lane) * 16 + k * 8192;
    v4u pl[3][4];
    gather<4>(rws, o4, tag, sv, pl);
    float s0 = (sv[0] + sv[2]) + (sv[4] + sv[6]), s1 = (sv[1] + sv[3]) + (sv[5] + sv[7]);
    s0 += dpp<XOR8>(s0); s0 = sum_x32(sum_x16(s0));
    s1 += dpp<XOR8>(s1); s1 = sum_x32(sum_x16(s1));
    if (lane < 8) *reinterpret_cast<f32x2*>(&lds[L_FIN + w * 16 + lane * 2]) = f32x2{s0, s1};
    int old = lds_inc(lds0 + (L_FLAG + F_FINCNT) * 4);
    f32x2 t2 = *reinterpret_cast<const f32x2*>(&lds[L_FIN + lane * 2]);
    f32x2 gv = *reinterpret_cast<const f32x2*>(&lds[L_G + (lane & 7) * 2]);
    unsigned xp = ldu[L_G + 16 + (lane & 7)];
    asm volatile("s_waitcnt lgkmcnt(0)" : "+v"(old), "+v"(t2), "+v"(gv), "+v"(xp));
    if (__builtin_amdgcn_readfirstlane(old) == 7) {
      t2.x += dpp<XOR8>(t2.x); t2.x = sum_x32(sum_x16(t2.x));
      t2.y += dpp<XOR8>(t2.y); t2.y = sum_x32(sum_x16(t2.y));
      const unsigned r = pk_bf16(lo_f(xp) + rnd(rnd(t2.x) * gv.x), hi_f(xp) + rnd(rnd(t2.y) * gv.y));
      __builtin_amdgcn_raw_buffer_store_b32(r, mk_rsrc(out, DM * 2), lane < 8 ? b * 32 + lane * 4 : OOB, 0, 0);
      if (b == 0 && lane == 0) __builtin_amdgcn_raw_buffer_store_b32(tag, rws, WS_EPOCH, 0, AUX_SC1);
    }
    gather_end<4>(pl);
  }
}

}

extern "C" void kernel_launch(void* const* d_in, const int* in_sizes, int n_in, void* d_out, int out_size, void* d_ws, size_t ws_size,
                              hipStream_t stream) {
  (void)in_sizes; (void)n_in; (void)out_size; (void)ws_size;
  fused_decode<<<NWG, NTHR, 0, stream>>>((const unsigned short*)d_in[0], (unsigned char*)d_ws, (const unsigned short*)d_in[3],
                                         (const unsigned short*)d_in[1], (const unsigned short*)d_in[6], (const unsigned short*)d_in[7],
                                         (const unsigned short*)d_in[8], (const unsigned short*)d_in[2], (const unsigned short*)d_in[4],
                                         (unsigned short*)d_out);
}
```

```cpp
#include <hip/hip_runtime.h>
#include <hip/hip_bf16.h>
#include <stdint.h>

namespace {

typedef unsigned v4u __attribute__((ext_vector_type(4)));
typedef unsigned v2u __attribute__((ext_vector_type(2)));
typedef float f32x4 __attribute__((ext_vector_type(4)));
typedef float f32x2 __attribute__((ext_vector_type(2)));
typedef __bf16 bf16x8 __attribute__((ext_vector_type(8)));
typedef __bf16 bf16x2 __attribute__((ext_vector_type(2)));
#define LAS __attribute__((address_space(3)))

constexpr int DM = 576, SK = 65536, FF = 4608, QD = 4096;
constexpr int NWG = 256;
constexpr int NSW = 4;
constexpr int NW = 9;
constexpr int NTHR = (NW + 1) * 64;
constexpr int NRED = 36;
constexpr int NFW = 72;
constexpr int TL = 4;
constexpr int NT = 64;
constexpr int OOB = 0x70000000;

#define KNOM 874
#define KCPS 4
#define KPAIR 0
#define KUP 1.010f
#define KDN 0.995f
#define KTH 1.004f
constexpr int TAU_NOM = KNOM;
constexpr int TAU_DN = 35, TAU_UP = 22, TAU_FAST = 20;
constexpr int BURST = 20, STALL = 100;
constexpr int CP_D = 6;
constexpr int ML_AT = 74;
constexpr int X_MIN = 4, X_MAX = 20, X_C = 68;
constexpr int NEAR_AT = 124;

constexpr int WS_EPOCH = 0;
constexpr int WS_QG = 4096;
constexpr int WS_ML = WS_QG + 16 * 4 * 256 * 8;
constexpr int WS_CP = WS_ML + NWG * 128;
constexpr int WS_P1 = WS_CP + NWG * 128;
constexpr int WS_P2 = WS_P1 + 16 * 16 * 16 * 128;
constexpr int WS_PF = WS_P2 + NRED * NWG * 128;
constexpr int WS_GU = WS_PF + NRED * NFW * 128;
constexpr int WS_PAD = WS_GU + NFW * 2 * 64 * 16;
constexpr int WS_END = WS_PAD + 128;

constexpr int AUX_NT = 2, AUX_SC1 = 16;
constexpr int RSRC_W3 = 0x00020000;
constexpr unsigned SPIN_CAP = 1u << 16;

constexpr int L_FLAG = 0;
enum { F_TAG, F_QRDY, F_XRDY, F_FDONE, F_POLL, F_P1CNT, F_SRDY, F_ORDY, F_PROG, F_STALL, F_TEND, F_SLOPE, F_ENDCNT, F_FINCNT, F_S1CNT };
constexpr int L_MW = 16;
constexpr int L_O = L_MW + 16;
constexpr int L_G = L_O + 16;
constexpr int L_Q = L_G + 32;
constexpr int L_XN = L_Q + 128;
constexpr int L_YF = L_XN + 640;
constexpr int L_FIN = L_YF + 576;
constexpr int L_ACC = L_FIN + 128;
constexpr int L_SC = L_ACC + NSW * 256;
constexpr int L_FLOATS = 24 * 1024;
static_assert(L_SC % 4 == 0 && L_ACC % 4 == 0 && L_Q % 4 == 0 && L_O % 4 == 0 && L_SC + NSW * NT * 32 <= L_FLOATS, "LDS map");
static_assert(16 * NSW * NT * 16 == SK && X_MIN >= 4, "key split");

template <int CTRL> __device__ __forceinline__ float dpp(float x) {
  return __builtin_bit_cast(float, __builtin_amdgcn_mov_dpp(__builtin_bit_cast(int, x), CTRL, 0xf, 0xf, true));
}
template <int CTRL> __device__ __forceinline__ unsigned dppu(unsigned x) {
  return (unsigned)__builtin_amdgcn_mov_dpp((int)x, CTRL, 0xf, 0xf, true);
}
constexpr int XOR1 = 0xB1, XOR2 = 0x4E, XOR7 = 0x141, XOR8 = 0x128, ROR4 = 0x124;
__device__ __forceinline__ float sum_x16(float x) {
  auto s = __builtin_amdgcn_permlane16_swap(__float_as_uint(x), __float_as_uint(x), false, false);
  return __uint_as_float(s[0]) + __uint_as_float(s[1]);
}
__device__ __forceinline__ float sum_x32(float x) {
  auto s = __builtin_amdgcn_permlane32_swap(__float_as_uint(x), __float_as_uint(x), false, false);
  return __uint_as_float(s[0]) + __uint_as_float(s[1]);
}
__device__ __forceinline__ float max_x16(float x) {
  auto s = __builtin_amdgcn_permlane16_swap(__float_as_uint(x), __float_as_uint(x), false, false);
  return fmaxf(__uint_as_float(s[0]), __uint_as_float(s[1]));
}
__device__ __forceinline__ float max_x32(float x) {
  auto s = __builtin_amdgcn_permlane32_swap(__float_as_uint(x), __float_as_uint(x), false, false);
  return fmaxf(__uint_as_float(s[0]), __uint_as_float(s[1]));
}
__device__ __forceinline__ float wave_sum(float x) {
  x += dpp<XOR1>(x); x += dpp<XOR2>(x); x += dpp<ROR4>(x); x += dpp<XOR8>(x);
  return sum_x32(sum_x16(x));
}
__device__ __forceinline__ unsigned pk_bf16(float lo, float hi) {
  bf16x2 v = {(__bf16)lo, (__bf16)hi};
  return __builtin_bit_cast(unsigned, v);
}
__device__ __forceinline__ float rnd(float x) { return (float)(__bf16)x; }
__device__ __forceinline__ float lo_f(unsigned u) { return __uint_as_float(u << 16); }
__device__ __forceinline__ float hi_f(unsigned u) { return __uint_as_float(u & 0xffff0000u); }
__device__ __forceinline__ float dot2(unsigned a, unsigned b, float c) {
  return __builtin_amdgcn_fdot2_f32_bf16(__builtin_bit_cast(bf16x2, a), __builtin_bit_cast(bf16x2, b), c, false);
}
__device__ __forceinline__ __amdgpu_buffer_rsrc_t mk_rsrc(const void* p, int bytes) {
  return __builtin_amdgcn_make_buffer_rsrc((void*)p, 0, bytes, RSRC_W3);
}
__device__ __forceinline__ v4u and4(unsigned x, const unsigned (&m)[4]) { return v4u{x & m[0], x & m[1], x & m[2], x & m[3]}; }
__device__ __forceinline__ f32x4 mfma16(v4u a, v4u bm, f32x4 cacc) {
  return __builtin_amdgcn_mfma_f32_16x16x32_bf16(__builtin_bit_cast(bf16x8, a), __builtin_bit_cast(bf16x8, bm), cacc, 0, 0, 0);
}
__device__ __forceinline__ f32x4 gemv_fin(f32x4 d, int lane) {
  const bool valid = (lane >> 5) == ((lane >> 3) & 1);
#pragma unroll
  for (int r = 0; r < 4; ++r) { float t = valid ? d[r] : 0.f; t += dpp<XOR8>(t); d[r] = sum_x32(t); }
  return d;
}
__device__ __forceinline__ int lds_inc(unsigned addr) {
  int r; unsigned long long save;
  asm volatile("s_mov_b64 %1, exec\n\ts_mov_b64 exec, 1\n\tds_add_rtn_u32 %0, %2, %3\n\ts_mov_b64 exec, %1"
               : "=&v"(r), "=&s"(save) : "v"(addr), "v"(1) : "memory");
  return r;
}
template <int N>
__device__ __forceinline__ void gather(__amdgpu_buffer_rsrc_t rs, int (&off)[N], unsigned tag, float (&val)[2 * N], v4u (&p)[3][N]) {
  auto issue = [&](int j) {
    asm volatile("" ::: "memory");
#pragma unroll
    for (int k = 0; k < N; ++k) p[j][k] = __builtin_amdgcn_raw_buffer_load_b128(rs, off[k], 0, AUX_SC1);
    __builtin_amdgcn_sched_barrier(0);
  };
  auto check = [&](int j) {
    bool done = true;
#pragma unroll
    for (int k = 0; k < N; ++k) {
      const bool ok = off[k] != OOB && ((p[j][k].y ^ tag) | (p[j][k].w ^ tag)) == 0;
      val[2 * k] = ok ? __uint_as_float(p[j][k].x) : val[2 * k]; val[2 * k + 1] = ok ? __uint_as_float(p[j][k].z) : val[2 * k + 1];
      off[k] = ok ? OOB : off[k];
      done = done && off[k] == OOB;
    }
    return __all(done) != 0;
  };
#pragma unroll
  for (int k = 0; k < 2 * N; ++k) val[k] = 0.f;
  issue(0); __builtin_amdgcn_s_sleep(5); issue(1); __builtin_amdgcn_s_sleep(5);
#pragma unroll 1
  for (unsigned spins = 0; spins < SPIN_CAP; ++spins) {
    issue(2); if (check(0)) break;
    issue(0); if (check(1)) break;
    issue(1); if (check(2)) break;
  }
}
template <int N>
__device__ __forceinline__ void gather_end(v4u (&p)[3][N]) {
#pragma unroll
  for (int j = 0; j < 3; ++j)
#pragma unroll
    for (int k = 0; k < N; ++k) asm volatile("" :: "v"(p[j][k]));
}
__device__ __forceinline__ void pace(unsigned& next, unsigned tau, unsigned& stall) {
  int d;
  asm volatile("1:\n\ts_memrealtime vcc\n\ts_waitcnt lgkmcnt(0)\n\ts_lshl_b32 %0, vcc_lo, 4\n\ts_sub_i32 %0, %0, %1\n\ts_cmp_ge_i32 %0, 0\n\ts_cbranch_scc1 2f\n\ts_sleep 1\n\ts_branch 1b\n2:"
               : "=&s"(d) : "s"(next) : "scc", "vcc");
  next += tau + (unsigned)max(0, d - BURST * 16);
  stall += d > STALL * 16 ? d : 0;
}

__global__ void __launch_bounds__(NTHR, 3)
fused_decode(const unsigned short* __restrict__ xg, unsigned char* __restrict__ ws, const unsigned short* __restrict__ wq,
             const unsigned short* __restrict__ kg, const unsigned short* __restrict__ wgate, const unsigned short* __restrict__ wup,
             const unsigned short* __restrict__ wdown, const unsigned short* __restrict__ vg, const unsigned short* __restrict__ wo,
             unsigned short* __restrict__ out) {
  __shared__ __attribute__((aligned(16))) float lds[L_FLOATS];
  asm volatile("" :: "s"(xg), "s"(ws), "s"(wq), "s"(kg), "s"(wgate), "s"(wup), "s"(wdown));
  const int lane = threadIdx.x & 63, pw = __builtin_amdgcn_readfirstlane(threadIdx.x >> 6);
  const int w = pw == 0 ? NW : pw - 1;
  const int b = blockIdx.x, g = b >> 4, s = b & 15;
  const int c = lane & 15;
  const int sr = (s + 3 * g) & 15, ns2 = 5 - (g & 1);
  const bool is1 = sr < 9, is2 = !is1 && sr < 9 + ns2;
  const int f1 = g * 9 + sr, f2 = (g >> 1) * 9 + (g & 1) * 5 + sr - 9;
  unsigned* const ldu = reinterpret_cast<unsigned*>(lds);
  const unsigned lds0 = (unsigned)(uintptr_t)(LAS float*)lds;

  const __amdgpu_buffer_rsrc_t rws = mk_rsrc(ws, WS_END);
  const __amdgpu_buffer_rsrc_t rx = mk_rsrc(xg, DM * 2);

  auto wait_flag = [&](int idx, unsigned want) {
#pragma unroll 1
    for (unsigned spins = 0; spins < (SPIN_CAP << 6); ++spins) {
      if (__builtin_amdgcn_readfirstlane(*(volatile LAS unsigned*)&ldu[L_FLAG + idx]) >= want) break;
      __builtin_amdgcn_s_sleep(1);
    }
    asm volatile("" ::: "memory");
  };
  auto set_flag = [&](int idx) {
    asm volatile("" ::: "memory");
    if (lane == 0) *(volatile LAS unsigned*)&ldu[L_FLAG + idx] = 1u;
  };
  auto count_up = [&](int idx) {
    int r = lds_inc(lds0 + (L_FLAG + idx) * 4);
    asm volatile("s_waitcnt lgkmcnt(0)" : "+v"(r));
  };
  unsigned gm[4];
#pragma unroll
  for (int r = 0; r < 4; ++r) gm[r] = ((lane & 7) >> 1) == r ? ((lane & 1) ? 0xffff0000u : 0x0000ffffu) : 0u;
  unsigned tag;

  if (__builtin_expect(w == NW, 1)) {
    __builtin_amdgcn_s_setprio(3);
    unsigned xd[5], xq[3];
#pragma unroll
    for (int k = 0; k < 5; ++k) xd[k] = __builtin_amdgcn_raw_buffer_load_b32(rx, lane * 4 + k * 256, 0, 0);
#pragma unroll
    for (int k = 0; k < 3; ++k) xq[k] = __builtin_amdgcn_raw_buffer_load_b16(rx, (144 * (s >> 2) + lane + 64 * k) * 2, 0, 0);
    const unsigned epoch = __builtin_amdgcn_raw_buffer_load_b32(rws, lane == 0 ? WS_EPOCH : OOB, 0, AUX_SC1);
    v4u wqv[18];
    {
      const __amdgpu_buffer_rsrc_t rq = mk_rsrc(wq, DM * QD * 2);
      const int o = (144 * (s >> 2) + (lane >> 3)) * (QD * 2) + (g * 4 + (s & 3)) * 128 + (lane & 7) * 16;
#pragma unroll
      for (int i = 0; i < 18; ++i) wqv[i] = __builtin_amdgcn_raw_buffer_load_b128(rq, o, i * 8 * QD * 2, AUX_NT);
    }
    if (lane < 16) ldu[L_FLAG + lane] = 0u;
    asm volatile("s_waitcnt lgkmcnt(0)" ::: "memory");
    __builtin_amdgcn_sched_barrier(0);
    __builtin_amdgcn_s_barrier();
    __builtin_amdgcn_sched_barrier(0);
    float mb, den;
    {
      float xv[10];
#pragma unroll
      for (int k = 0; k < 5; ++k) { xv[2 * k] = lo_f(xd[k]); xv[2 * k + 1] = hi_f(xd[k]); }
      float sm = 0.f;
#pragma unroll
      for (int k = 0; k < 10; ++k) sm += xv[k];
      const float mean = wave_sum(sm) * (1.0f / DM);
      float sq = 0.f;
#pragma unroll
      for (int k = 0; k < 8; ++k) { const float d = xv[k] - mean; sq = fmaf(d, d, sq); }
      const float d8 = lane < 32 ? xv[8] - mean : 0.f, d9 = lane < 32 ? xv[9] - mean : 0.f;
      sq = fmaf(d8, d8, sq); sq = fmaf(d9, d9, sq);
      const float var = wave_sum(sq) * (1.0f / DM);
      mb = rnd(mean);
      den = rnd(sqrtf(rnd(rnd(var) + 1e-5f)));
    }
    unsigned xn[3];
#pragma unroll
    for (int k = 0; k < 3; ++k) { const float v = rnd(lo_f(xq[k]) - mb) / den; xn[k] = pk_bf16(v, v); }
    unsigned xr[18];
#pragma unroll
    for (int i = 0; i < 18; ++i) xr[i] = __shfl(xn[i >> 3], (8 * i + (lane >> 3)) & 63);
#pragma unroll
    for (int i = 0; i < 18; ++i) asm volatile("" : "+v"(xr[i]));
    __builtin_amdgcn_sched_barrier(0);
    f32x4 qa = {0.f, 0.f, 0.f, 0.f};
#pragma unroll
    for (int i = 0; i < 18; ++i) { qa = mfma16(and4(xr[i], gm), wqv[i], qa); __builtin_amdgcn_sched_barrier(0); }
    qa = gemv_fin(qa, lane);
    tag = __builtin_amdgcn_readfirstlane(epoch) + 1u;
    {
      const bool up2 = (lane & 8) != 0;
      const int col = (lane & 7) * 8 + 4 * ((lane >> 4) & 1) + (up2 ? 2 : 0);
      __builtin_amdgcn_raw_buffer_store_b128(v4u{__float_as_uint(up2 ? qa[2] : qa[0]), tag, __float_as_uint(up2 ? qa[3] : qa[1]), tag}, rws,
                                             lane < 32 ? WS_QG + ((g * 4 + (s >> 2)) * 256 + (s & 3) * 64 + col) * 8 : OOB, 0, AUX_SC1);
    }
    if (lane == 0) ldu[L_FLAG + F_TAG] = tag;
    if (is1) {
#pragma unroll
      for (int k = 0; k < 5; ++k) {
        const float v0 = rnd(lo_f(xd[k]) - mb) / den, v1 = rnd(hi_f(xd[k]) - mb) / den;
        *reinterpret_cast<v2u*>(&ldu[L_XN + 2 * (lane + 64 * k)]) = v2u{pk_bf16(v0, v0), pk_bf16(v1, v1)};
      }
      set_flag(F_XRDY);
    }
    for (int i = 0; i < 5; ++i) __builtin_amdgcn_s_sleep(8);
    float q4[4];
    for (unsigned spins = 0;; ++spins) {
      asm volatile("" ::: "memory");
      v4u a[8];
#pragma unroll
      for (int r = 0; r < 4; ++r) {
        a[2 * r] = __builtin_amdgcn_raw_buffer_load_b128(rws, WS_QG + g * 8192 + lane * 32, r * 2048, AUX_SC1);
        a[2 * r + 1] = __builtin_amdgcn_raw_buffer_load_b128(rws, WS_QG + g * 8192 + lane * 32 + 16, r * 2048, AUX_SC1);
      }
      __builtin_amdgcn_sched_barrier(0);
      set_flag(F_POLL);
      __builtin_amdgcn_sched_barrier(0);
      unsigned bad = 0;
#pragma unroll
      for (int j = 0; j < 4; ++j) q4[j] = 0.f;
#pragma unroll
      for (int r = 0; r < 4; ++r) {
        bad |= (a[2 * r].y ^ tag) | (a[2 * r].w ^ tag) | (a[2 * r + 1].y ^ tag) | (a[2 * r + 1].w ^ tag);
        q4[0] += __uint_as_float(a[2 * r].x); q4[1] += __uint_as_float(a[2 * r].z);
        q4[2] += __uint_as_float(a[2 * r + 1].x); q4[3] += __uint_as_float(a[2 * r + 1].z);
      }
      if (__all(bad == 0) || spins > SPIN_CAP) break;
      __builtin_amdgcn_s_sleep(1);
    }
    *reinterpret_cast<v2u*>(&ldu[L_Q + 2 * lane]) = v2u{pk_bf16(q4[0], q4[1]), pk_bf16(q4[2], q4[3])};
    set_flag(F_QRDY);
    __builtin_amdgcn_s_setprio(0);
    float slope = (float)TAU_NOM;
    unsigned hist = 0, prev16 = 0, pstall = 0;
    auto checkpoint = [&](int k, int idx, int span) {
      wait_flag(F_PROG, idx);
      const unsigned t1 = (unsigned)__builtin_amdgcn_s_memrealtime();
      const unsigned stall = __builtin_amdgcn_readfirstlane(*(volatile LAS unsigned*)&ldu[L_FLAG + F_STALL]);
      hist = lane == k ? t1 : hist;
      const unsigned tg = lane <= k ? tag : ~tag;
      __builtin_amdgcn_raw_buffer_store_b128(v4u{hist, tg, hist, tg}, rws, lane < 8 ? WS_CP + b * 128 + lane * 16 : OOB, 0, AUX_SC1);
      wait_flag(F_PROG, idx + CP_D);
      const v2u cp = __builtin_amdgcn_raw_buffer_load_b64(rws, WS_CP + (g * 16 + c) * 128 + k * 16, 0, AUX_SC1);
      float dt = cp.y == tag ? (float)(int)(cp.x - t1) : (float)(CP_D * TAU_NOM / 16);
      dt += dpp<XOR1>(dt); dt += dpp<XOR2>(dt); dt += dpp<ROR4>(dt); dt += dpp<XOR8>(dt);
      const unsigned t16 = (t1 << 4) + (unsigned)(int)dt;
      if (k > 0) {
        const float meas = (float)(int)(t16 - prev16 - (stall - pstall)) / (float)span;
        slope = fminf(meas > slope * KTH ? meas * KUP : fmaxf((float)TAU_NOM, slope * KDN), 2.0f * TAU_NOM);
      }
      prev16 = t16; pstall = stall;
      if (lane == 0) *reinterpret_cast<v2u*>(&ldu[L_FLAG + F_TEND]) = v2u{(t16 + (unsigned)(int)(slope * (float)(2 * NT - idx)) - stall) | 1u, (unsigned)(int)slope};
    };
    checkpoint(0, 12, 0); checkpoint(1, 32, 20); checkpoint(2, 56, 24);
    wait_flag(F_P1CNT, NSW);
    {
      const float mw = lds[L_MW + (lane & 7)];
      float mx = fmaxf(mw, dpp<XOR2>(mw)); mx = fmaxf(mx, dpp<ROR4>(mx));
      float l = 0.f;
#pragma unroll 2
      for (int i = 0; i < NSW * NT / 8; ++i) {
        const f32x4 v = *reinterpret_cast<const f32x4*>(&lds[L_SC + i * 256 + lane * 4]);
        l += (__builtin_amdgcn_exp2f(v[0] - mx) + __builtin_amdgcn_exp2f(v[1] - mx)) + (__builtin_amdgcn_exp2f(v[2] - mx) + __builtin_amdgcn_exp2f(v[3] - mx));
      }
      l += dpp<XOR2>(l); l += dpp<ROR4>(l); l += dpp<XOR8>(l); l = sum_x32(sum_x16(l));
      __builtin_amdgcn_raw_buffer_store_b128(v4u{__float_as_uint(mx), tag, __float_as_uint(l), tag}, rws,
                                             lane < 8 ? WS_ML + b * 128 + lane * 16 : OOB, 0, AUX_SC1);
      wait_flag(F_PROG, ML_AT);
      v4u ml;
      for (unsigned spins = 0;; ++spins) {
        asm volatile("" ::: "memory");
        ml = __builtin_amdgcn_raw_buffer_load_b128(rws, WS_ML + (g * 16 + ((lane >> 1) & 15)) * 128 + (lane & 1) * 16, 0, AUX_SC1);
        if (__all(((ml.y ^ tag) | (ml.w ^ tag)) == 0) || spins > SPIN_CAP) break;
        __builtin_amdgcn_s_sleep(32);
      }
      const float mp = __uint_as_float(ml.x);
      float M = fmaxf(mp, dpp<XOR2>(mp)); M = fmaxf(M, dpp<ROR4>(M)); M = fmaxf(M, dpp<XOR8>(M)); M = max_x16(M);
      float L = __builtin_amdgcn_exp2f(mp - M) * __uint_as_float(ml.z);
      asm volatile("" : "+v"(L));
      L += dpp<XOR2>(L); L += dpp<ROR4>(L); L += dpp<XOR8>(L); L = sum_x16(L);
      if (lane < 8) lds[L_MW + lane] = __builtin_amdgcn_exp2f(mw - M) / L;
      set_flag(F_SRDY);
    }
    checkpoint(3, 84, 28);
    if (is2) {
      wait_flag(F_PROG, NEAR_AT);
      wait_flag(F_FDONE, 3);
#pragma unroll
      for (int k = 0; k < 5; ++k) {
        const int t = k * 64 + lane;
        const f32x2 yf = *reinterpret_cast<const f32x2*>(&lds[L_YF + 2 * (t < 288 ? t : 0)]);
        __builtin_amdgcn_raw_buffer_store_b128(v4u{__float_as_uint(yf.x), tag, __float_as_uint(yf.y), tag}, rws,
                                               t < 288 ? WS_PF + ((t >> 3) * NFW + f2) * 128 + (t & 7) * 16 : OOB, 0, AUX_SC1);
      }
    }
    __builtin_amdgcn_s_setprio(3);
    wait_flag(F_ENDCNT, NSW);
    for (int i = 0; i < 3; ++i) __builtin_amdgcn_s_sleep(4);
    {
      int o2[2] = {WS_P1 + (g * 16 + s) * 2048 + lane * 16, WS_P1 + (g * 16 + s) * 2048 + 1024 + lane * 16};
      float pv4[4];
      v4u pl[3][2];
      gather<2>(rws, o2, tag, pv4, pl);
      auto h0 = __builtin_amdgcn_permlane32_swap(__float_as_uint(pv4[0] + pv4[2]), __float_as_uint(pv4[1] + pv4[3]), false, false);
      float v = sum_x16(__uint_as_float(h0[0]) + __uint_as_float(h0[1]));
      v += dpp<XOR8>(v);
      if ((lane & 24) == 0) lds[L_O + 2 * (lane & 7) + (lane >> 5)] = rnd(v);
      set_flag(F_ORDY);
      gather_end<2>(pl);
    }
    return;
  }

  const int nrole = is1 ? 4 : is2 ? 3 : 0;
  const int voff = lane * 16;
  __builtin_amdgcn_sched_barrier(0);
  __builtin_amdgcn_s_barrier();
  __builtin_amdgcn_sched_barrier(0);

  if (w >= NSW && w < NSW + nrole) {
    const int fw = w - NSW;
    v4u fr[24];
    if (is1) {
      const __amdgpu_buffer_rsrc_t rfg = mk_rsrc(wgate, DM * FF * 2);
      const __amdgpu_buffer_rsrc_t rfu = mk_rsrc(wup, DM * FF * 2);
      const int row0 = 288 * (f1 & 1) + 72 * fw + (lane >> 3);
      const int foff = row0 * (FF * 2) + (f1 >> 1) * 128 + (lane & 7) * 16;
#pragma unroll
      for (int i = 0; i < 9; ++i) {
        fr[2 * i] = __builtin_amdgcn_raw_buffer_load_b128(rfg, foff, i * 8 * FF * 2, AUX_NT);
        fr[2 * i + 1] = __builtin_amdgcn_raw_buffer_load_b128(rfu, foff, i * 8 * FF * 2, AUX_NT);
        __builtin_amdgcn_sched_barrier(0); asm volatile("s_waitcnt vmcnt(6)"); __builtin_amdgcn_sched_barrier(0);
      }
      wait_flag(F_XRDY, 1);
      tag = ldu[L_FLAG + F_TAG];
      f32x4 ag = {0.f, 0.f, 0.f, 0.f}, au = {0.f, 0.f, 0.f, 0.f};
#pragma unroll
      for (int i = 0; i < 9; ++i) {
        const v4u av = and4(ldu[L_XN + row0 + 8 * i], gm);
        ag = mfma16(av, fr[2 * i], ag); au = mfma16(av, fr[2 * i + 1], au);
      }
      ag = gemv_fin(ag, lane); au = gemv_fin(au, lane);
      if ((lane & 8) == 0) *reinterpret_cast<f32x4*>(&lds[L_YF + fw * 128 + (lane >> 5) * 64 + (lane & 7) * 8 + 4 * ((lane >> 4) & 1)]) = lane < 32 ? ag : au;
      int old = lds_inc(lds0 + (L_FLAG + F_S1CNT) * 4);
      float gs = 0.f, us = 0.f;
#pragma unroll
      for (int ww = 0; ww < 4; ++ww) { gs += lds[L_YF + ww * 128 + lane]; us += lds[L_YF + ww * 128 + 64 + lane]; }
      asm volatile("s_waitcnt lgkmcnt(0)" : "+v"(old), "+v"(gs), "+v"(us));
      if (__builtin_amdgcn_readfirstlane(old) == 3)
        __builtin_amdgcn_raw_buffer_store_b128(v4u{__float_as_uint(gs), tag, __float_as_uint(us), tag}, rws, WS_GU + (f1 * 64 + lane) * 16, 0, AUX_SC1);
    } else {
      const __amdgpu_buffer_rsrc_t rwd = mk_rsrc(wdown + (size_t)f2 * 64 * DM, 64 * DM * 2);
      const int doff = (lane >> 3) * (DM * 2) + fw * 384 + (lane & 7) * 16;
#pragma unroll
      for (int rg = 0; rg < 8; ++rg) {
#pragma unroll
        for (int k = 0; k < 3; ++k) fr[rg * 3 + k] = __builtin_amdgcn_raw_buffer_load_b128(rwd, doff + k * 128, rg * 8 * DM * 2, AUX_NT);
        __builtin_amdgcn_sched_barrier(0); asm volatile("s_waitcnt vmcnt(7)"); __builtin_amdgcn_sched_barrier(0);
      }
      wait_flag(F_QRDY, 1);
      tag = ldu[L_FLAG + F_TAG];
      for (int i = 0; i < 8; ++i) __builtin_amdgcn_s_sleep(8);
      v4u p0, p1;
      for (unsigned spins = 0;; ++spins) {
        asm volatile("" ::: "memory");
        p0 = __builtin_amdgcn_raw_buffer_load_b128(rws, WS_GU + (f2 * 128 + lane) * 16, 0, AUX_SC1);
        p1 = __builtin_amdgcn_raw_buffer_load_b128(rws, WS_GU + (f2 * 128 + lane) * 16, 1024, AUX_SC1);
        if (__all(((p0.y ^ tag) | (p0.w ^ tag) | (p1.y ^ tag) | (p1.w ^ tag)) == 0) || spins > SPIN_CAP) break;
        __builtin_amdgcn_s_sleep(16);
      }
      const float gt = rnd(__uint_as_float(p0.x) + __uint_as_float(p1.x)), up = rnd(__uint_as_float(p0.z) + __uint_as_float(p1.z));
      const float inner = 0.7978845608028654f * (gt + 0.044715f * gt * gt * gt);
      const float hv = rnd(0.5f * gt * (1.0f + tanhf(inner))) * up;
      const unsigned hp = pk_bf16(hv, hv);
      f32x4 ya[3];
#pragma unroll
      for (int k = 0; k < 3; ++k) ya[k] = f32x4{0.f, 0.f, 0.f, 0.f};
#pragma unroll
      for (int rg = 0; rg < 8; ++rg) {
        const v4u av = and4(__shfl(hp, rg * 8 + (lane >> 3)), gm);
#pragma unroll
        for (int k = 0; k < 3; ++k) ya[k] = mfma16(av, fr[rg * 3 + k], ya[k]);
      }
#pragma unroll
      for (int k = 0; k < 3; ++k) {
        ya[k] = gemv_fin(ya[k], lane);
        if (lane < 32 && (lane & 8) == 0) *reinterpret_cast<f32x4*>(&lds[L_YF + fw * 192 + k * 64 + (lane & 7) * 8 + 4 * (lane >> 4)]) = ya[k];
      }
      count_up(F_FDONE);
    }
  } else if (w < NSW) {
    const __amdgpu_buffer_rsrc_t rk = mk_rsrc(kg + (size_t)g * SK * 128, SK * 256);
    const __amdgpu_buffer_rsrc_t rv = mk_rsrc(vg + (size_t)g * SK * 128, SK * 256);
    const int tfirst = (KPAIR ? ((s & 7) << 21) + ((s >> 3) << 15) : ((s >> 1) << 21) + ((s & 1) << 15)) + (w << 12);
#define TOFF(t) (tfirst + (((t) >> 1) << 16))
#define TSUB 16384
#define VOFF(t) ((t) < NT ? TOFF(t) + ((t) & 1) * TSUB : OOB)
#define tb(n, i) buf[(n) * TL + (i)]
    v4u buf[4 * TL];
#pragma unroll
    for (int i = 0; i < 2 * TL; ++i) buf[i] = __builtin_amdgcn_raw_buffer_load_b128(rk, voff + (i & 3) * 1024, TOFF(0) + (i >> 2) * TSUB, AUX_NT);
    __builtin_amdgcn_sched_barrier(0);
    wait_flag(F_POLL, 1);
#pragma unroll
    for (int i = 0; i < 2 * TL; ++i) buf[2 * TL + i] = __builtin_amdgcn_raw_buffer_load_b128(rk, voff + (i & 3) * 1024, TOFF(2) + (i >> 2) * TSUB, AUX_NT);
    __builtin_amdgcn_sched_barrier(0);
    wait_flag(F_QRDY, 1);
    const int hf = (c >> 2) & 1;
    const v4u qf = *reinterpret_cast<const v4u*>(&ldu[L_Q + hf * 64 + 4 * c]);
    const v4u qs = *reinterpret_cast<const v4u*>(&ldu[L_Q + (hf ^ 1) * 64 + 4 * c]);
    constexpr float LOG2E = 1.4426950408889634f, SCALE = 0.08838834764831845f;
    const bool b0 = (c & 1) != 0, b1 = (c & 2) != 0;
    float* const scw = &lds[L_SC + w * NT * 32 + (lane >> 4) * 8 + (lane & 7)];
    float m = -INFINITY;
    float y[4];
    auto qk1 = [&](const v4u* kt) {
#pragma unroll
      for (int i = 0; i < TL; ++i) {
        float xf = 0.f, xs = 0.f;
#pragma unroll
        for (int j = 0; j < 4; ++j) { xf = dot2(kt[i][j], qf[j], xf); xs = dot2(kt[i][j], qs[j], xs); }
        y[i] = xf + dpp<XOR7>(xs);
      }
      asm volatile("" : "+v"(y[0]), "+v"(y[1]), "+v"(y[2]), "+v"(y[3]));
      __builtin_amdgcn_sched_barrier(0);
    };
    auto qk2 = [&](int t) {
      const float u0 = (b0 ? y[1] : y[0]) + dpp<XOR1>(b0 ? y[0] : y[1]);
      const float u1 = (b0 ? y[3] : y[2]) + dpp<XOR1>(b0 ? y[2] : y[3]);
      float sc = (b1 ? u1 : u0) + dpp<XOR2>(b1 ? u0 : u1);
      sc += dpp<XOR8>(sc);
      const float tl2 = rnd(rnd(sc) * SCALE) * LOG2E;
      m = fmaxf(m, tl2);
      scw[t * 32] = tl2;
    };
#pragma unroll
    for (int n = 0; n < 2; ++n) {
      qk1(&tb(n, 0));
#pragma unroll
      for (int i = 0; i < TL; ++i) tb(n, i) = __builtin_amdgcn_raw_buffer_load_b128(rk, voff + i * 1024, TOFF(4) + n * TSUB, AUX_NT);
      __builtin_amdgcn_sched_barrier(0);
      qk2(n);
    }
    qk1(&tb(2, 0)); qk2(2);
    qk1(&tb(3, 0)); qk2(3);
    unsigned next = (unsigned)__builtin_amdgcn_s_memrealtime() << 4, tau = TAU_NOM;
    unsigned stall = 0, tend = 0;
    int slope = TAU_NOM;
    auto aim = [&](int done) {
      if (w == 0 && lane == 0) *(volatile LAS v2u*)&ldu[L_FLAG + F_PROG] = v2u{(unsigned)done, stall};
      if (done & 2) return;
      const v2u te = *(volatile LAS v2u*)&ldu[L_FLAG + F_TEND];
      const unsigned t0 = __builtin_amdgcn_readfirstlane(te.x);
      if (t0 == 0) return;
      tend = t0; slope = __builtin_amdgcn_readfirstlane(te.y);
      const int v = __builtin_amdgcn_readfirstlane((int)((float)(int)(tend + stall - next) * __builtin_amdgcn_rcpf((float)(2 * NT - done))));
      tau = (unsigned)min(slope + TAU_UP, max(slope - TAU_DN, v));
    };
#pragma unroll 1
    for (int t = 4; t < NT; t += 2) {
      const bool lastk = t + 2 >= NT;
      const __amdgpu_buffer_rsrc_t rn = lastk ? rv : rk;
      const int nxt = TOFF(lastk ? 0 : t + 2);
      if (t <= 12) stall = 0;
      aim(t);
#pragma unroll
      for (int n = 0; n < 2; ++n) {
        qk1(&tb(n, 0));
        pace(next, tau, stall);
#pragma unroll
        for (int i = 0; i < TL; ++i) tb(n, i) = __builtin_amdgcn_raw_buffer_load_b128(rn, voff + i * 1024, nxt + n * TSUB, AUX_NT);
        __builtin_amdgcn_sched_barrier(0);
        qk2(t + n);
      }
    }
    m = fmaxf(m, dpp<XOR1>(m)); m = fmaxf(m, dpp<XOR2>(m));
    m = max_x32(max_x16(m));
    if ((lane & 59) == 0) lds[L_MW + 2 * w + (lane >> 2)] = m;
    count_up(F_P1CNT);
    f32x4 acc = {0.f, 0.f, 0.f, 0.f};
    unsigned amask[4];
    {
      const int jp = (c & 3) + 4 * (c >> 3);
#pragma unroll
      for (int r = 0; r < 4; ++r) amask[r] = (jp >> 1) == r ? ((jp & 1) ? 0xffff0000u : 0x0000ffffu) : 0u;
    }
    auto pv = [&](float sv, const v4u* vt) {
      const float p = __builtin_amdgcn_exp2f(sv - m);
      const unsigned pp = pk_bf16(p, p);
#pragma unroll
      for (int i = 0; i < TL; ++i) {
        const unsigned pb = i == 0 ? dppu<0x00>(pp) : i == 1 ? dppu<0x55>(pp) : i == 2 ? dppu<0xAA>(pp) : dppu<0xFF>(pp);
        acc = mfma16(and4(pb, amask), vt[i], acc);
      }
      asm volatile("" : "+v"(acc));
      __builtin_amdgcn_sched_barrier(0);
    };
    int t = 0;
    auto vrun = [&](int lim) {
#pragma unroll 1
      for (; t < lim; t += 2) {
        aim(NT + t);
        const int nxt = TOFF(t + 2);
        const float s0 = scw[t * 32], s1 = scw[t * 32 + 32];
#pragma unroll
        for (int n = 0; n < 2; ++n) {
          pv(n ? s1 : s0, &tb(n, 0));
          pace(next, tau, stall);
#pragma unroll
          for (int i = 0; i < TL; ++i) tb(n, i) = __builtin_amdgcn_raw_buffer_load_b128(rv, voff + i * 1024, nxt + n * TSUB, AUX_NT);
          __builtin_amdgcn_sched_barrier(0);
        }
      }
    };
    vrun(NT - X_MAX);
    {
      const int late = min(1 << 16, max(0, (int)(next + X_MAX * (slope - TAU_FAST) - tend - stall)));
      vrun(NT - min(X_MAX, X_MIN + 2 * ((late * X_C) >> 15)));
    }
#pragma unroll
    for (int k = 2; k < 4; ++k) {
#pragma unroll
      for (int i = 0; i < TL; ++i) tb(k, i) = __builtin_amdgcn_raw_buffer_load_b128(rv, voff + i * 1024, TOFF(t + 2) + (k & 1) * TSUB, AUX_NT);
      __builtin_amdgcn_sched_barrier(0);
    }
    __builtin_amdgcn_s_setprio(2);
    wait_flag(F_SRDY, 1);
    const float ed = lds[L_MW + 2 * w + ((lane >> 4) & 1)];
    tag = ldu[L_FLAG + F_TAG];
    const int po = WS_P1 + ((g * 16 + (lane >> 3)) * 16 + s) * 128 + (lane & 7) * 16;
#pragma unroll 1
    for (; t + 4 < NT; t += 4) {
      if (w == 0 && lane == 0) *(volatile LAS int*)&ldu[L_FLAG + F_PROG] = NT + t + 2;
#pragma unroll
      for (int k = 0; k < 4; ++k) {
        const int o = VOFF(t + k + 4);
        pv(scw[(t + k) * 32], &tb(k, 0));
#pragma unroll
        for (int i = 0; i < TL; ++i) tb(k, i) = __builtin_amdgcn_raw_buffer_load_b128(rv, voff + i * 1024, o, AUX_NT);
        __builtin_amdgcn_sched_barrier(0);
      }
    }
    {
      if (w == 0 && lane == 0) *(volatile LAS int*)&ldu[L_FLAG + F_PROG] = 126;
      v4u av[4][TL];
#pragma unroll
      for (int k = 0; k < 4; ++k) {
        const float p = __builtin_amdgcn_exp2f((t + k < NT ? scw[(t + k) * 32] : -INFINITY) - m);
        const unsigned pp = pk_bf16(p, p);
        av[k][0] = and4(dppu<0x00>(pp), amask); av[k][1] = and4(dppu<0x55>(pp), amask);
        av[k][2] = and4(dppu<0xAA>(pp), amask); av[k][3] = and4(dppu<0xFF>(pp), amask);
        asm volatile("" : "+v"(av[k][0]), "+v"(av[k][1]), "+v"(av[k][2]), "+v"(av[k][3]));
      }
      __builtin_amdgcn_sched_barrier(0);
#pragma unroll
      for (int k = 0; k < 2; ++k)
#pragma unroll
        for (int i = 0; i < TL; ++i) acc = mfma16(av[k][i], tb(k, i), acc);
      if (t + 2 < NT) {
#pragma unroll
        for (int k = 2; k < 4; ++k)
#pragma unroll
          for (int i = 0; i < TL; ++i) acc = mfma16(av[k][i], tb(k, i), acc);
      }
    }
    *reinterpret_cast<f32x4*>(&lds[L_ACC + w * 256 + ((lane >> 4) & 1) * 128 + 8 * c + 4 * (lane >> 5)]) = acc * ed;
    int old = lds_inc(lds0 + (L_FLAG + F_ENDCNT) * 4);
    f32x2 o0 = {0.f, 0.f}, o1 = {0.f, 0.f};
#pragma unroll
    for (int ww = 0; ww < NSW; ++ww) { o0 += *reinterpret_cast<const f32x2*>(&lds[L_ACC + ww * 256 + 2 * lane]); o1 += *reinterpret_cast<const f32x2*>(&lds[L_ACC + ww * 256 + 128 + 2 * lane]); }
    asm volatile("s_waitcnt lgkmcnt(0)" : "+v"(old), "+v"(o0), "+v"(o1));
    if (__builtin_amdgcn_readfirstlane(old) == NSW - 1) {
      __builtin_amdgcn_raw_buffer_store_b128(v4u{__float_as_uint(o0[0]), tag, __float_as_uint(o0[1]), tag}, rws, po, 0, AUX_SC1);
      __builtin_amdgcn_raw_buffer_store_b128(v4u{__float_as_uint(o1[0]), tag, __float_as_uint(o1[1]), tag}, rws, po, 8 * 2048, AUX_SC1);
    }
  }

  if (w >= NSW) {
    const int t = (w - NSW) * 64 + lane;
    const __amdgpu_buffer_rsrc_t rwo = mk_rsrc(wo + (size_t)b * 16 * DM, 16 * DM * 2);
    if (w == NW - 1) {
      const unsigned t0 = __builtin_amdgcn_raw_buffer_load_b32(mk_rsrc(vg + (size_t)g * SK * 128, SK * 256), lane < 2 ? ((KPAIR ? s & 7 : s >> 1) << 21) + lane * 0x1ffffc : OOB, 0, AUX_NT);
      const unsigned t1 = __builtin_amdgcn_raw_buffer_load_b32(rwo, lane == 0 ? 0 : OOB, 0, AUX_NT);
      const unsigned t2 = __builtin_amdgcn_raw_buffer_load_b32(mk_rsrc(out, DM * 2), lane == 0 && b < NRED ? b * 32 : OOB, 0, AUX_SC1);
      const unsigned t3 = __builtin_amdgcn_raw_buffer_load_b32(rws, lane == 0 ? WS_PAD : OOB, 0, AUX_NT);
      asm volatile("" :: "v"(t0), "v"(t1), "v"(t2), "v"(t3));
    }
#pragma unroll 1
    for (unsigned spins = 0; spins < (SPIN_CAP << 5); ++spins) {
      if (__builtin_amdgcn_readfirstlane(*(volatile LAS unsigned*)&ldu[L_FLAG + F_ENDCNT]) >= 1u) break;
      __builtin_amdgcn_s_sleep(2);
    }
    float wf[32];
    {
      unsigned wod[16];
#pragma unroll
      for (int j = 0; j < 16; ++j) wod[j] = __builtin_amdgcn_raw_buffer_load_b32(rwo, t < 288 ? t * 4 : OOB, j * DM * 2, AUX_NT);
#pragma unroll
      for (int j = 0; j < 16; ++j) { wf[2 * j] = lo_f(wod[j]); wf[2 * j + 1] = hi_f(wod[j]); }
#pragma unroll
      for (int j = 0; j < 32; ++j) asm volatile("" : "+v"(wf[j]));
    }
    tag = ldu[L_FLAG + F_TAG];
    const int po = t < 288 ? WS_P2 + ((t >> 3) * NWG + b) * 128 + (t & 7) * 16 : OOB;
    f32x4 o4[4];
#pragma unroll 1
    for (unsigned spins = 0; spins < (SPIN_CAP << 8); ++spins) {
      unsigned f = *(volatile LAS unsigned*)&ldu[L_FLAG + F_ORDY];
      asm volatile("" ::: "memory");
#pragma unroll
      for (int k = 0; k < 4; ++k) o4[k] = *reinterpret_cast<const f32x4*>(&lds[L_O + 4 * k]);
      asm volatile("" : "+v"(o4[0]), "+v"(o4[1]), "+v"(o4[2]), "+v"(o4[3]) :: "memory");
      if (__builtin_amdgcn_readfirstlane(f) != 0u) break;
    }
    f32x2 ya = {0.f, 0.f}, yb = {0.f, 0.f};
#pragma unroll
    for (int j = 0; j < 16; j += 2) {
      ya += o4[j >> 2][j & 3] * f32x2{wf[2 * j], wf[2 * j + 1]};
      yb += o4[j >> 2][(j & 3) + 1] * f32x2{wf[2 * j + 2], wf[2 * j + 3]};
    }
    __builtin_amdgcn_raw_buffer_store_b128(v4u{__float_as_uint(ya.x + yb.x), tag, __float_as_uint(ya.y + yb.y), tag}, rws, po, 0, AUX_SC1);
  }

  if (b < NRED && w < 8) {
    if (w == 0) {
      const unsigned xp = __builtin_amdgcn_raw_buffer_load_b32(rx, lane < 8 ? b * 32 + lane * 4 : OOB, 0, 0);
      float f0, f1v;
      for (unsigned spins = 0;; ++spins) {
        asm volatile("" ::: "memory");
        v4u pf[9];
#pragma unroll
        for (int k = 0; k < 9; ++k) pf[k] = __builtin_amdgcn_raw_buffer_load_b128(rws, WS_PF + b * NFW * 128 + lane * 16, k * 1024, AUX_SC1);
        unsigned bad = 0; f0 = 0.f; f1v = 0.f;
#pragma unroll
        for (int k = 0; k < 9; ++k) { bad |= (pf[k].y ^ tag) | (pf[k].w ^ tag); f0 += __uint_as_float(pf[k].x); f1v += __uint_as_float(pf[k].z); }
        if (__all(bad == 0) || spins > SPIN_CAP) break;
        __builtin_amdgcn_s_sleep(8);
      }
      f0 += dpp<XOR8>(f0); f0 = rnd(sum_x32(sum_x16(f0)));
      f1v += dpp<XOR8>(f1v); f1v = rnd(sum_x32(sum_x16(f1v)));
      if (lane < 8) {
        *reinterpret_cast<f32x2*>(&lds[L_G + lane * 2]) = f32x2{rnd(f0 / (1.0f + __expf(-f0))), rnd(f1v / (1.0f + __expf(-f1v)))};
        ldu[L_G + 16 + lane] = xp;
      }
    }
    if (w < NSW) wait_flag(F_ORDY, 1);
    for (int i = 0; i < 4; ++i) __builtin_amdgcn_s_sleep(4);
    int o4[4];
    float sv[8];
#pragma unroll
    for (int k = 0; k < 4; ++k) o4[k] = WS_P2 + b * NWG * 128 + (w * 64 + lane) * 16 + k * 8192;
    v4u pl[3][4];
    gather<4>(rws, o4, tag, sv, pl);
    float s0 = (sv[0] + sv[2]) + (sv[4] + sv[6]), s1 = (sv[1] + sv[3]) + (sv[5] + sv[7]);
    s0 += dpp<XOR8>(s0); s0 = sum_x32(sum_x16(s0));
    s1 += dpp<XOR8>(s1); s1 = sum_x32(sum_x16(s1));
    if (lane < 8) *reinterpret_cast<f32x2*>(&lds[L_FIN + w * 16 + lane * 2]) = f32x2{s0, s1};
    int old = lds_inc(lds0 + (L_FLAG + F_FINCNT) * 4);
    f32x2 t2 = *reinterpret_cast<const f32x2*>(&lds[L_FIN + lane * 2]);
    f32x2 gv = *reinterpret_cast<const f32x2*>(&lds[L_G + (lane & 7) * 2]);
    unsigned xp = ldu[L_G + 16 + (lane & 7)];
    asm volatile("s_waitcnt lgkmcnt(0)" : "+v"(old), "+v"(t2), "+v"(gv), "+v"(xp));
    if (__builtin_amdgcn_readfirstlane(old) == 7) {
      t2.x += dpp<XOR8>(t2.x); t2.x = sum_x32(sum_x16(t2.x));
      t2.y += dpp<XOR8>(t2.y); t2.y = sum_x32(sum_x16(t2.y));
      const unsigned r = pk_bf16(lo_f(xp) + rnd(rnd(t2.x) * gv.x), hi_f(xp) + rnd(rnd(t2.y) * gv.y));
      __builtin_amdgcn_raw_buffer_store_b32(r, mk_rsrc(out, DM * 2), lane < 8 ? b * 32 + lane * 4 : OOB, 0, 0);
      if (b == 0 && lane == 0) __builtin_amdgcn_raw_buffer_store_b32(tag, rws, WS_EPOCH, 0, AUX_SC1);
    }
    gather_end<4>(pl);
  }
}

}

extern "C" void kernel_launch(void* const* d_in, const int* in_sizes, int n_in, void* d_out, int out_size, void* d_ws, size_t ws_size,
                              hipStream_t stream) {
  (void)in_sizes; (void)n_in; (void)out_size; (void)ws_size;
  fused_decode<<<NWG, NTHR, 0, stream>>>((const unsigned short*)d_in[0], (unsigned char*)d_ws, (const unsigned short*)d_in[3],
                                         (const unsigned short*)d_in[1], (const unsigned short*)d_in[6], (const unsigned short*)d_in[7],
                                         (const unsigned short*)d_in[8], (const unsigned short*)d_in[2], (const unsigned short*)d_in[4],
                                         (unsigned short*)d_out);
}
```

```cpp
#include <hip/hip_runtime.h>
#include <hip/hip_bf16.h>
#include <stdint.h>

namespace {

typedef unsigned v4u __attribute__((ext_vector_type(4)));
typedef unsigned v2u __attribute__((ext_vector_type(2)));
typedef float f32x4 __attribute__((ext_vector_type(4)));
typedef float f32x2 __attribute__((ext_vector_type(2)));
typedef __bf16 bf16x8 __attribute__((ext_vector_type(8)));
typedef __bf16 bf16x2 __attribute__((ext_vector_type(2)));
#define LAS __attribute__((address_space(3)))

constexpr int DM = 576, SK = 65536, FF = 4608, QD = 4096;
constexpr int NWG = 256;
constexpr int NSW = 4;
constexpr int NW = 9;
constexpr int NTHR = (NW + 1) * 64;
constexpr int NRED = 36;
constexpr int NFW = 72;
constexpr int TL = 4;
constexpr int NT = 64;
constexpr int OOB = 0x70000000;

constexpr int TAU_NOM = 874;
constexpr int TAU_DN = 35, TAU_UP = 22, TAU_FAST = 20;
constexpr int BURST = 20, STALL = 100;
constexpr int CP_D = 6;
constexpr int ML_AT = 74;
constexpr int X_MIN = 4, X_MAX = 20, X_C = 68;
constexpr int NEAR_AT = 124;

constexpr int WS_EPOCH = 0;
constexpr int WS_QG = 4096;
constexpr int WS_ML = WS_QG + 16 * 4 * 256 * 8;
constexpr int WS_CP = WS_ML + NWG * 128;
constexpr int WS_P1 = WS_CP + NWG * 128;
constexpr int WS_P2 = WS_P1 + 16 * 16 * 16 * 128;
constexpr int WS_PF = WS_P2 + NRED * NWG * 128;
constexpr int WS_GU = WS_PF + NRED * NFW * 128;
constexpr int WS_PAD = WS_GU + NFW * 2 * 64 * 16;
constexpr int WS_END = WS_PAD + 128;

constexpr int AUX_NT = 2, AUX_SC1 = 16;
constexpr int RSRC_W3 = 0x00020000;
constexpr unsigned SPIN_CAP = 1u << 16;

constexpr int L_FLAG = 0;
enum { F_TAG, F_QRDY, F_XRDY, F_FDONE, F_POLL, F_P1CNT, F_SRDY, F_ORDY, F_PROG, F_STALL, F_TEND, F_SLOPE, F_ENDCNT, F_FINCNT, F_S1CNT };
constexpr int L_MW = 16;
constexpr int L_O = L_MW + 16;
constexpr int L_G = L_O + 16;
constexpr int L_Q = L_G + 32;
constexpr int L_XN = L_Q + 128;
constexpr int L_YF = L_XN + 640;
constexpr int L_FIN = L_YF + 576;
constexpr int L_ACC = L_FIN + 128;
constexpr int L_SC = L_ACC + NSW * 256;
constexpr int L_FLOATS = 24 * 1024;
static_assert(L_SC % 4 == 0 && L_ACC % 4 == 0 && L_Q % 4 == 0 && L_O % 4 == 0 && L_SC + NSW * NT * 32 <= L_FLOATS, "LDS map");
static_assert(16 * NSW * NT * 16 == SK && X_MIN >= 4, "key split");

template <int CTRL> __device__ __forceinline__ float dpp(float x) {
  return __builtin_bit_cast(float, __builtin_amdgcn_mov_dpp(__builtin_bit_cast(int, x), CTRL, 0xf, 0xf, true));
}
template <int CTRL> __device__ __forceinline__ unsigned dppu(unsigned x) {
  return (unsigned)__builtin_amdgcn_mov_dpp((int)x, CTRL, 0xf, 0xf, true);
}
constexpr int XOR1 = 0xB1, XOR2 = 0x4E, XOR7 = 0x141, XOR8 = 0x128, ROR4 = 0x124;
__device__ __forceinline__ float sum_x16(float x) {
  auto s = __builtin_amdgcn_permlane16_swap(__float_as_uint(x), __float_as_uint(x), false, false);
  return __uint_as_float(s[0]) + __uint_as_float(s[1]);
}
__device__ __forceinline__ float sum_x32(float x) {
  auto s = __builtin_amdgcn_permlane32_swap(__float_as_uint(x), __float_as_uint(x), false, false);
  return __uint_as_float(s[0]) + __uint_as_float(s[1]);
}
__device__ __forceinline__ float max_x16(float x) {
  auto s = __builtin_amdgcn_permlane16_swap(__float_as_uint(x), __float_as_uint(x), false, false);
  return fmaxf(__uint_as_float(s[0]), __uint_as_float(s[1]));
}
__device__ __forceinline__ float max_x32(float x) {
  auto s = __builtin_amdgcn_permlane32_swap(__float_as_uint(x), __float_as_uint(x), false, false);
  return fmaxf(__uint_as_float(s[0]), __uint_as_float(s[1]));
}
__device__ __forceinline__ float wave_sum(float x) {
  x += dpp<XOR1>(x); x += dpp<XOR2>(x); x += dpp<ROR4>(x); x += dpp<XOR8>(x);
  return sum_x32(sum_x16(x));
}
__device__ __forceinline__ unsigned pk_bf16(float lo, float hi) {
  bf16x2 v = {(__bf16)lo, (__bf16)hi};
  return __builtin_bit_cast(unsigned, v);
}
__device__ __forceinline__ float rnd(float x) { return (float)(__bf16)x; }
__device__ __forceinline__ float lo_f(unsigned u) { return __uint_as_float(u << 16); }
__device__ __forceinline__ float hi_f(unsigned u) { return __uint_as_float(u & 0xffff0000u); }
__device__ __forceinline__ float dot2(unsigned a, unsigned b, float c) {
  return __builtin_amdgcn_fdot2_f32_bf16(__builtin_bit_cast(bf16x2, a), __builtin_bit_cast(bf16x2, b), c, false);
}
__device__ __forceinline__ __amdgpu_buffer_rsrc_t mk_rsrc(const void* p, int bytes) {
  return __builtin_amdgcn_make_buffer_rsrc((void*)p, 0, bytes, RSRC_W3);
}
__device__ __forceinline__ v4u and4(unsigned x, const unsigned (&m)[4]) { return v4u{x & m[0], x & m[1], x & m[2], x & m[3]}; }
__device__ __forceinline__ f32x4 mfma16(v4u a, v4u bm, f32x4 cacc) {
  return __builtin_amdgcn_mfma_f32_16x16x32_bf16(__builtin_bit_cast(bf16x8, a), __builtin_bit_cast(bf16x8, bm), cacc, 0, 0, 0);
}
__device__ __forceinline__ f32x4 gemv_fin(f32x4 d, int lane) {
  const bool valid = (lane >> 5) == ((lane >> 3) & 1);
#pragma unroll
  for (int r = 0; r < 4; ++r) { float t = valid ? d[r] : 0.f; t += dpp<XOR8>(t); d[r] = sum_x32(t); }
  return d;
}
__device__ __forceinline__ int lds_inc(unsigned addr) {
  int r; unsigned long long save;
  asm volatile("s_mov_b64 %1, exec\n\ts_mov_b64 exec, 1\n\tds_add_rtn_u32 %0, %2, %3\n\ts_mov_b64 exec, %1"
               : "=&v"(r), "=&s"(save) : "v"(addr), "v"(1) : "memory");
  return r;
}
template <int N>
__device__ __forceinline__ void gather(__amdgpu_buffer_rsrc_t rs, int (&off)[N], unsigned tag, float (&val)[2 * N], v4u (&p)[3][N]) {
  auto issue = [&](int j) {
    asm volatile("" ::: "memory");
#pragma unroll
    for (int k = 0; k < N; ++k) p[j][k] = __builtin_amdgcn_raw_buffer_load_b128(rs, off[k], 0, AUX_SC1);
    __builtin_amdgcn_sched_barrier(0);
  };
  auto check = [&](int j) {
    bool done = true;
#pragma unroll
    for (int k = 0; k < N; ++k) {
      const bool ok = off[k] != OOB && ((p[j][k].y ^ tag) | (p[j][k].w ^ tag)) == 0;
      val[2 * k] = ok ? __uint_as_float(p[j][k].x) : val[2 * k]; val[2 * k + 1] = ok ? __uint_as_float(p[j][k].z) : val[2 * k + 1];
      off[k] = ok ? OOB : off[k];
      done = done && off[k] == OOB;
    }
    return __all(done) != 0;
  };
#pragma unroll
  for (int k = 0; k < 2 * N; ++k) val[k] = 0.f;
  issue(0); __builtin_amdgcn_s_sleep(5); issue(1); __builtin_amdgcn_s_sleep(5);
#pragma unroll 1
  for (unsigned spins = 0; spins < SPIN_CAP; ++spins) {
    issue(2); if (check(0)) break;
    issue(0); if (check(1)) break;
    issue(1); if (check(2)) break;
  }
}
template <int N>
__device__ __forceinline__ void gather_end(v4u (&p)[3][N]) {
#pragma unroll
  for (int j = 0; j < 3; ++j)
#pragma unroll
    for (int k = 0; k < N; ++k) asm volatile("" :: "v"(p[j][k]));
}
__device__ __forceinline__ void pace(unsigned& next, unsigned tau, unsigned& stall) {
  int d;
  asm volatile("1:\n\ts_memrealtime vcc\n\ts_waitcnt lgkmcnt(0)\n\ts_lshl_b32 %0, vcc_lo, 4\n\ts_sub_i32 %0, %0, %1\n\ts_cmp_ge_i32 %0, 0\n\ts_cbranch_scc1 2f\n\ts_sleep 1\n\ts_branch 1b\n2:"
               : "=&s"(d) : "s"(next) : "scc", "vcc");
  next += tau + (unsigned)max(0, d - BURST * 16);
  stall += d > STALL * 16 ? d : 0;
}

__global__ void __launch_bounds__(NTHR, 3)
fused_decode(const unsigned short* __restrict__ xg, unsigned char* __restrict__ ws, const unsigned short* __restrict__ wq,
             const unsigned short* __restrict__ kg, const unsigned short* __restrict__ wgate, const unsigned short* __restrict__ wup,
             const unsigned short* __restrict__ wdown, const unsigned short* __restrict__ vg, const unsigned short* __restrict__ wo,
             unsigned short* __restrict__ out) {
  __shared__ __attribute__((aligned(16))) float lds[L_FLOATS];
  asm volatile("" :: "s"(xg), "s"(ws), "s"(wq), "s"(kg), "s"(wgate), "s"(wup), "s"(wdown));
  const int lane = threadIdx.x & 63, pw = __builtin_amdgcn_readfirstlane(threadIdx.x >> 6);
  const int w = pw == 0 ? NW : pw - 1;
  const int b = blockIdx.x, g = b >> 4, s = b & 15;
  const int c = lane & 15;
  const int sr = (s + 3 * g) & 15, ns2 = 5 - (g & 1);
  const bool is1 = sr < 9, is2 = !is1 && sr < 9 + ns2;
  const int f1 = g * 9 + sr, f2 = (g >> 1) * 9 + (g & 1) * 5 + sr - 9;
  unsigned* const ldu = reinterpret_cast<unsigned*>(lds);
  const unsigned lds0 = (unsigned)(uintptr_t)(LAS float*)lds;

  const __amdgpu_buffer_rsrc_t rws = mk_rsrc(ws, WS_END);
  const __amdgpu_buffer_rsrc_t rx = mk_rsrc(xg, DM * 2);

  auto wait_flag = [&](int idx, unsigned want) {
#pragma unroll 1
    for (unsigned spins = 0; spins < (SPIN_CAP << 6); ++spins) {
      if (__builtin_amdgcn_readfirstlane(*(volatile LAS unsigned*)&ldu[L_FLAG + idx]) >= want) break;
      __builtin_amdgcn_s_sleep(1);
    }
    asm volatile("" ::: "memory");
  };
  auto set_flag = [&](int idx) {
    asm volatile("" ::: "memory");
    if (lane == 0) *(volatile LAS unsigned*)&ldu[L_FLAG + idx] = 1u;
  };
  auto count_up = [&](int idx) {
    int r = lds_inc(lds0 + (L_FLAG + idx) * 4);
    asm volatile("s_waitcnt lgkmcnt(0)" : "+v"(r));
  };
  unsigned gm[4];
#pragma unroll
  for (int r = 0; r < 4; ++r) gm[r] = ((lane & 7) >> 1) == r ? ((lane & 1) ? 0xffff0000u : 0x0000ffffu) : 0u;
  unsigned tag;

  if (__builtin_expect(w == NW, 1)) {
    __builtin_amdgcn_s_setprio(3);
    unsigned xd[5], xq[3];
#pragma unroll
    for (int k = 0; k < 5; ++k) xd[k] = __builtin_amdgcn_raw_buffer_load_b32(rx, lane * 4 + k * 256, 0, 0);
#pragma unroll
    for (int k = 0; k < 3; ++k) xq[k] = __builtin_amdgcn_raw_buffer_load_b16(rx, (144 * (s >> 2) + lane + 64 * k) * 2, 0, 0);
    const unsigned epoch = __builtin_amdgcn_raw_buffer_load_b32(rws, lane == 0 ? WS_EPOCH : OOB, 0, AUX_SC1);
    v4u wqv[18];
    {
      const __amdgpu_buffer_rsrc_t rq = mk_rsrc(wq, DM * QD * 2);
      const int o = (144 * (s >> 2) + (lane >> 3)) * (QD * 2) + (g * 4 + (s & 3)) * 128 + (lane & 7) * 16;
#pragma unroll
      for (int i = 0; i < 18; ++i) wqv[i] = __builtin_amdgcn_raw_buffer_load_b128(rq, o, i * 8 * QD * 2, AUX_NT);
    }
    if (lane < 16) ldu[L_FLAG + lane] = 0u;
    asm volatile("s_waitcnt lgkmcnt(0)" ::: "memory");
    __builtin_amdgcn_sched_barrier(0);
    __builtin_amdgcn_s_barrier();
    __builtin_amdgcn_sched_barrier(0);
    float mb, den;
    {
      float xv[10];
#pragma unroll
      for (int k = 0; k < 5; ++k) { xv[2 * k] = lo_f(xd[k]); xv[2 * k + 1] = hi_f(xd[k]); }
      float sm = 0.f;
#pragma unroll
      for (int k = 0; k < 10; ++k) sm += xv[k];
      const float mean = wave_sum(sm) * (1.0f / DM);
      float sq = 0.f;
#pragma unroll
      for (int k = 0; k < 8; ++k) { const float d = xv[k] - mean; sq = fmaf(d, d, sq); }
      const float d8 = lane < 32 ? xv[8] - mean : 0.f, d9 = lane < 32 ? xv[9] - mean : 0.f;
      sq = fmaf(d8, d8, sq); sq = fmaf(d9, d9, sq);
      const float var = wave_sum(sq) * (1.0f / DM);
      mb = rnd(mean);
      den = rnd(sqrtf(rnd(rnd(var) + 1e-5f)));
    }
    unsigned xn[3];
#pragma unroll
    for (int k = 0; k < 3; ++k) { const float v = rnd(lo_f(xq[k]) - mb) / den; xn[k] = pk_bf16(v, v); }
    unsigned xr[18];
#pragma unroll
    for (int i = 0; i < 18; ++i) xr[i] = __shfl(xn[i >> 3], (8 * i + (lane >> 3)) & 63);
#pragma unroll
    for (int i = 0; i < 18; ++i) asm volatile("" : "+v"(xr[i]));
    __builtin_amdgcn_sched_barrier(0);
    f32x4 qa = {0.f, 0.f, 0.f, 0.f};
#pragma unroll
    for (int i = 0; i < 18; ++i) { qa = mfma16(and4(xr[i], gm), wqv[i], qa); __builtin_amdgcn_sched_barrier(0); }
    qa = gemv_fin(qa, lane);
    tag = __builtin_amdgcn_readfirstlane(epoch) + 1u;
    {
      const bool up2 = (lane & 8) != 0;
      const int col = (lane & 7) * 8 + 4 * ((lane >> 4) & 1) + (up2 ? 2 : 0);
      __builtin_amdgcn_raw_buffer_store_b128(v4u{__float_as_uint(up2 ? qa[2] : qa[0]), tag, __float_as_uint(up2 ? qa[3] : qa[1]), tag}, rws,
                                             lane < 32 ? WS_QG + ((g * 4 + (s >> 2)) * 256 + (s & 3) * 64 + col) * 8 : OOB, 0, AUX_SC1);
    }
    if (lane == 0) ldu[L_FLAG + F_TAG] = tag;
    if (is1) {
#pragma unroll
      for (int k = 0; k < 5; ++k) {
        const float v0 = rnd(lo_f(xd[k]) - mb) / den, v1 = rnd(hi_f(xd[k]) - mb) / den;
        *reinterpret_cast<v2u*>(&ldu[L_XN + 2 * (lane + 64 * k)]) = v2u{pk_bf16(v0, v0), pk_bf16(v1, v1)};
      }
      set_flag(F_XRDY);
    }
    for (int i = 0; i < 5; ++i) __builtin_amdgcn_s_sleep(8);
    float q4[4];
    for (unsigned spins = 0;; ++spins) {
      asm volatile("" ::: "memory");
      v4u a[8];
#pragma unroll
      for (int r = 0; r < 4; ++r) {
        a[2 * r] = __builtin_amdgcn_raw_buffer_load_b128(rws, WS_QG + g * 8192 + lane * 32, r * 2048, AUX_SC1);
        a[2 * r + 1] = __builtin_amdgcn_raw_buffer_load_b128(rws, WS_QG + g * 8192 + lane * 32 + 16, r * 2048, AUX_SC1);
      }
      __builtin_amdgcn_sched_barrier(0);
      set_flag(F_POLL);
      __builtin_amdgcn_sched_barrier(0);
      unsigned bad = 0;
#pragma unroll
      for (int j = 0; j < 4; ++j) q4[j] = 0.f;
#pragma unroll
      for (int r = 0; r < 4; ++r) {
        bad |= (a[2 * r].y ^ tag) | (a[2 * r].w ^ tag) | (a[2 * r + 1].y ^ tag) | (a[2 * r + 1].w ^ tag);
        q4[0] += __uint_as_float(a[2 * r].x); q4[1] += __uint_as_float(a[2 * r].z);
        q4[2] += __uint_as_float(a[2 * r + 1].x); q4[3] += __uint_as_float(a[2 * r + 1].z);
      }
      if (__all(bad == 0) || spins > SPIN_CAP) break;
      __builtin_amdgcn_s_sleep(1);
    }
    *reinterpret_cast<v2u*>(&ldu[L_Q + 2 * lane]) = v2u{pk_bf16(q4[0], q4[1]), pk_bf16(q4[2], q4[3])};
    set_flag(F_QRDY);
    __builtin_amdgcn_s_setprio(0);
    float slope = (float)TAU_NOM;
    unsigned hist = 0, prev16 = 0, pstall = 0;
    auto checkpoint = [&](int k, int idx, int span) {
      wait_flag(F_PROG, idx);
      const unsigned t1 = (unsigned)__builtin_amdgcn_s_memrealtime();
      const unsigned stall = __builtin_amdgcn_readfirstlane(*(volatile LAS unsigned*)&ldu[L_FLAG + F_STALL]);
      hist = lane == k ? t1 : hist;
      const unsigned tg = lane <= k ? tag : ~tag;
      __builtin_amdgcn_raw_buffer_store_b128(v4u{hist, tg, hist, tg}, rws, lane < 8 ? WS_CP + b * 128 + lane * 16 : OOB, 0, AUX_SC1);
      wait_flag(F_PROG, idx + CP_D);
      const v2u cp = __builtin_amdgcn_raw_buffer_load_b64(rws, WS_CP + (g * 16 + c) * 128 + k * 16, 0, AUX_SC1);
      float dt = cp.y == tag ? (float)(int)(cp.x - t1) : (float)(CP_D * TAU_NOM / 16);
      dt += dpp<XOR1>(dt); dt += dpp<XOR2>(dt); dt += dpp<ROR4>(dt); dt += dpp<XOR8>(dt);
      const unsigned t16 = (t1 << 4) + (unsigned)(int)dt;
      if (k > 0) {
        const float meas = (float)(int)(t16 - prev16 - (stall - pstall)) / (float)span;
        slope = fminf(meas > slope * 1.006f ? meas * 1.01f : fmaxf((float)TAU_NOM, slope * 0.995f), 2.0f * TAU_NOM);
      }
      prev16 = t16; pstall = stall;
      if (lane == 0) *reinterpret_cast<v2u*>(&ldu[L_FLAG + F_TEND]) = v2u{(t16 + (unsigned)(int)(slope * (float)(2 * NT - idx)) - stall) | 1u, (unsigned)(int)slope};
    };
    checkpoint(0, 12, 0); checkpoint(1, 32, 20); checkpoint(2, 56, 24);
    wait_flag(F_P1CNT, NSW);
    {
      const float mw = lds[L_MW + (lane & 7)];
      float mx = fmaxf(mw, dpp<XOR2>(mw)); mx = fmaxf(mx, dpp<ROR4>(mx));
      float l = 0.f;
#pragma unroll 2
      for (int i = 0; i < NSW * NT / 8; ++i) {
        const f32x4 v = *reinterpret_cast<const f32x4*>(&lds[L_SC + i * 256 + lane * 4]);
        l += (__builtin_amdgcn_exp2f(v[0] - mx) + __builtin_amdgcn_exp2f(v[1] - mx)) + (__builtin_amdgcn_exp2f(v[2] - mx) + __builtin_amdgcn_exp2f(v[3] - mx));
      }
      l += dpp<XOR2>(l); l += dpp<ROR4>(l); l += dpp<XOR8>(l); l = sum_x32(sum_x16(l));
      __builtin_amdgcn_raw_buffer_store_b128(v4u{__float_as_uint(mx), tag, __float_as_uint(l), tag}, rws,
                                             lane < 8 ? WS_ML + b * 128 + lane * 16 : OOB, 0, AUX_SC1);
      wait_flag(F_PROG, ML_AT);
      v4u ml;
      for (unsigned spins = 0;; ++spins) {
        asm volatile("" ::: "memory");
        ml = __builtin_amdgcn_raw_buffer_load_b128(rws, WS_ML + (g * 16 + ((lane >> 1) & 15)) * 128 + (lane & 1) * 16, 0, AUX_SC1);
        if (__all(((ml.y ^ tag) | (ml.w ^ tag)) == 0) || spins > SPIN_CAP) break;
        __builtin_amdgcn_s_sleep(32);
      }
      const float mp = __uint_as_float(ml.x);
      float M = fmaxf(mp, dpp<XOR2>(mp)); M = fmaxf(M, dpp<ROR4>(M)); M = fmaxf(M, dpp<XOR8>(M)); M = max_x16(M);
      float L = __builtin_amdgcn_exp2f(mp - M) * __uint_as_float(ml.z);
      asm volatile("" : "+v"(L));
      L += dpp<XOR2>(L); L += dpp<ROR4>(L); L += dpp<XOR8>(L); L = sum_x16(L);
      if (lane < 8) lds[L_MW + lane] = __builtin_amdgcn_exp2f(mw - M) / L;
      set_flag(F_SRDY);
    }
    checkpoint(3, 84, 28);
    if (is2) {
      wait_flag(F_PROG, NEAR_AT);
      wait_flag(F_FDONE, 3);
#pragma unroll
      for (int k = 0; k < 5; ++k) {
        const int t = k * 64 + lane;
        const f32x2 yf = *reinterpret_cast<const f32x2*>(&lds[L_YF + 2 * (t < 288 ? t : 0)]);
        __builtin_amdgcn_raw_buffer_store_b128(v4u{__float_as_uint(yf.x), tag, __float_as_uint(yf.y), tag}, rws,
                                               t < 288 ? WS_PF + ((t >> 3) * NFW + f2) * 128 + (t & 7) * 16 : OOB, 0, AUX_SC1);
      }
    }
    __builtin_amdgcn_s_setprio(3);
    wait_flag(F_ENDCNT, NSW);
    for (int i = 0; i < 3; ++i) __builtin_amdgcn_s_sleep(4);
    {
      int o2[2] = {WS_P1 + (g * 16 + s) * 2048 + lane * 16, WS_P1 + (g * 16 + s) * 2048 + 1024 + lane * 16};
      float pv4[4];
      v4u pl[3][2];
      gather<2>(rws, o2, tag, pv4, pl);
      auto h0 = __builtin_amdgcn_permlane32_swap(__float_as_uint(pv4[0] + pv4[2]), __float_as_uint(pv4[1] + pv4[3]), false, false);
      float v = sum_x16(__uint_as_float(h0[0]) + __uint_as_float(h0[1]));
      v += dpp<XOR8>(v);
      if ((lane & 24) == 0) lds[L_O + 2 * (lane & 7) + (lane >> 5)] = rnd(v);
      set_flag(F_ORDY);
      gather_end<2>(pl);
    }
    return;
  }

  const int nrole = is1 ? 4 : is2 ? 3 : 0;
  const int voff = lane * 16;
  __builtin_amdgcn_sched_barrier(0);
  __builtin_amdgcn_s_barrier();
  __builtin_amdgcn_sched_barrier(0);

  if (w >= NSW && w < NSW + nrole) {
    const int fw = w - NSW;
    v4u fr[24];
    if (is1) {
      const __amdgpu_buffer_rsrc_t rfg = mk_rsrc(wgate, DM * FF * 2);
      const __amdgpu_buffer_rsrc_t rfu = mk_rsrc(wup, DM * FF * 2);
      const int row0 = 288 * (f1 & 1) + 72 * fw + (lane >> 3);
      const int foff = row0 * (FF * 2) + (f1 >> 1) * 128 + (lane & 7) * 16;
#pragma unroll
      for (int i = 0; i < 9; ++i) {
        fr[2 * i] = __builtin_amdgcn_raw_buffer_load_b128(rfg, foff, i * 8 * FF * 2, AUX_NT);
        fr[2 * i + 1] = __builtin_amdgcn_raw_buffer_load_b128(rfu, foff, i * 8 * FF * 2, AUX_NT);
        __builtin_amdgcn_sched_barrier(0); asm volatile("s_waitcnt vmcnt(6)"); __builtin_amdgcn_sched_barrier(0);
      }
      wait_flag(F_XRDY, 1);
      tag = ldu[L_FLAG + F_TAG];
      f32x4 ag = {0.f, 0.f, 0.f, 0.f}, au = {0.f, 0.f, 0.f, 0.f};
#pragma unroll
      for (int i = 0; i < 9; ++i) {
        const v4u av = and4(ldu[L_XN + row0 + 8 * i], gm);
        ag = mfma16(av, fr[2 * i], ag); au = mfma16(av, fr[2 * i + 1], au);
      }
      ag = gemv_fin(ag, lane); au = gemv_fin(au, lane);
      if ((lane & 8) == 0) *reinterpret_cast<f32x4*>(&lds[L_YF + fw * 128 + (lane >> 5) * 64 + (lane & 7) * 8 + 4 * ((lane >> 4) & 1)]) = lane < 32 ? ag : au;
      int old = lds_inc(lds0 + (L_FLAG + F_S1CNT) * 4);
      float gs = 0.f, us = 0.f;
#pragma unroll
      for (int ww = 0; ww < 4; ++ww) { gs += lds[L_YF + ww * 128 + lane]; us += lds[L_YF + ww * 128 + 64 + lane]; }
      asm volatile("s_waitcnt lgkmcnt(0)" : "+v"(old), "+v"(gs), "+v"(us));
      if (__builtin_amdgcn_readfirstlane(old) == 3)
        __builtin_amdgcn_raw_buffer_store_b128(v4u{__float_as_uint(gs), tag, __float_as_uint(us), tag}, rws, WS_GU + (f1 * 64 + lane) * 16, 0, AUX_SC1);
    } else {
      const __amdgpu_buffer_rsrc_t rwd = mk_rsrc(wdown + (size_t)f2 * 64 * DM, 64 * DM * 2);
      const int doff = (lane >> 3) * (DM * 2) + fw * 384 + (lane & 7) * 16;
#pragma unroll
      for (int rg = 0; rg < 8; ++rg) {
#pragma unroll
        for (int k = 0; k < 3; ++k) fr[rg * 3 + k] = __builtin_amdgcn_raw_buffer_load_b128(rwd, doff + k * 128, rg * 8 * DM * 2, AUX_NT);
        __builtin_amdgcn_sched_barrier(0); asm volatile("s_waitcnt vmcnt(7)"); __builtin_amdgcn_sched_barrier(0);
      }
      wait_flag(F_QRDY, 1);
      tag = ldu[L_FLAG + F_TAG];
      for (int i = 0; i < 8; ++i) __builtin_amdgcn_s_sleep(8);
      v4u p0, p1;
      for (unsigned spins = 0;; ++spins) {
        asm volatile("" ::: "memory");
        p0 = __builtin_amdgcn_raw_buffer_load_b128(rws, WS_GU + (f2 * 128 + lane) * 16, 0, AUX_SC1);
        p1 = __builtin_amdgcn_raw_buffer_load_b128(rws, WS_GU + (f2 * 128 + lane) * 16, 1024, AUX_SC1);
        if (__all(((p0.y ^ tag) | (p0.w ^ tag) | (p1.y ^ tag) | (p1.w ^ tag)) == 0) || spins > SPIN_CAP) break;
        __builtin_amdgcn_s_sleep(16);
      }
      const float gt = rnd(__uint_as_float(p0.x) + __uint_as_float(p1.x)), up = rnd(__uint_as_float(p0.z) + __uint_as_float(p1.z));
      const float inner = 0.7978845608028654f * (gt + 0.044715f * gt * gt * gt);
      const float hv = rnd(0.5f * gt * (1.0f + tanhf(inner))) * up;
      const unsigned hp = pk_bf16(hv, hv);
      f32x4 ya[3];
#pragma unroll
      for (int k = 0; k < 3; ++k) ya[k] = f32x4{0.f, 0.f, 0.f, 0.f};
#pragma unroll
      for (int rg = 0; rg < 8; ++rg) {
        const v4u av = and4(__shfl(hp, rg * 8 + (lane >> 3)), gm);
#pragma unroll
        for (int k = 0; k < 3; ++k) ya[k] = mfma16(av, fr[rg * 3 + k], ya[k]);
      }
#pragma unroll
      for (int k = 0; k < 3; ++k) {
        ya[k] = gemv_fin(ya[k], lane);
        if (lane < 32 && (lane & 8) == 0) *reinterpret_cast<f32x4*>(&lds[L_YF + fw * 192 + k * 64 + (lane & 7) * 8 + 4 * (lane >> 4)]) = ya[k];
      }
      count_up(F_FDONE);
    }
  } else if (w < NSW) {
    const __amdgpu_buffer_rsrc_t rk = mk_rsrc(kg + (size_t)g * SK * 128, SK * 256);
    const __amdgpu_buffer_rsrc_t rv = mk_rsrc(vg + (size_t)g * SK * 128, SK * 256);
    const int tfirst = ((s & 7) << 21) + ((s >> 3) << 15) + (w << 12);
#define TOFF(t) (tfirst + (((t) >> 1) << 16))
#define TSUB 16384
#define VOFF(t) ((t) < NT ? TOFF(t) + ((t) & 1) * TSUB : OOB)
#define tb(n, i) buf[(n) * TL + (i)]
    v4u buf[4 * TL];
#pragma unroll
    for (int i = 0; i < 2 * TL; ++i) buf[i] = __builtin_amdgcn_raw_buffer_load_b128(rk, voff + (i & 3) * 1024, TOFF(0) + (i >> 2) * TSUB, AUX_NT);
    __builtin_amdgcn_sched_barrier(0);
    wait_flag(F_POLL, 1);
#pragma unroll
    for (int i = 0; i < 2 * TL; ++i) buf[2 * TL + i] = __builtin_amdgcn_raw_buffer_load_b128(rk, voff + (i & 3) * 1024, TOFF(2) + (i >> 2) * TSUB, AUX_NT);
    __builtin_amdgcn_sched_barrier(0);
    wait_flag(F_QRDY, 1);
    const int hf = (c >> 2) & 1;
    const v4u qf = *reinterpret_cast<const v4u*>(&ldu[L_Q + hf * 64 + 4 * c]);
    const v4u qs = *reinterpret_cast<const v4u*>(&ldu[L_Q + (hf ^ 1) * 64 + 4 * c]);
    constexpr float LOG2E = 1.4426950408889634f, SCALE = 0.08838834764831845f;
    const bool b0 = (c & 1) != 0, b1 = (c & 2) != 0;
    float* const scw = &lds[L_SC + w * NT * 32 + (lane >> 4) * 8 + (lane & 7)];
    float m = -INFINITY;
    float y[4];
    auto qk1 = [&](const v4u* kt) {
#pragma unroll
      for (int i = 0; i < TL; ++i) {
        float xf = 0.f, xs = 0.f;
#pragma unroll
        for (int j = 0; j < 4; ++j) { xf = dot2(kt[i][j], qf[j], xf); xs = dot2(kt[i][j], qs[j], xs); }
        y[i] = xf + dpp<XOR7>(xs);
      }
      asm volatile("" : "+v"(y[0]), "+v"(y[1]), "+v"(y[2]), "+v"(y[3]));
      __builtin_amdgcn_sched_barrier(0);
    };
    auto qk2 = [&](int t) {
      const float u0 = (b0 ? y[1] : y[0]) + dpp<XOR1>(b0 ? y[0] : y[1]);
      const float u1 = (b0 ? y[3] : y[2]) + dpp<XOR1>(b0 ? y[2] : y[3]);
      float sc = (b1 ? u1 : u0) + dpp<XOR2>(b1 ? u0 : u1);
      sc += dpp<XOR8>(sc);
      const float tl2 = rnd(rnd(sc) * SCALE) * LOG2E;
      m = fmaxf(m, tl2);
      scw[t * 32] = tl2;
    };
#pragma unroll
    for (int n = 0; n < 2; ++n) {
      qk1(&tb(n, 0));
#pragma unroll
      for (int i = 0; i < TL; ++i) tb(n, i) = __builtin_amdgcn_raw_buffer_load_b128(rk, voff + i * 1024, TOFF(4) + n * TSUB, AUX_NT);
      __builtin_amdgcn_sched_barrier(0);
      qk2(n);
    }
    qk1(&tb(2, 0)); qk2(2);
    qk1(&tb(3, 0)); qk2(3);
    unsigned next = (unsigned)__builtin_amdgcn_s_memrealtime() << 4, tau = TAU_NOM;
    unsigned stall = 0, tend = 0;
    int slope = TAU_NOM;
    auto aim = [&](int done) {
      if (w == 0 && lane == 0) *(volatile LAS v2u*)&ldu[L_FLAG + F_PROG] = v2u{(unsigned)done, stall};
      if (done & 2) return;
      const v2u te = *(volatile LAS v2u*)&ldu[L_FLAG + F_TEND];
      const unsigned t0 = __builtin_amdgcn_readfirstlane(te.x);
      if (t0 == 0) return;
      tend = t0; slope = __builtin_amdgcn_readfirstlane(te.y);
      const int v = __builtin_amdgcn_readfirstlane((int)((float)(int)(tend + stall - next) * __builtin_amdgcn_rcpf((float)(2 * NT - done))));
      tau = (unsigned)min(slope + TAU_UP, max(slope - TAU_DN, v));
    };
#pragma unroll 1
    for (int t = 4; t < NT; t += 2) {
      const bool lastk = t + 2 >= NT;
      const __amdgpu_buffer_rsrc_t rn = lastk ? rv : rk;
      const int nxt = TOFF(lastk ? 0 : t + 2);
      if (t <= 12) stall = 0;
      aim(t);
#pragma unroll
      for (int n = 0; n < 2; ++n) {
        qk1(&tb(n, 0));
        pace(next, tau, stall);
#pragma unroll
        for (int i = 0; i < TL; ++i) tb(n, i) = __builtin_amdgcn_raw_buffer_load_b128(rn, voff + i * 1024, nxt + n * TSUB, AUX_NT);
        __builtin_amdgcn_sched_barrier(0);
        qk2(t + n);
      }
    }
    m = fmaxf(m, dpp<XOR1>(m)); m = fmaxf(m, dpp<XOR2>(m));
    m = max_x32(max_x16(m));
    if ((lane & 59) == 0) lds[L_MW + 2 * w + (lane >> 2)] = m;
    count_up(F_P1CNT);
    f32x4 acc = {0.f, 0.f, 0.f, 0.f};
    unsigned amask[4];
    {
      const int jp = (c & 3) + 4 * (c >> 3);
#pragma unroll
      for (int r = 0; r < 4; ++r) amask[r] = (jp >> 1) == r ? ((jp & 1) ? 0xffff0000u : 0x0000ffffu) : 0u;
    }
    auto pv = [&](float sv, const v4u* vt) {
      const float p = __builtin_amdgcn_exp2f(sv - m);
      const unsigned pp = pk_bf16(p, p);
#pragma unroll
      for (int i = 0; i < TL; ++i) {
        const unsigned pb = i == 0 ? dppu<0x00>(pp) : i == 1 ? dppu<0x55>(pp) : i == 2 ? dppu<0xAA>(pp) : dppu<0xFF>(pp);
        acc = mfma16(and4(pb, amask), vt[i], acc);
      }
      asm volatile("" : "+v"(acc));
      __builtin_amdgcn_sched_barrier(0);
    };
    int t = 0;
    auto vrun = [&](int lim) {
#pragma unroll 1
      for (; t < lim; t += 2) {
        aim(NT + t);
        const int nxt = TOFF(t + 2);
        const float s0 = scw[t * 32], s1 = scw[t * 32 + 32];
#pragma unroll
        for (int n = 0; n < 2; ++n) {
          pv(n ? s1 : s0, &tb(n, 0));
          pace(next, tau, stall);
#pragma unroll
          for (int i = 0; i < TL; ++i) tb(n, i) = __builtin_amdgcn_raw_buffer_load_b128(rv, voff + i * 1024, nxt + n * TSUB, AUX_NT);
          __builtin_amdgcn_sched_barrier(0);
        }
      }
    };
    vrun(NT - X_MAX);
    {
      const int late = min(1 << 16, max(0, (int)(next + X_MAX * (slope - TAU_FAST) - tend - stall)));
      vrun(NT - min(X_MAX, X_MIN + 2 * ((late * X_C) >> 15)));
    }
#pragma unroll
    for (int k = 2; k < 4; ++k) {
#pragma unroll
      for (int i = 0; i < TL; ++i) tb(k, i) = __builtin_amdgcn_raw_buffer_load_b128(rv, voff + i * 1024, TOFF(t + 2) + (k & 1) * TSUB, AUX_NT);
      __builtin_amdgcn_sched_barrier(0);
    }
    __builtin_amdgcn_s_setprio(2);
    wait_flag(F_SRDY, 1);
    const float ed = lds[L_MW + 2 * w + ((lane >> 4) & 1)];
    tag = ldu[L_FLAG + F_TAG];
    const int po = WS_P1 + ((g * 16 + (lane >> 3)) * 16 + s) * 128 + (lane & 7) * 16;
#pragma unroll 1
    for (; t + 4 < NT; t += 4) {
      if (w == 0 && lane == 0) *(volatile LAS int*)&ldu[L_FLAG + F_PROG] = NT + t + 2;
#pragma unroll
      for (int k = 0; k < 4; ++k) {
        const int o = VOFF(t + k + 4);
        pv(scw[(t + k) * 32], &tb(k, 0));
#pragma unroll
        for (int i = 0; i < TL; ++i) tb(k, i) = __builtin_amdgcn_raw_buffer_load_b128(rv, voff + i * 1024, o, AUX_NT);
        __builtin_amdgcn_sched_barrier(0);
      }
    }
    {
      if (w == 0 && lane == 0) *(volatile LAS int*)&ldu[L_FLAG + F_PROG] = 126;
      v4u av[4][TL];
#pragma unroll
      for (int k = 0; k < 4; ++k) {
        const float p = __builtin_amdgcn_exp2f((t + k < NT ? scw[(t + k) * 32] : -INFINITY) - m);
        const unsigned pp = pk_bf16(p, p);
        av[k][0] = and4(dppu<0x00>(pp), amask); av[k][1] = and4(dppu<0x55>(pp), amask);
        av[k][2] = and4(dppu<0xAA>(pp), amask); av[k][3] = and4(dppu<0xFF>(pp), amask);
        asm volatile("" : "+v"(av[k][0]), "+v"(av[k][1]), "+v"(av[k][2]), "+v"(av[k][3]));
      }
      __builtin_amdgcn_sched_barrier(0);
#pragma unroll
      for (int k = 0; k < 2; ++k)
#pragma unroll
        for (int i = 0; i < TL; ++i) acc = mfma16(av[k][i], tb(k, i), acc);
      if (t + 2 < NT) {
#pragma unroll
        for (int k = 2; k < 4; ++k)
#pragma unroll
          for (int i = 0; i < TL; ++i) acc = mfma16(av[k][i], tb(k, i), acc);
      }
    }
    *reinterpret_cast<f32x4*>(&lds[L_ACC + w * 256 + ((lane >> 4) & 1) * 128 + 8 * c + 4 * (lane >> 5)]) = acc * ed;
    int old = lds_inc(lds0 + (L_FLAG + F_ENDCNT) * 4);
    f32x2 o0 = {0.f, 0.f}, o1 = {0.f, 0.f};
#pragma unroll
    for (int ww = 0; ww < NSW; ++ww) { o0 += *reinterpret_cast<const f32x2*>(&lds[L_ACC + ww * 256 + 2 * lane]); o1 += *reinterpret_cast<const f32x2*>(&lds[L_ACC + ww * 256 + 128 + 2 * lane]); }
    asm volatile("s_waitcnt lgkmcnt(0)" : "+v"(old), "+v"(o0), "+v"(o1));
    if (__builtin_amdgcn_readfirstlane(old) == NSW - 1) {
      __builtin_amdgcn_raw_buffer_store_b128(v4u{__float_as_uint(o0[0]), tag, __float_as_uint(o0[1]), tag}, rws, po, 0, AUX_SC1);
      __builtin_amdgcn_raw_buffer_store_b128(v4u{__float_as_uint(o1[0]), tag, __float_as_uint(o1[1]), tag}, rws, po, 8 * 2048, AUX_SC1);
    }
  }

  if (w >= NSW) {
    const int t = (w - NSW) * 64 + lane;
    const __amdgpu_buffer_rsrc_t rwo = mk_rsrc(wo + (size_t)b * 16 * DM, 16 * DM * 2);
    if (w == NW - 1) {
      const unsigned t0 = __builtin_amdgcn_raw_buffer_load_b32(mk_rsrc(vg + (size_t)g * SK * 128, SK * 256), lane < 2 ? ((s & 7) << 21) + lane * 0x1ffffc : OOB, 0, AUX_NT);
      const unsigned t1 = __builtin_amdgcn_raw_buffer_load_b32(rwo, lane == 0 ? 0 : OOB, 0, AUX_NT);
      const unsigned t2 = __builtin_amdgcn_raw_buffer_load_b32(mk_rsrc(out, DM * 2), lane == 0 && b < NRED ? b * 32 : OOB, 0, AUX_SC1);
      const unsigned t3 = __builtin_amdgcn_raw_buffer_load_b32(rws, lane == 0 ? WS_PAD : OOB, 0, AUX_NT);
      asm volatile("" :: "v"(t0), "v"(t1), "v"(t2), "v"(t3));
    }
#pragma unroll 1
    for (unsigned spins = 0; spins < (SPIN_CAP << 5); ++spins) {
      if (__builtin_amdgcn_readfirstlane(*(volatile LAS unsigned*)&ldu[L_FLAG + F_ENDCNT]) >= 1u) break;
      __builtin_amdgcn_s_sleep(2);
    }
    float wf[32];
    {
      unsigned wod[16];
#pragma unroll
      for (int j = 0; j < 16; ++j) wod[j] = __builtin_amdgcn_raw_buffer_load_b32(rwo, t < 288 ? t * 4 : OOB, j * DM * 2, AUX_NT);
#pragma unroll
      for (int j = 0; j < 16; ++j) { wf[2 * j] = lo_f(wod[j]); wf[2 * j + 1] = hi_f(wod[j]); }
#pragma unroll
      for (int j = 0; j < 32; ++j) asm volatile("" : "+v"(wf[j]));
    }
    tag = ldu[L_FLAG + F_TAG];
    const int po = t < 288 ? WS_P2 + ((t >> 3) * NWG + b) * 128 + (t & 7) * 16 : OOB;
    f32x4 o4[4];
#pragma unroll 1
    for (unsigned spins = 0; spins < (SPIN_CAP << 8); ++spins) {
      unsigned f = *(volatile LAS unsigned*)&ldu[L_FLAG + F_ORDY];
      asm volatile("" ::: "memory");
#pragma unroll
      for (int k = 0; k < 4; ++k) o4[k] = *reinterpret_cast<const f32x4*>(&lds[L_O + 4 * k]);
      asm volatile("" : "+v"(o4[0]), "+v"(o4[1]), "+v"(o4[2]), "+v"(o4[3]) :: "memory");
      if (__builtin_amdgcn_readfirstlane(f) != 0u) break;
    }
    f32x2 ya = {0.f, 0.f}, yb = {0.f, 0.f};
#pragma unroll
    for (int j = 0; j < 16; j += 2) {
      ya += o4[j >> 2][j & 3] * f32x2{wf[2 * j], wf[2 * j + 1]};
      yb += o4[j >> 2][(j & 3) + 1] * f32x2{wf[2 * j + 2], wf[2 * j + 3]};
    }
    __builtin_amdgcn_raw_buffer_store_b128(v4u{__float_as_uint(ya.x + yb.x), tag, __float_as_uint(ya.y + yb.y), tag}, rws, po, 0, AUX_SC1);
  }

  if (b < NRED && w < 8) {
    if (w == 0) {
      const unsigned xp = __builtin_amdgcn_raw_buffer_load_b32(rx, lane < 8 ? b * 32 + lane * 4 : OOB, 0, 0);
      float f0, f1v;
      for (unsigned spins = 0;; ++spins) {
        asm volatile("" ::: "memory");
        v4u pf[9];
#pragma unroll
        for (int k = 0; k < 9; ++k) pf[k] = __builtin_amdgcn_raw_buffer_load_b128(rws, WS_PF + b * NFW * 128 + lane * 16, k * 1024, AUX_SC1);
        unsigned bad = 0; f0 = 0.f; f1v = 0.f;
#pragma unroll
        for (int k = 0; k < 9; ++k) { bad |= (pf[k].y ^ tag) | (pf[k].w ^ tag); f0 += __uint_as_float(pf[k].x); f1v += __uint_as_float(pf[k].z); }
        if (__all(bad == 0) || spins > SPIN_CAP) break;
        __builtin_amdgcn_s_sleep(8);
      }
      f0 += dpp<XOR8>(f0); f0 = rnd(sum_x32(sum_x16(f0)));
      f1v += dpp<XOR8>(f1v); f1v = rnd(sum_x32(sum_x16(f1v)));
      if (lane < 8) {
        *reinterpret_cast<f32x2*>(&lds[L_G + lane * 2]) = f32x2{rnd(f0 / (1.0f + __expf(-f0))), rnd(f1v / (1.0f + __expf(-f1v)))};
        ldu[L_G + 16 + lane] = xp;
      }
    }
    if (w < NSW) wait_flag(F_ORDY, 1);
    for (int i = 0; i < 4; ++i) __builtin_amdgcn_s_sleep(4);
    int o4[4];
    float sv[8];
#pragma unroll
    for (int k = 0; k < 4; ++k) o4[k] = WS_P2 + b * NWG * 128 + (w * 64 + lane) * 16 + k * 8192;
    v4u pl[3][4];
    gather<4>(rws, o4, tag, sv, pl);
    float s0 = (sv[0] + sv[2]) + (sv[4] + sv[6]), s1 = (sv[1] + sv[3]) + (sv[5] + sv[7]);
    s0 += dpp<XOR8>(s0); s0 = sum_x32(sum_x16(s0));
    s1 += dpp<XOR8>(s1); s1 = sum_x32(sum_x16(s1));
    if (lane < 8) *reinterpret_cast<f32x2*>(&lds[L_FIN + w * 16 + lane * 2]) = f32x2{s0, s1};
    int old = lds_inc(lds0 + (L_FLAG + F_FINCNT) * 4);
    f32x2 t2 = *reinterpret_cast<const f32x2*>(&lds[L_FIN + lane * 2]);
    f32x2 gv = *reinterpret_cast<const f32x2*>(&lds[L_G + (lane & 7) * 2]);
    unsigned xp = ldu[L_G + 16 + (lane & 7)];
    asm volatile("s_waitcnt lgkmcnt(0)" : "+v"(old), "+v"(t2), "+v"(gv), "+v"(xp));
    if (__builtin_amdgcn_readfirstlane(old) == 7) {
      t2.x += dpp<XOR8>(t2.x); t2.x = sum_x32(sum_x16(t2.x));
      t2.y += dpp<XOR8>(t2.y); t2.y = sum_x32(sum_x16(t2.y));
      const unsigned r = pk_bf16(lo_f(xp) + rnd(rnd(t2.x) * gv.x), hi_f(xp) + rnd(rnd(t2.y) * gv.y));
      __builtin_amdgcn_raw_buffer_store_b32(r, mk_rsrc(out, DM * 2), lane < 8 ? b * 32 + lane * 4 : OOB, 0, 0);
      if (b == 0 && lane == 0) __builtin_amdgcn_raw_buffer_store_b32(tag, rws, WS_EPOCH, 0, AUX_SC1);
    }
    gather_end<4>(pl);
  }
}

}

extern "C" void kernel_launch(void* const* d_in, const int* in_sizes, int n_in, void* d_out, int out_size, void* d_ws, size_t ws_size,
                              hipStream_t stream) {
  (void)in_sizes; (void)n_in; (void)out_size; (void)ws_size;
  fused_decode<<<NWG, NTHR, 0, stream>>>((const unsigned short*)d_in[0], (unsigned char*)d_ws, (const unsigned short*)d_in[3],
                                         (const unsigned short*)d_in[1], (const unsigned short*)d_in[6], (const unsigned short*)d_in[7],
                                         (const unsigned short*)d_in[8], (const unsigned short*)d_in[2], (const unsigned short*)d_in[4],
                                         (unsigned short*)d_out);
}
```

```cpp
#include <hip/hip_runtime.h>
#include <hip/hip_bf16.h>
#include <stdint.h>

namespace {

typedef unsigned v4u __attribute__((ext_vector_type(4)));
typedef unsigned v2u __attribute__((ext_vector_type(2)));
typedef float f32x4 __attribute__((ext_vector_type(4)));
typedef float f32x2 __attribute__((ext_vector_type(2)));
typedef __bf16 bf16x8 __attribute__((ext_vector_type(8)));
typedef __bf16 bf16x2 __attribute__((ext_vector_type(2)));
#define LAS __attribute__((address_space(3)))

constexpr int DM = 576, SK = 65536, FF = 4608, QD = 4096;
constexpr int NWG = 256;
constexpr int NSW = 4;
constexpr int NW = 9;
constexpr int NTHR = (NW + 1) * 64;
constexpr int NRED = 36;
constexpr int NFW = 72;
constexpr int TL = 4;
constexpr int NT = 64;
constexpr int OOB = 0x70000000;

constexpr int TAU_NOM = 874;
constexpr int TAU_DN = 35, TAU_UP = 22, TAU_FAST = 20;
constexpr int BURST = 20, STALL = 100;
constexpr int CP_D = 6;
constexpr int ML_AT = 74;
constexpr int X_MIN = 4, X_MAX = 20, X_C = 68;
#define KTCH 1
#define KTV 2
#define KKLATE 0
#define KQD 7
constexpr int QDELAY = KQD;
constexpr int NEAR_AT = 124;

constexpr int WS_EPOCH = 0;
constexpr int WS_QG = 4096;
constexpr int WS_ML = WS_QG + 16 * 4 * 256 * 8;
constexpr int WS_CP = WS_ML + NWG * 128;
constexpr int WS_P1 = WS_CP + NWG * 128;
constexpr int WS_P2 = WS_P1 + 16 * 16 * 16 * 128;
constexpr int WS_PF = WS_P2 + NRED * NWG * 128;
constexpr int WS_GU = WS_PF + NRED * NFW * 128;
constexpr int WS_PAD = WS_GU + NFW * 2 * 64 * 16;
constexpr int WS_END = WS_PAD + 128;

constexpr int AUX_NT = 2, AUX_SC1 = 16;
constexpr int RSRC_W3 = 0x00020000;
constexpr unsigned SPIN_CAP = 1u << 16;

constexpr int L_FLAG = 0;
enum { F_TAG, F_QRDY, F_XRDY, F_FDONE, F_POLL, F_P1CNT, F_SRDY, F_ORDY, F_PROG, F_STALL, F_TEND, F_SLOPE, F_ENDCNT, F_FINCNT, F_S1CNT };
constexpr int L_MW = 16;
constexpr int L_O = L_MW + 16;
constexpr int L_G = L_O + 16;
constexpr int L_Q = L_G + 32;
constexpr int L_LN = L_Q + 128;
constexpr int L_YF = L_LN + 4;
constexpr int L_FIN = L_YF + 576;
constexpr int L_ACC = L_FIN + 128;
constexpr int L_SC = L_ACC + NSW * 256;
constexpr int L_FLOATS = 24 * 1024;
static_assert(L_SC % 4 == 0 && L_ACC % 4 == 0 && L_Q % 4 == 0 && L_O % 4 == 0 && L_SC + NSW * NT * 32 <= L_FLOATS, "LDS map");
static_assert(16 * NSW * NT * 16 == SK && X_MIN >= 4, "key split");

template <int CTRL> __device__ __forceinline__ float dpp(float x) {
  return __builtin_bit_cast(float, __builtin_amdgcn_mov_dpp(__builtin_bit_cast(int, x), CTRL, 0xf, 0xf, true));
}
template <int CTRL> __device__ __forceinline__ unsigned dppu(unsigned x) {
  return (unsigned)__builtin_amdgcn_mov_dpp((int)x, CTRL, 0xf, 0xf, true);
}
constexpr int XOR1 = 0xB1, XOR2 = 0x4E, XOR7 = 0x141, XOR8 = 0x128, ROR4 = 0x124;
__device__ __forceinline__ float sum_x16(float x) {
  auto s = __builtin_amdgcn_permlane16_swap(__float_as_uint(x), __float_as_uint(x), false, false);
  return __uint_as_float(s[0]) + __uint_as_float(s[1]);
}
__device__ __forceinline__ float sum_x32(float x) {
  auto s = __builtin_amdgcn_permlane32_swap(__float_as_uint(x), __float_as_uint(x), false, false);
  return __uint_as_float(s[0]) + __uint_as_float(s[1]);
}
__device__ __forceinline__ float max_x16(float x) {
  auto s = __builtin_amdgcn_permlane16_swap(__float_as_uint(x), __float_as_uint(x), false, false);
  return fmaxf(__uint_as_float(s[0]), __uint_as_float(s[1]));
}
__device__ __forceinline__ float max_x32(float x) {
  auto s = __builtin_amdgcn_permlane32_swap(__float_as_uint(x), __float_as_uint(x), false, false);
  return fmaxf(__uint_as_float(s[0]), __uint_as_float(s[1]));
}
__device__ __forceinline__ float wave_sum(float x) {
  x += dpp<XOR1>(x); x += dpp<XOR2>(x); x += dpp<ROR4>(x); x += dpp<XOR8>(x);
  return sum_x32(sum_x16(x));
}
__device__ __forceinline__ unsigned pk_bf16(float lo, float hi) {
  bf16x2 v = {(__bf16)lo, (__bf16)hi};
  return __builtin_bit_cast(unsigned, v);
}
__device__ __forceinline__ float rnd(float x) { return (float)(__bf16)x; }
__device__ __forceinline__ float lo_f(unsigned u) { return __uint_as_float(u << 16); }
__device__ __forceinline__ float hi_f(unsigned u) { return __uint_as_float(u & 0xffff0000u); }
__device__ __forceinline__ float dot2(unsigned a, unsigned b, float c) {
  return __builtin_amdgcn_fdot2_f32_bf16(__builtin_bit_cast(bf16x2, a), __builtin_bit_cast(bf16x2, b), c, false);
}
__device__ __forceinline__ __amdgpu_buffer_rsrc_t mk_rsrc(const void* p, int bytes) {
  return __builtin_amdgcn_make_buffer_rsrc((void*)p, 0, bytes, RSRC_W3);
}
__device__ __forceinline__ v4u and4(unsigned x, const unsigned (&m)[4]) { return v4u{x & m[0], x & m[1], x & m[2], x & m[3]}; }
__device__ __forceinline__ f32x4 mfma16(v4u a, v4u bm, f32x4 cacc) {
  return __builtin_amdgcn_mfma_f32_16x16x32_bf16(__builtin_bit_cast(bf16x8, a), __builtin_bit_cast(bf16x8, bm), cacc, 0, 0, 0);
}
__device__ __forceinline__ f32x4 gemv_fin(f32x4 d, int lane) {
  const bool valid = (lane >> 5) == ((lane >> 3) & 1);
#pragma unroll
  for (int r = 0; r < 4; ++r) { float t = valid ? d[r] : 0.f; t += dpp<XOR8>(t); d[r] = sum_x32(t); }
  return d;
}
__device__ __forceinline__ int lds_inc(unsigned addr) {
  int r; unsigned long long save;
  asm volatile("s_mov_b64 %1, exec\n\ts_mov_b64 exec, 1\n\tds_add_rtn_u32 %0, %2, %3\n\ts_mov_b64 exec, %1"
               : "=&v"(r), "=&s"(save) : "v"(addr), "v"(1) : "memory");
  return r;
}
template <int N>
__device__ __forceinline__ void gather(__amdgpu_buffer_rsrc_t rs, int (&off)[N], unsigned tag, float (&val)[2 * N], v4u (&p)[3][N]) {
  auto issue = [&](int j) {
    asm volatile("" ::: "memory");
#pragma unroll
    for (int k = 0; k < N; ++k) p[j][k] = __builtin_amdgcn_raw_buffer_load_b128(rs, off[k], 0, AUX_SC1);
    __builtin_amdgcn_sched_barrier(0);
  };
  auto check = [&](int j) {
    bool done = true;
#pragma unroll
    for (int k = 0; k < N; ++k) {
      const bool ok = off[k] != OOB && ((p[j][k].y ^ tag) | (p[j][k].w ^ tag)) == 0;
      val[2 * k] = ok ? __uint_as_float(p[j][k].x) : val[2 * k]; val[2 * k + 1] = ok ? __uint_as_float(p[j][k].z) : val[2 * k + 1];
      off[k] = ok ? OOB : off[k];
      done = done && off[k] == OOB;
    }
    return __all(done) != 0;
  };
#pragma unroll
  for (int k = 0; k < 2 * N; ++k) val[k] = 0.f;
  issue(0); __builtin_amdgcn_s_sleep(5); issue(1); __builtin_amdgcn_s_sleep(5);
#pragma unroll 1
  for (unsigned spins = 0; spins < SPIN_CAP; ++spins) {
    issue(2); if (check(0)) break;
    issue(0); if (check(1)) break;
    issue(1); if (check(2)) break;
  }
}
template <int N>
__device__ __forceinline__ void gather_end(v4u (&p)[3][N]) {
#pragma unroll
  for (int j = 0; j < 3; ++j)
#pragma unroll
    for (int k = 0; k < N; ++k) asm volatile("" :: "v"(p[j][k]));
}
__device__ __forceinline__ void pace(unsigned& next, unsigned tau, unsigned& stall) {
  int d;
  asm volatile("1:\n\ts_memrealtime vcc\n\ts_waitcnt lgkmcnt(0)\n\ts_lshl_b32 %0, vcc_lo, 4\n\ts_sub_i32 %0, %0, %1\n\ts_cmp_ge_i32 %0, 0\n\ts_cbranch_scc1 2f\n\ts_sleep 1\n\ts_branch 1b\n2:"
               : "=&s"(d) : "s"(next) : "scc", "vcc");
  next += tau + (unsigned)max(0, d - BURST * 16);
  stall += d > STALL * 16 ? d : 0;
}

__global__ void __launch_bounds__(NTHR, 3)
fused_decode(const unsigned short* __restrict__ xg, unsigned char* __restrict__ ws, const unsigned short* __restrict__ wq,
             const unsigned short* __restrict__ kg, const unsigned short* __restrict__ wgate, const unsigned short* __restrict__ wup,
             const unsigned short* __restrict__ wdown, const unsigned short* __restrict__ vg, const unsigned short* __restrict__ wo,
             unsigned short* __restrict__ out) {
  __shared__ __attribute__((aligned(16))) float lds[L_FLOATS];
  asm volatile("" :: "s"(xg), "s"(ws), "s"(wq), "s"(kg), "s"(wgate), "s"(wup), "s"(wdown));
  const int lane = threadIdx.x & 63, pw = __builtin_amdgcn_readfirstlane(threadIdx.x >> 6);
  const int w = pw == 0 ? NW : pw - 1;
  const int b = blockIdx.x, g = b >> 4, s = b & 15;
  const int c = lane & 15;
  const int sr = (s + 3 * g) & 15, ns2 = 5 - (g & 1);
  const bool is1 = sr < 9, is2 = !is1 && sr < 9 + ns2;
  const int f1 = g * 9 + sr, f2 = (g >> 1) * 9 + (g & 1) * 5 + sr - 9;
  unsigned* const ldu = reinterpret_cast<unsigned*>(lds);
  const unsigned lds0 = (unsigned)(uintptr_t)(LAS float*)lds;

  const __amdgpu_buffer_rsrc_t rws = mk_rsrc(ws, WS_END);
  const __amdgpu_buffer_rsrc_t rx = mk_rsrc(xg, DM * 2);

  auto wait_flag = [&](int idx, unsigned want) {
#pragma unroll 1
    for (unsigned spins = 0; spins < (SPIN_CAP << 6); ++spins) {
      if (__builtin_amdgcn_readfirstlane(*(volatile LAS unsigned*)&ldu[L_FLAG + idx]) >= want) break;
      __builtin_amdgcn_s_sleep(1);
    }
    asm volatile("" ::: "memory");
  };
  auto set_flag = [&](int idx) {
    asm volatile("" ::: "memory");
    if (lane == 0) *(volatile LAS unsigned*)&ldu[L_FLAG + idx] = 1u;
  };
  auto count_up = [&](int idx) {
    int r = lds_inc(lds0 + (L_FLAG + idx) * 4);
    asm volatile("s_waitcnt lgkmcnt(0)" : "+v"(r));
  };
  unsigned gm[4];
#pragma unroll
  for (int r = 0; r < 4; ++r) gm[r] = ((lane & 7) >> 1) == r ? ((lane & 1) ? 0xffff0000u : 0x0000ffffu) : 0u;
  unsigned tag;

  if (__builtin_expect(w == NW, 1)) {
    __builtin_amdgcn_s_setprio(3);
    unsigned xd[5], xq[3];
#pragma unroll
    for (int k = 0; k < 5; ++k) xd[k] = __builtin_amdgcn_raw_buffer_load_b32(rx, lane * 4 + k * 256, 0, 0);
#pragma unroll
    for (int k = 0; k < 3; ++k) xq[k] = __builtin_amdgcn_raw_buffer_load_b16(rx, (144 * (s >> 2) + lane + 64 * k) * 2, 0, 0);
    const unsigned epoch = __builtin_amdgcn_raw_buffer_load_b32(rws, lane == 0 ? WS_EPOCH : OOB, 0, AUX_SC1);
    v4u wqv[18];
    {
      const __amdgpu_buffer_rsrc_t rq = mk_rsrc(wq, DM * QD * 2);
      const int o = (144 * (s >> 2) + (lane >> 3)) * (QD * 2) + (g * 4 + (s & 3)) * 128 + (lane & 7) * 16;
#pragma unroll
      for (int i = 0; i < 18; ++i) wqv[i] = __builtin_amdgcn_raw_buffer_load_b128(rq, o, i * 8 * QD * 2, AUX_NT);
    }
    if (lane < 16) ldu[L_FLAG + lane] = 0u;
    asm volatile("s_waitcnt lgkmcnt(0)" ::: "memory");
    __builtin_amdgcn_sched_barrier(0);
    __builtin_amdgcn_s_barrier();
    __builtin_amdgcn_sched_barrier(0);
    float mb, den;
    {
      float xv[10];
#pragma unroll
      for (int k = 0; k < 5; ++k) { xv[2 * k] = lo_f(xd[k]); xv[2 * k + 1] = hi_f(xd[k]); }
      float sm = 0.f;
#pragma unroll
      for (int k = 0; k < 10; ++k) sm += xv[k];
      const float mean = wave_sum(sm) * (1.0f / DM);
      float sq = 0.f;
#pragma unroll
      for (int k = 0; k < 8; ++k) { const float d = xv[k] - mean; sq = fmaf(d, d, sq); }
      const float d8 = lane < 32 ? xv[8] - mean : 0.f, d9 = lane < 32 ? xv[9] - mean : 0.f;
      sq = fmaf(d8, d8, sq); sq = fmaf(d9, d9, sq);
      const float var = wave_sum(sq) * (1.0f / DM);
      mb = rnd(mean);
      den = rnd(sqrtf(rnd(rnd(var) + 1e-5f)));
    }
    if (lane == 0) *reinterpret_cast<f32x2*>(&lds[L_LN]) = f32x2{mb, den};
    set_flag(F_XRDY);
    unsigned xn[3];
#pragma unroll
    for (int k = 0; k < 3; ++k) { const float v = rnd(lo_f(xq[k]) - mb) / den; xn[k] = pk_bf16(v, v); }
    unsigned xr[18];
#pragma unroll
    for (int i = 0; i < 18; ++i) xr[i] = __shfl(xn[i >> 3], (8 * i + (lane >> 3)) & 63);
#pragma unroll
    for (int i = 0; i < 18; ++i) asm volatile("" : "+v"(xr[i]));
    __builtin_amdgcn_sched_barrier(0);
    f32x4 qa = {0.f, 0.f, 0.f, 0.f};
#pragma unroll
    for (int i = 0; i < 18; ++i) { qa = mfma16(and4(xr[i], gm), wqv[i], qa); __builtin_amdgcn_sched_barrier(0); }
    qa = gemv_fin(qa, lane);
    tag = __builtin_amdgcn_readfirstlane(epoch) + 1u;
    {
      const bool up2 = (lane & 8) != 0;
      const int col = (lane & 7) * 8 + 4 * ((lane >> 4) & 1) + (up2 ? 2 : 0);
      __builtin_amdgcn_raw_buffer_store_b128(v4u{__float_as_uint(up2 ? qa[2] : qa[0]), tag, __float_as_uint(up2 ? qa[3] : qa[1]), tag}, rws,
                                             lane < 32 ? WS_QG + ((g * 4 + (s >> 2)) * 256 + (s & 3) * 64 + col) * 8 : OOB, 0, AUX_SC1);
    }
    if (lane == 0) ldu[L_FLAG + F_TAG] = tag;
    for (int i = 0; i < QDELAY; ++i) __builtin_amdgcn_s_sleep(8);
    float q4[4];
    for (unsigned spins = 0;; ++spins) {
      asm volatile("" ::: "memory");
      v4u a[8];
#pragma unroll
      for (int r = 0; r < 4; ++r) {
        a[2 * r] = __builtin_amdgcn_raw_buffer_load_b128(rws, WS_QG + g * 8192 + lane * 32, r * 2048, AUX_SC1);
        a[2 * r + 1] = __builtin_amdgcn_raw_buffer_load_b128(rws, WS_QG + g * 8192 + lane * 32 + 16, r * 2048, AUX_SC1);
      }
      __builtin_amdgcn_sched_barrier(0);
      set_flag(F_POLL);
      __builtin_amdgcn_sched_barrier(0);
      unsigned bad = 0;
#pragma unroll
      for (int j = 0; j < 4; ++j) q4[j] = 0.f;
#pragma unroll
      for (int r = 0; r < 4; ++r) {
        bad |= (a[2 * r].y ^ tag) | (a[2 * r].w ^ tag) | (a[2 * r + 1].y ^ tag) | (a[2 * r + 1].w ^ tag);
        q4[0] += __uint_as_float(a[2 * r].x); q4[1] += __uint_as_float(a[2 * r].z);
        q4[2] += __uint_as_float(a[2 * r + 1].x); q4[3] += __uint_as_float(a[2 * r + 1].z);
      }
      if (__all(bad == 0) || spins > SPIN_CAP) break;
      __builtin_amdgcn_s_sleep(1);
    }
    *reinterpret_cast<v2u*>(&ldu[L_Q + 2 * lane]) = v2u{pk_bf16(q4[0], q4[1]), pk_bf16(q4[2], q4[3])};
    set_flag(F_QRDY);
    __builtin_amdgcn_s_setprio(0);
    float slope = (float)TAU_NOM;
    unsigned hist = 0, prev16 = 0, pstall = 0;
    auto checkpoint = [&](int k, int idx, int span) {
      wait_flag(F_PROG, idx);
      const unsigned t1 = (unsigned)__builtin_amdgcn_s_memrealtime();
      const unsigned stall = __builtin_amdgcn_readfirstlane(*(volatile LAS unsigned*)&ldu[L_FLAG + F_STALL]);
      hist = lane == k ? t1 : hist;
      const unsigned tg = lane <= k ? tag : ~tag;
      __builtin_amdgcn_raw_buffer_store_b128(v4u{hist, tg, hist, tg}, rws, lane < 8 ? WS_CP + b * 128 + lane * 16 : OOB, 0, AUX_SC1);
      wait_flag(F_PROG, idx + CP_D);
      const v2u cp = __builtin_amdgcn_raw_buffer_load_b64(rws, WS_CP + (g * 16 + c) * 128 + k * 16, 0, AUX_SC1);
      float dt = cp.y == tag ? (float)(int)(cp.x - t1) : (float)(CP_D * TAU_NOM / 16);
      dt += dpp<XOR1>(dt); dt += dpp<XOR2>(dt); dt += dpp<ROR4>(dt); dt += dpp<XOR8>(dt);
      const unsigned t16 = (t1 << 4) + (unsigned)(int)dt;
      if (k > 0) {
        const float meas = (float)(int)(t16 - prev16 - (stall - pstall)) / (float)span;
        slope = fminf(meas > slope * 1.006f ? meas * 1.01f : fmaxf((float)TAU_NOM, slope * 0.995f), 2.0f * TAU_NOM);
      }
      prev16 = t16; pstall = stall;
      if (lane == 0) *reinterpret_cast<v2u*>(&ldu[L_FLAG + F_TEND]) = v2u{(t16 + (unsigned)(int)(slope * (float)(2 * NT - idx)) - stall) | 1u, (unsigned)(int)slope};
    };
    checkpoint(0, 12, 0); checkpoint(1, 32, 20); checkpoint(2, 56, 24);
    wait_flag(F_P1CNT, NSW);
    {
      const float mw = lds[L_MW + (lane & 7)];
      float mx = fmaxf(mw, dpp<XOR2>(mw)); mx = fmaxf(mx, dpp<ROR4>(mx));
      float l = 0.f;
#pragma unroll 2
      for (int i = 0; i < NSW * NT / 8; ++i) {
        const f32x4 v = *reinterpret_cast<const f32x4*>(&lds[L_SC + i * 256 + lane * 4]);
        l += (__builtin_amdgcn_exp2f(v[0] - mx) + __builtin_amdgcn_exp2f(v[1] - mx)) + (__builtin_amdgcn_exp2f(v[2] - mx) + __builtin_amdgcn_exp2f(v[3] - mx));
      }
      l += dpp<XOR2>(l); l += dpp<ROR4>(l); l += dpp<XOR8>(l); l = sum_x32(sum_x16(l));
      __builtin_amdgcn_raw_buffer_store_b128(v4u{__float_as_uint(mx), tag, __float_as_uint(l), tag}, rws,
                                             lane < 8 ? WS_ML + b * 128 + lane * 16 : OOB, 0, AUX_SC1);
      wait_flag(F_PROG, ML_AT);
      v4u ml;
      for (unsigned spins = 0;; ++spins) {
        asm volatile("" ::: "memory");
        ml = __builtin_amdgcn_raw_buffer_load_b128(rws, WS_ML + (g * 16 + ((lane >> 1) & 15)) * 128 + (lane & 1) * 16, 0, AUX_SC1);
        if (__all(((ml.y ^ tag) | (ml.w ^ tag)) == 0) || spins > SPIN_CAP) break;
        __builtin_amdgcn_s_sleep(32);
      }
      const float mp = __uint_as_float(ml.x);
      float M = fmaxf(mp, dpp<XOR2>(mp)); M = fmaxf(M, dpp<ROR4>(M)); M = fmaxf(M, dpp<XOR8>(M)); M = max_x16(M);
      float L = __builtin_amdgcn_exp2f(mp - M) * __uint_as_float(ml.z);
      asm volatile("" : "+v"(L));
      L += dpp<XOR2>(L); L += dpp<ROR4>(L); L += dpp<XOR8>(L); L = sum_x16(L);
      if (lane < 8) lds[L_MW + lane] = __builtin_amdgcn_exp2f(mw - M) / L;
      set_flag(F_SRDY);
    }
    checkpoint(3, 84, 28);
    if (is2) {
      wait_flag(F_PROG, NEAR_AT);
      wait_flag(F_FDONE, 3);
#pragma unroll
      for (int k = 0; k < 5; ++k) {
        const int t = k * 64 + lane;
        const f32x2 yf = *reinterpret_cast<const f32x2*>(&lds[L_YF + 2 * (t < 288 ? t : 0)]);
        __builtin_amdgcn_raw_buffer_store_b128(v4u{__float_as_uint(yf.x), tag, __float_as_uint(yf.y), tag}, rws,
                                               t < 288 ? WS_PF + ((t >> 3) * NFW + f2) * 128 + (t & 7) * 16 : OOB, 0, AUX_SC1);
      }
    }
    __builtin_amdgcn_s_setprio(3);
    wait_flag(F_ENDCNT, NSW);
    for (int i = 0; i < 3; ++i) __builtin_amdgcn_s_sleep(4);
    {
      int o2[2] = {WS_P1 + (g * 16 + s) * 2048 + lane * 16, WS_P1 + (g * 16 + s) * 2048 + 1024 + lane * 16};
      float pv4[4];
      v4u pl[3][2];
      gather<2>(rws, o2, tag, pv4, pl);
      auto h0 = __builtin_amdgcn_permlane32_swap(__float_as_uint(pv4[0] + pv4[2]), __float_as_uint(pv4[1] + pv4[3]), false, false);
      float v = sum_x16(__uint_as_float(h0[0]) + __uint_as_float(h0[1]));
      v += dpp<XOR8>(v);
      if ((lane & 24) == 0) lds[L_O + 2 * (lane & 7) + (lane >> 5)] = rnd(v);
      set_flag(F_ORDY);
      gather_end<2>(pl);
    }
    return;
  }

  const int nrole = is1 ? 4 : is2 ? 3 : 0;
  const int voff = lane * 16;
  __builtin_amdgcn_sched_barrier(0);
  __builtin_amdgcn_s_barrier();
  __builtin_amdgcn_sched_barrier(0);

  if (w >= NSW && w < NSW + nrole) {
    const int fw = w - NSW;
    v4u fr[24];
    if (is1) {
      const __amdgpu_buffer_rsrc_t rfg = mk_rsrc(wgate, DM * FF * 2);
      const __amdgpu_buffer_rsrc_t rfu = mk_rsrc(wup, DM * FF * 2);
      const int row0 = 288 * (f1 & 1) + 72 * fw + (lane >> 3);
      const int foff = row0 * (FF * 2) + (f1 >> 1) * 128 + (lane & 7) * 16;
      const unsigned xa = __builtin_amdgcn_raw_buffer_load_b16(rx, (288 * (f1 & 1) + 72 * fw + lane) * 2, 0, 0);
      const unsigned xb = __builtin_amdgcn_raw_buffer_load_b16(rx, (row0 + 64) * 2, 0, 0);
#pragma unroll
      for (int i = 0; i < 9; ++i) {
        fr[2 * i] = __builtin_amdgcn_raw_buffer_load_b128(rfg, foff, i * 8 * FF * 2, AUX_NT);
        fr[2 * i + 1] = __builtin_amdgcn_raw_buffer_load_b128(rfu, foff, i * 8 * FF * 2, AUX_NT);
        __builtin_amdgcn_sched_barrier(0); asm volatile("s_waitcnt vmcnt(6)"); __builtin_amdgcn_sched_barrier(0);
      }
      wait_flag(F_XRDY, 1);
      const f32x2 ln = *reinterpret_cast<const f32x2*>(&lds[L_LN]);
      const float va = rnd(lo_f(xa) - ln.x) / ln.y, vb = rnd(lo_f(xb) - ln.x) / ln.y;
      const unsigned na = pk_bf16(va, va), nb = pk_bf16(vb, vb);
      f32x4 ag = {0.f, 0.f, 0.f, 0.f}, au = {0.f, 0.f, 0.f, 0.f};
#pragma unroll
      for (int i = 0; i < 9; ++i) {
        const v4u av = and4(i < 8 ? __shfl(na, 8 * i + (lane >> 3)) : nb, gm);
        ag = mfma16(av, fr[2 * i], ag); au = mfma16(av, fr[2 * i + 1], au);
      }
      ag = gemv_fin(ag, lane); au = gemv_fin(au, lane);
      if ((lane & 8) == 0) *reinterpret_cast<f32x4*>(&lds[L_YF + fw * 128 + (lane >> 5) * 64 + (lane & 7) * 8 + 4 * ((lane >> 4) & 1)]) = lane < 32 ? ag : au;
      wait_flag(F_POLL, 1);
      tag = ldu[L_FLAG + F_TAG];
      int old = lds_inc(lds0 + (L_FLAG + F_S1CNT) * 4);
      float gs = 0.f, us = 0.f;
#pragma unroll
      for (int ww = 0; ww < 4; ++ww) { gs += lds[L_YF + ww * 128 + lane]; us += lds[L_YF + ww * 128 + 64 + lane]; }
      asm volatile("s_waitcnt lgkmcnt(0)" : "+v"(old), "+v"(gs), "+v"(us));
      if (__builtin_amdgcn_readfirstlane(old) == 3)
        __builtin_amdgcn_raw_buffer_store_b128(v4u{__float_as_uint(gs), tag, __float_as_uint(us), tag}, rws, WS_GU + (f1 * 64 + lane) * 16, 0, AUX_SC1);
    } else {
      const __amdgpu_buffer_rsrc_t rwd = mk_rsrc(wdown + (size_t)f2 * 64 * DM, 64 * DM * 2);
      const int doff = (lane >> 3) * (DM * 2) + fw * 384 + (lane & 7) * 16;
#pragma unroll
      for (int rg = 0; rg < 8; ++rg) {
#pragma unroll
        for (int k = 0; k < 3; ++k) fr[rg * 3 + k] = __builtin_amdgcn_raw_buffer_load_b128(rwd, doff + k * 128, rg * 8 * DM * 2, AUX_NT);
        __builtin_amdgcn_sched_barrier(0); asm volatile("s_waitcnt vmcnt(7)"); __builtin_amdgcn_sched_barrier(0);
      }
      wait_flag(F_QRDY, 1);
      tag = ldu[L_FLAG + F_TAG];
      for (int i = 0; i < 8; ++i) __builtin_amdgcn_s_sleep(8);
      v4u p0, p1;
      for (unsigned spins = 0;; ++spins) {
        asm volatile("" ::: "memory");
        p0 = __builtin_amdgcn_raw_buffer_load_b128(rws, WS_GU + (f2 * 128 + lane) * 16, 0, AUX_SC1);
        p1 = __builtin_amdgcn_raw_buffer_load_b128(rws, WS_GU + (f2 * 128 + lane) * 16, 1024, AUX_SC1);
        if (__all(((p0.y ^ tag) | (p0.w ^ tag) | (p1.y ^ tag) | (p1.w ^ tag)) == 0) || spins > SPIN_CAP) break;
        __builtin_amdgcn_s_sleep(16);
      }
      const float gt = rnd(__uint_as_float(p0.x) + __uint_as_float(p1.x)), up = rnd(__uint_as_float(p0.z) + __uint_as_float(p1.z));
      const float inner = 0.7978845608028654f * (gt + 0.044715f * gt * gt * gt);
      const float hv = rnd(0.5f * gt * (1.0f + tanhf(inner))) * up;
      const unsigned hp = pk_bf16(hv, hv);
      f32x4 ya[3];
#pragma unroll
      for (int k = 0; k < 3; ++k) ya[k] = f32x4{0.f, 0.f, 0.f, 0.f};
#pragma unroll
      for (int rg = 0; rg < 8; ++rg) {
        const v4u av = and4(__shfl(hp, rg * 8 + (lane >> 3)), gm);
#pragma unroll
        for (int k = 0; k < 3; ++k) ya[k] = mfma16(av, fr[rg * 3 + k], ya[k]);
      }
#pragma unroll
      for (int k = 0; k < 3; ++k) {
        ya[k] = gemv_fin(ya[k], lane);
        if (lane < 32 && (lane & 8) == 0) *reinterpret_cast<f32x4*>(&lds[L_YF + fw * 192 + k * 64 + (lane & 7) * 8 + 4 * (lane >> 4)]) = ya[k];
      }
      count_up(F_FDONE);
    }
  } else if (w < NSW) {
    const __amdgpu_buffer_rsrc_t rk = mk_rsrc(kg + (size_t)g * SK * 128, SK * 256);
    const __amdgpu_buffer_rsrc_t rv = mk_rsrc(vg + (size_t)g * SK * 128, SK * 256);
    const int tfirst = ((s & 7) << 21) + ((s >> 3) << 15) + (w << 12);
#define TOFF(t) (tfirst + (((t) >> 1) << 16))
#define TSUB 16384
#define VOFF(t) ((t) < NT ? TOFF(t) + ((t) & 1) * TSUB : OOB)
#define tb(n, i) buf[(n) * TL + (i)]
    v4u buf[4 * TL];
    for (int i = 0; i < KKLATE; ++i) __builtin_amdgcn_s_sleep(8);
#pragma unroll
    for (int i = 0; i < 2 * TL; ++i) buf[i] = __builtin_amdgcn_raw_buffer_load_b128(rk, voff + (i & 3) * 1024, TOFF(0) + (i >> 2) * TSUB, AUX_NT);
    __builtin_amdgcn_sched_barrier(0);
    wait_flag(F_POLL, 1);
#pragma unroll
    for (int i = 0; i < 2 * TL; ++i) buf[2 * TL + i] = __builtin_amdgcn_raw_buffer_load_b128(rk, voff + (i & 3) * 1024, TOFF(2) + (i >> 2) * TSUB, AUX_NT);
    __builtin_amdgcn_sched_barrier(0);
    wait_flag(F_QRDY, 1);
    const int hf = (c >> 2) & 1;
    const v4u qf = *reinterpret_cast<const v4u*>(&ldu[L_Q + hf * 64 + 4 * c]);
    const v4u qs = *reinterpret_cast<const v4u*>(&ldu[L_Q + (hf ^ 1) * 64 + 4 * c]);
    constexpr float LOG2E = 1.4426950408889634f, SCALE = 0.08838834764831845f;
    const bool b0 = (c & 1) != 0, b1 = (c & 2) != 0;
    float* const scw = &lds[L_SC + w * NT * 32 + (lane >> 4) * 8 + (lane & 7)];
    float m = -INFINITY;
    float y[4];
    auto qk1 = [&](const v4u* kt) {
#pragma unroll
      for (int i = 0; i < TL; ++i) {
        float xf = 0.f, xs = 0.f;
#pragma unroll
        for (int j = 0; j < 4; ++j) { xf = dot2(kt[i][j], qf[j], xf); xs = dot2(kt[i][j], qs[j], xs); }
        y[i] = xf + dpp<XOR7>(xs);
      }
      asm volatile("" : "+v"(y[0]), "+v"(y[1]), "+v"(y[2]), "+v"(y[3]));
      __builtin_amdgcn_sched_barrier(0);
    };
    auto qk2 = [&](int t) {
      const float u0 = (b0 ? y[1] : y[0]) + dpp<XOR1>(b0 ? y[0] : y[1]);
      const float u1 = (b0 ? y[3] : y[2]) + dpp<XOR1>(b0 ? y[2] : y[3]);
      float sc = (b1 ? u1 : u0) + dpp<XOR2>(b1 ? u0 : u1);
      sc += dpp<XOR8>(sc);
      const float tl2 = rnd(rnd(sc) * SCALE) * LOG2E;
      m = fmaxf(m, tl2);
      scw[t * 32] = tl2;
    };
#pragma unroll
    for (int n = 0; n < 2; ++n) {
      qk1(&tb(n, 0));
#pragma unroll
      for (int i = 0; i < TL; ++i) tb(n, i) = __builtin_amdgcn_raw_buffer_load_b128(rk, voff + i * 1024, TOFF(4) + n * TSUB, AUX_NT);
      __builtin_amdgcn_sched_barrier(0);
      qk2(n);
    }
    qk1(&tb(2, 0)); qk2(2);
    qk1(&tb(3, 0)); qk2(3);
    unsigned next = (unsigned)__builtin_amdgcn_s_memrealtime() << 4, tau = TAU_NOM;
    unsigned stall = 0, tend = 0;
    int slope = TAU_NOM;
    auto aim = [&](int done) {
      if (w == 0 && lane == 0) *(volatile LAS v2u*)&ldu[L_FLAG + F_PROG] = v2u{(unsigned)done, stall};
      if (done & 2) return;
      const v2u te = *(volatile LAS v2u*)&ldu[L_FLAG + F_TEND];
      const unsigned t0 = __builtin_amdgcn_readfirstlane(te.x);
      if (t0 == 0) return;
      tend = t0; slope = __builtin_amdgcn_readfirstlane(te.y);
      const int v = __builtin_amdgcn_readfirstlane((int)((float)(int)(tend + stall - next) * __builtin_amdgcn_rcpf((float)(2 * NT - done))));
      tau = (unsigned)min(slope + TAU_UP, max(slope - TAU_DN, v));
    };
#pragma unroll 1
    for (int t = 4; t < NT; t += 2) {
      const bool lastk = t + 2 >= NT;
      const __amdgpu_buffer_rsrc_t rn = lastk ? rv : rk;
      const int nxt = TOFF(lastk ? 0 : t + 2);
      if (t <= 12) stall = 0;
      aim(t);
#pragma unroll
      for (int n = 0; n < 2; ++n) {
        qk1(&tb(n, 0));
        pace(next, tau, stall);
#pragma unroll
        for (int i = 0; i < TL; ++i) tb(n, i) = __builtin_amdgcn_raw_buffer_load_b128(rn, voff + i * 1024, nxt + n * TSUB, AUX_NT);
        __builtin_amdgcn_sched_barrier(0);
        qk2(t + n);
      }
    }
    m = fmaxf(m, dpp<XOR1>(m)); m = fmaxf(m, dpp<XOR2>(m));
    m = max_x32(max_x16(m));
    if ((lane & 59) == 0) lds[L_MW + 2 * w + (lane >> 2)] = m;
    count_up(F_P1CNT);
    f32x4 acc = {0.f, 0.f, 0.f, 0.f};
    unsigned amask[4];
    {
      const int jp = (c & 3) + 4 * (c >> 3);
#pragma unroll
      for (int r = 0; r < 4; ++r) amask[r] = (jp >> 1) == r ? ((jp & 1) ? 0xffff0000u : 0x0000ffffu) : 0u;
    }
    auto pv = [&](float sv, const v4u* vt) {
      const float p = __builtin_amdgcn_exp2f(sv - m);
      const unsigned pp = pk_bf16(p, p);
#pragma unroll
      for (int i = 0; i < TL; ++i) {
        const unsigned pb = i == 0 ? dppu<0x00>(pp) : i == 1 ? dppu<0x55>(pp) : i == 2 ? dppu<0xAA>(pp) : dppu<0xFF>(pp);
        acc = mfma16(and4(pb, amask), vt[i], acc);
      }
      asm volatile("" : "+v"(acc));
      __builtin_amdgcn_sched_barrier(0);
    };
    int t = 0;
    auto vrun = [&](int lim) {
#pragma unroll 1
      for (; t < lim; t += 2) {
        aim(NT + t);
        const int nxt = TOFF(t + 2);
        const float s0 = scw[t * 32], s1 = scw[t * 32 + 32];
#pragma unroll
        for (int n = 0; n < 2; ++n) {
          pv(n ? s1 : s0, &tb(n, 0));
          pace(next, tau, stall);
#pragma unroll
          for (int i = 0; i < TL; ++i) tb(n, i) = __builtin_amdgcn_raw_buffer_load_b128(rv, voff + i * 1024, nxt + n * TSUB, AUX_NT);
          __builtin_amdgcn_sched_barrier(0);
        }
      }
    };
    vrun(NT - X_MAX);
    {
      const int late = min(1 << 16, max(0, (int)(next + X_MAX * (slope - TAU_FAST) - tend - stall)));
      vrun(NT - min(X_MAX, X_MIN + 2 * ((late * X_C) >> 15)));
    }
#pragma unroll
    for (int k = 2; k < 4; ++k) {
#pragma unroll
      for (int i = 0; i < TL; ++i) tb(k, i) = __builtin_amdgcn_raw_buffer_load_b128(rv, voff + i * 1024, TOFF(t + 2) + (k & 1) * TSUB, AUX_NT);
      __builtin_amdgcn_sched_barrier(0);
    }
    __builtin_amdgcn_s_setprio(2);
    wait_flag(F_SRDY, 1);
    const float ed = lds[L_MW + 2 * w + ((lane >> 4) & 1)];
    tag = ldu[L_FLAG + F_TAG];
    const int po = WS_P1 + ((g * 16 + (lane >> 3)) * 16 + s) * 128 + (lane & 7) * 16;
#pragma unroll 1
    for (; t + 4 < NT; t += 4) {
      if (w == 0 && lane == 0) *(volatile LAS int*)&ldu[L_FLAG + F_PROG] = NT + t + 2;
#pragma unroll
      for (int k = 0; k < 4; ++k) {
        const int o = VOFF(t + k + 4);
        pv(scw[(t + k) * 32], &tb(k, 0));
#pragma unroll
        for (int i = 0; i < TL; ++i) tb(k, i) = __builtin_amdgcn_raw_buffer_load_b128(rv, voff + i * 1024, o, AUX_NT);
        __builtin_amdgcn_sched_barrier(0);
      }
    }
    {
      if (w == 0 && lane == 0) *(volatile LAS int*)&ldu[L_FLAG + F_PROG] = 126;
      v4u av[4][TL];
#pragma unroll
      for (int k = 0; k < 4; ++k) {
        const float p = __builtin_amdgcn_exp2f((t + k < NT ? scw[(t + k) * 32] : -INFINITY) - m);
        const unsigned pp = pk_bf16(p, p);
        av[k][0] = and4(dppu<0x00>(pp), amask); av[k][1] = and4(dppu<0x55>(pp), amask);
        av[k][2] = and4(dppu<0xAA>(pp), amask); av[k][3] = and4(dppu<0xFF>(pp), amask);
        asm volatile("" : "+v"(av[k][0]), "+v"(av[k][1]), "+v"(av[k][2]), "+v"(av[k][3]));
      }
      __builtin_amdgcn_sched_barrier(0);
#pragma unroll
      for (int k = 0; k < 2; ++k)
#pragma unroll
        for (int i = 0; i < TL; ++i) acc = mfma16(av[k][i], tb(k, i), acc);
      if (t + 2 < NT) {
#pragma unroll
        for (int k = 2; k < 4; ++k)
#pragma unroll
          for (int i = 0; i < TL; ++i) acc = mfma16(av[k][i], tb(k, i), acc);
      }
    }
    *reinterpret_cast<f32x4*>(&lds[L_ACC + w * 256 + ((lane >> 4) & 1) * 128 + 8 * c + 4 * (lane >> 5)]) = acc * ed;
    int old = lds_inc(lds0 + (L_FLAG + F_ENDCNT) * 4);
    f32x2 o0 = {0.f, 0.f}, o1 = {0.f, 0.f};
#pragma unroll
    for (int ww = 0; ww < NSW; ++ww) { o0 += *reinterpret_cast<const f32x2*>(&lds[L_ACC + ww * 256 + 2 * lane]); o1 += *reinterpret_cast<const f32x2*>(&lds[L_ACC + ww * 256 + 128 + 2 * lane]); }
    asm volatile("s_waitcnt lgkmcnt(0)" : "+v"(old), "+v"(o0), "+v"(o1));
    if (__builtin_amdgcn_readfirstlane(old) == NSW - 1) {
      __builtin_amdgcn_raw_buffer_store_b128(v4u{__float_as_uint(o0[0]), tag, __float_as_uint(o0[1]), tag}, rws, po, 0, AUX_SC1);
      __builtin_amdgcn_raw_buffer_store_b128(v4u{__float_as_uint(o1[0]), tag, __float_as_uint(o1[1]), tag}, rws, po, 8 * 2048, AUX_SC1);
    }
  }

  if (w >= NSW) {
    const int t = (w - NSW) * 64 + lane;
    const __amdgpu_buffer_rsrc_t rwo = mk_rsrc(wo + (size_t)b * 16 * DM, 16 * DM * 2);
    if (KTCH && w == NW - 1) {
      if (KTCH == 2) wait_flag(F_POLL, 1);
      if (KTCH == 3) wait_flag(F_PROG, 8 + 2 * (b & 15));
      const unsigned t0 = __builtin_amdgcn_raw_buffer_load_b32(mk_rsrc(vg + (size_t)g * SK * 128, SK * 256), lane < KTV ? ((s & 7) << 21) + lane * 0x1ffffc : OOB, 0, AUX_NT);
      const unsigned t1 = __builtin_amdgcn_raw_buffer_load_b32(rwo, lane == 0 ? 0 : OOB, 0, AUX_NT);
      const unsigned t2 = __builtin_amdgcn_raw_buffer_load_b32(mk_rsrc(out, DM * 2), lane == 0 && b < NRED ? b * 32 : OOB, 0, AUX_SC1);
      const unsigned t3 = __builtin_amdgcn_raw_buffer_load_b32(rws, lane == 0 ? WS_PAD : OOB, 0, AUX_NT);
      asm volatile("" :: "v"(t0), "v"(t1), "v"(t2), "v"(t3));
    }
#pragma unroll 1
    for (unsigned spins = 0; spins < (SPIN_CAP << 5); ++spins) {
      if (__builtin_amdgcn_readfirstlane(*(volatile LAS unsigned*)&ldu[L_FLAG + F_ENDCNT]) >= 1u) break;
      __builtin_amdgcn_s_sleep(2);
    }
    float wf[32];
    {
      unsigned wod[16];
#pragma unroll
      for (int j = 0; j < 16; ++j) wod[j] = __builtin_amdgcn_raw_buffer_load_b32(rwo, t < 288 ? t * 4 : OOB, j * DM * 2, AUX_NT);
#pragma unroll
      for (int j = 0; j < 16; ++j) { wf[2 * j] = lo_f(wod[j]); wf[2 * j + 1] = hi_f(wod[j]); }
#pragma unroll
      for (int j = 0; j < 32; ++j) asm volatile("" : "+v"(wf[j]));
    }
    tag = ldu[L_FLAG + F_TAG];
    const int po = t < 288 ? WS_P2 + ((t >> 3) * NWG + b) * 128 + (t & 7) * 16 : OOB;
    f32x4 o4[4];
#pragma unroll 1
    for (unsigned spins = 0; spins < (SPIN_CAP << 8); ++spins) {
      unsigned f = *(volatile LAS unsigned*)&ldu[L_FLAG + F_ORDY];
      asm volatile("" ::: "memory");
#pragma unroll
      for (int k = 0; k < 4; ++k) o4[k] = *reinterpret_cast<const f32x4*>(&lds[L_O + 4 * k]);
      asm volatile("" : "+v"(o4[0]), "+v"(o4[1]), "+v"(o4[2]), "+v"(o4[3]) :: "memory");
      if (__builtin_amdgcn_readfirstlane(f) != 0u) break;
    }
    f32x2 ya = {0.f, 0.f}, yb = {0.f, 0.f};
#pragma unroll
    for (int j = 0; j < 16; j += 2) {
      ya += o4[j >> 2][j & 3] * f32x2{wf[2 * j], wf[2 * j + 1]};
      yb += o4[j >> 2][(j & 3) + 1] * f32x2{wf[2 * j + 2], wf[2 * j + 3]};
    }
    __builtin_amdgcn_raw_buffer_store_b128(v4u{__float_as_uint(ya.x + yb.x), tag, __float_as_uint(ya.y + yb.y), tag}, rws, po, 0, AUX_SC1);
  }

  if (b < NRED && w < 8) {
    if (w == 0) {
      const unsigned xp = __builtin_amdgcn_raw_buffer_load_b32(rx, lane < 8 ? b * 32 + lane * 4 : OOB, 0, 0);
      float f0, f1v;
      for (unsigned spins = 0;; ++spins) {
        asm volatile("" ::: "memory");
        v4u pf[9];
#pragma unroll
        for (int k = 0; k < 9; ++k) pf[k] = __builtin_amdgcn_raw_buffer_load_b128(rws, WS_PF + b * NFW * 128 + lane * 16, k * 1024, AUX_SC1);
        unsigned bad = 0; f0 = 0.f; f1v = 0.f;
#pragma unroll
        for (int k = 0; k < 9; ++k) { bad |= (pf[k].y ^ tag) | (pf[k].w ^ tag); f0 += __uint_as_float(pf[k].x); f1v += __uint_as_float(pf[k].z); }
        if (__all(bad == 0) || spins > SPIN_CAP) break;
        __builtin_amdgcn_s_sleep(8);
      }
      f0 += dpp<XOR8>(f0); f0 = rnd(sum_x32(sum_x16(f0)));
      f1v += dpp<XOR8>(f1v); f1v = rnd(sum_x32(sum_x16(f1v)));
      if (lane < 8) {
        *reinterpret_cast<f32x2*>(&lds[L_G + lane * 2]) = f32x2{rnd(f0 / (1.0f + __expf(-f0))), rnd(f1v / (1.0f + __expf(-f1v)))};
        ldu[L_G + 16 + lane] = xp;
      }
    }
    if (w < NSW) wait_flag(F_ORDY, 1);
    for (int i = 0; i < 4; ++i) __builtin_amdgcn_s_sleep(4);
    int o4[4];
    float sv[8];
#pragma unroll
    for (int k = 0; k < 4; ++k) o4[k] = WS_P2 + b * NWG * 128 + (w * 64 + lane) * 16 + k * 8192;
    v4u pl[3][4];
    gather<4>(rws, o4, tag, sv, pl);
    float s0 = (sv[0] + sv[2]) + (sv[4] + sv[6]), s1 = (sv[1] + sv[3]) + (sv[5] + sv[7]);
    s0 += dpp<XOR8>(s0); s0 = sum_x32(sum_x16(s0));
    s1 += dpp<XOR8>(s1); s1 = sum_x32(sum_x16(s1));
    if (lane < 8) *reinterpret_cast<f32x2*>(&lds[L_FIN + w * 16 + lane * 2]) = f32x2{s0, s1};
    int old = lds_inc(lds0 + (L_FLAG + F_FINCNT) * 4);
    f32x2 t2 = *reinterpret_cast<const f32x2*>(&lds[L_FIN + lane * 2]);
    f32x2 gv = *reinterpret_cast<const f32x2*>(&lds[L_G + (lane & 7) * 2]);
    unsigned xp = ldu[L_G + 16 + (lane & 7)];
    asm volatile("s_waitcnt lgkmcnt(0)" : "+v"(old), "+v"(t2), "+v"(gv), "+v"(xp));
    if (__builtin_amdgcn_readfirstlane(old) == 7) {
      t2.x += dpp<XOR8>(t2.x); t2.x = sum_x32(sum_x16(t2.x));
      t2.y += dpp<XOR8>(t2.y); t2.y = sum_x32(sum_x16(t2.y));
      const unsigned r = pk_bf16(lo_f(xp) + rnd(rnd(t2.x) * gv.x), hi_f(xp) + rnd(rnd(t2.y) * gv.y));
      __builtin_amdgcn_raw_buffer_store_b32(r, mk_rsrc(out, DM * 2), lane < 8 ? b * 32 + lane * 4 : OOB, 0, 0);
      if (b == 0 && lane == 0) __builtin_amdgcn_raw_buffer_store_b32(tag, rws, WS_EPOCH, 0, AUX_SC1);
    }
    gather_end<4>(pl);
  }
}

}

extern "C" void kernel_launch(void* const* d_in, const int* in_sizes, int n_in, void* d_out, int out_size, void* d_ws, size_t ws_size,
                              hipStream_t stream) {
  (void)in_sizes; (void)n_in; (void)out_size; (void)ws_size;
  fused_decode<<<NWG, NTHR, 0, stream>>>((const unsigned short*)d_in[0], (unsigned char*)d_ws, (const unsigned short*)d_in[3],
                                         (const unsigned short*)d_in[1], (const unsigned short*)d_in[6], (const unsigned short*)d_in[7],
                                         (const unsigned short*)d_in[8], (const unsigned short*)d_in[2], (const unsigned short*)d_in[4],
                                         (unsigned short*)d_out);
}
```

```cpp
#include <hip/hip_runtime.h>
#include <hip/hip_bf16.h>
#include <stdint.h>

namespace {

typedef unsigned v4u __attribute__((ext_vector_type(4)));
typedef unsigned v2u __attribute__((ext_vector_type(2)));
typedef float f32x4 __attribute__((ext_vector_type(4)));
typedef float f32x2 __attribute__((ext_vector_type(2)));
typedef __bf16 bf16x8 __attribute__((ext_vector_type(8)));
typedef __bf16 bf16x2 __attribute__((ext_vector_type(2)));
#define LAS __attribute__((address_space(3)))

constexpr int DM = 576, SK = 65536, FF = 4608, QD = 4096;
constexpr int NWG = 256;
constexpr int NSW = 4;
constexpr int NW = 9;
constexpr int NTHR = (NW + 1) * 64;
constexpr int NRED = 36;
constexpr int NFW = 72;
constexpr int TL = 4;
constexpr int NT = 64;
constexpr int OOB = 0x70000000;

constexpr int TAU_NOM = 874;
constexpr int TAU_DN = 35, TAU_UP = 22, TAU_FAST = 20;
constexpr int BURST = 20, STALL = 100;
constexpr int CP_D = 6;
constexpr int ML_AT = 74;
constexpr int X_MIN = 4, X_MAX = 20, X_C = 68;
#define KTCH 1
#define KTV 2
#define KKLATE 0
#define KGATE 1
#define KF1 6
#define KF2 7
#define KGL 2
#define KQD 7
constexpr int QDELAY = KQD;
constexpr int NEAR_AT = 124;

constexpr int WS_EPOCH = 0;
constexpr int WS_QG = 4096;
constexpr int WS_ML = WS_QG + 16 * 4 * 256 * 8;
constexpr int WS_CP = WS_ML + NWG * 128;
constexpr int WS_P1 = WS_CP + NWG * 128;
constexpr int WS_P2 = WS_P1 + 16 * 16 * 16 * 128;
constexpr int WS_PF = WS_P2 + NRED * NWG * 128;
constexpr int WS_GU = WS_PF + NRED * NFW * 128;
constexpr int WS_PAD = WS_GU + NFW * 2 * 64 * 16;
constexpr int WS_END = WS_PAD + 128;

constexpr int AUX_NT = 2, AUX_SC1 = 16;
constexpr int RSRC_W3 = 0x00020000;
constexpr unsigned SPIN_CAP = 1u << 16;

constexpr int L_FLAG = 0;
enum { F_TAG, F_QRDY, F_XRDY, F_FDONE, F_POLL, F_P1CNT, F_SRDY, F_ORDY, F_PROG, F_STALL, F_TEND, F_SLOPE, F_ENDCNT, F_FINCNT, F_S1CNT };
constexpr int L_MW = 16;
constexpr int L_O = L_MW + 16;
constexpr int L_G = L_O + 16;
constexpr int L_Q = L_G + 32;
constexpr int L_LN = L_Q + 128;
constexpr int L_YF = L_LN + 4;
constexpr int L_FIN = L_YF + 576;
constexpr int L_ACC = L_FIN + 128;
constexpr int L_SC = L_ACC + NSW * 256;
constexpr int L_FLOATS = 24 * 1024;
static_assert(L_SC % 4 == 0 && L_ACC % 4 == 0 && L_Q % 4 == 0 && L_O % 4 == 0 && L_SC + NSW * NT * 32 <= L_FLOATS, "LDS map");
static_assert(16 * NSW * NT * 16 == SK && X_MIN >= 4, "key split");

template <int CTRL> __device__ __forceinline__ float dpp(float x) {
  return __builtin_bit_cast(float, __builtin_amdgcn_mov_dpp(__builtin_bit_cast(int, x), CTRL, 0xf, 0xf, true));
}
template <int CTRL> __device__ __forceinline__ unsigned dppu(unsigned x) {
  return (unsigned)__builtin_amdgcn_mov_dpp((int)x, CTRL, 0xf, 0xf, true);
}
constexpr int XOR1 = 0xB1, XOR2 = 0x4E, XOR7 = 0x141, XOR8 = 0x128, ROR4 = 0x124;
__device__ __forceinline__ float sum_x16(float x) {
  auto s = __builtin_amdgcn_permlane16_swap(__float_as_uint(x), __float_as_uint(x), false, false);
  return __uint_as_float(s[0]) + __uint_as_float(s[1]);
}
__device__ __forceinline__ float sum_x32(float x) {
  auto s = __builtin_amdgcn_permlane32_swap(__float_as_uint(x), __float_as_uint(x), false, false);
  return __uint_as_float(s[0]) + __uint_as_float(s[1]);
}
__device__ __forceinline__ float max_x16(float x) {
  auto s = __builtin_amdgcn_permlane16_swap(__float_as_uint(x), __float_as_uint(x), false, false);
  return fmaxf(__uint_as_float(s[0]), __uint_as_float(s[1]));
}
__device__ __forceinline__ float max_x32(float x) {
  auto s = __builtin_amdgcn_permlane32_swap(__float_as_uint(x), __float_as_uint(x), false, false);
  return fmaxf(__uint_as_float(s[0]), __uint_as_float(s[1]));
}
__device__ __forceinline__ float wave_sum(float x) {
  x += dpp<XOR1>(x); x += dpp<XOR2>(x); x += dpp<ROR4>(x); x += dpp<XOR8>(x);
  return sum_x32(sum_x16(x));
}
__device__ __forceinline__ unsigned pk_bf16(float lo, float hi) {
  bf16x2 v = {(__bf16)lo, (__bf16)hi};
  return __builtin_bit_cast(unsigned, v);
}
__device__ __forceinline__ float rnd(float x) { return (float)(__bf16)x; }
__device__ __forceinline__ float lo_f(unsigned u) { return __uint_as_float(u << 16); }
__device__ __forceinline__ float hi_f(unsigned u) { return __uint_as_float(u & 0xffff0000u); }
__device__ __forceinline__ float dot2(unsigned a, unsigned b, float c) {
  return __builtin_amdgcn_fdot2_f32_bf16(__builtin_bit_cast(bf16x2, a), __builtin_bit_cast(bf16x2, b), c, false);
}
__device__ __forceinline__ __amdgpu_buffer_rsrc_t mk_rsrc(const void* p, int bytes) {
  return __builtin_amdgcn_make_buffer_rsrc((void*)p, 0, bytes, RSRC_W3);
}
__device__ __forceinline__ v4u and4(unsigned x, const unsigned (&m)[4]) { return v4u{x & m[0], x & m[1], x & m[2], x & m[3]}; }
__device__ __forceinline__ f32x4 mfma16(v4u a, v4u bm, f32x4 cacc) {
  return __builtin_amdgcn_mfma_f32_16x16x32_bf16(__builtin_bit_cast(bf16x8, a), __builtin_bit_cast(bf16x8, bm), cacc, 0, 0, 0);
}
__device__ __forceinline__ f32x4 gemv_fin(f32x4 d, int lane) {
  const bool valid = (lane >> 5) == ((lane >> 3) & 1);
#pragma unroll
  for (int r = 0; r < 4; ++r) { float t = valid ? d[r] : 0.f; t += dpp<XOR8>(t); d[r] = sum_x32(t); }
  return d;
}
__device__ __forceinline__ int lds_inc(unsigned addr) {
  int r; unsigned long long save;
  asm volatile("s_mov_b64 %1, exec\n\ts_mov_b64 exec, 1\n\tds_add_rtn_u32 %0, %2, %3\n\ts_mov_b64 exec, %1"
               : "=&v"(r), "=&s"(save) : "v"(addr), "v"(1) : "memory");
  return r;
}
template <int N>
__device__ __forceinline__ void gather(__amdgpu_buffer_rsrc_t rs, int (&off)[N], unsigned tag, float (&val)[2 * N], v4u (&p)[3][N]) {
  auto issue = [&](int j) {
    asm volatile("" ::: "memory");
#pragma unroll
    for (int k = 0; k < N; ++k) p[j][k] = __builtin_amdgcn_raw_buffer_load_b128(rs, off[k], 0, AUX_SC1);
    __builtin_amdgcn_sched_barrier(0);
  };
  auto check = [&](int j) {
    bool done = true;
#pragma unroll
    for (int k = 0; k < N; ++k) {
      const bool ok = off[k] != OOB && ((p[j][k].y ^ tag) | (p[j][k].w ^ tag)) == 0;
      val[2 * k] = ok ? __uint_as_float(p[j][k].x) : val[2 * k]; val[2 * k + 1] = ok ? __uint_as_float(p[j][k].z) : val[2 * k + 1];
      off[k] = ok ? OOB : off[k];
      done = done && off[k] == OOB;
    }
    return __all(done) != 0;
  };
#pragma unroll
  for (int k = 0; k < 2 * N; ++k) val[k] = 0.f;
  issue(0); __builtin_amdgcn_s_sleep(5); issue(1); __builtin_amdgcn_s_sleep(5);
#pragma unroll 1
  for (unsigned spins = 0; spins < SPIN_CAP; ++spins) {
    issue(2); if (check(0)) break;
    issue(0); if (check(1)) break;
    issue(1); if (check(2)) break;
  }
}
template <int N>
__device__ __forceinline__ void gather_end(v4u (&p)[3][N]) {
#pragma unroll
  for (int j = 0; j < 3; ++j)
#pragma unroll
    for (int k = 0; k < N; ++k) asm volatile("" :: "v"(p[j][k]));
}
__device__ __forceinline__ void pace(unsigned& next, unsigned tau, unsigned& stall) {
  int d;
  asm volatile("1:\n\ts_memrealtime vcc\n\ts_waitcnt lgkmcnt(0)\n\ts_lshl_b32 %0, vcc_lo, 4\n\ts_sub_i32 %0, %0, %1\n\ts_cmp_ge_i32 %0, 0\n\ts_cbranch_scc1 2f\n\ts_sleep 1\n\ts_branch 1b\n2:"
               : "=&s"(d) : "s"(next) : "scc", "vcc");
  next += tau + (unsigned)max(0, d - BURST * 16);
  stall += d > STALL * 16 ? d : 0;
}

__global__ void __launch_bounds__(NTHR, 3)
fused_decode(const unsigned short* __restrict__ xg, unsigned char* __restrict__ ws, const unsigned short* __restrict__ wq,
             const unsigned short* __restrict__ kg, const unsigned short* __restrict__ wgate, const unsigned short* __restrict__ wup,
             const unsigned short* __restrict__ wdown, const unsigned short* __restrict__ vg, const unsigned short* __restrict__ wo,
             unsigned short* __restrict__ out) {
  __shared__ __attribute__((aligned(16))) float lds[L_FLOATS];
  asm volatile("" :: "s"(xg), "s"(ws), "s"(wq), "s"(kg), "s"(wgate), "s"(wup), "s"(wdown));
  const int lane = threadIdx.x & 63, pw = __builtin_amdgcn_readfirstlane(threadIdx.x >> 6);
  const int w = pw == 0 ? NW : pw - 1;
  const int b = blockIdx.x, g = b >> 4, s = b & 15;
  const int c = lane & 15;
  const int sr = (s + 3 * g) & 15, ns2 = 5 - (g & 1);
  const bool is1 = sr < 9, is2 = !is1 && sr < 9 + ns2;
  const int f1 = g * 9 + sr, f2 = (g >> 1) * 9 + (g & 1) * 5 + sr - 9;
  unsigned* const ldu = reinterpret_cast<unsigned*>(lds);
  const unsigned lds0 = (unsigned)(uintptr_t)(LAS float*)lds;

  const __amdgpu_buffer_rsrc_t rws = mk_rsrc(ws, WS_END);
  const __amdgpu_buffer_rsrc_t rx = mk_rsrc(xg, DM * 2);

  auto wait_flag = [&](int idx, unsigned want) {
#pragma unroll 1
    for (unsigned spins = 0; spins < (SPIN_CAP << 6); ++spins) {
      if (__builtin_amdgcn_readfirstlane(*(volatile LAS unsigned*)&ldu[L_FLAG + idx]) >= want) break;
      __builtin_amdgcn_s_sleep(1);
    }
    asm volatile("" ::: "memory");
  };
  auto set_flag = [&](int idx) {
    asm volatile("" ::: "memory");
    if (lane == 0) *(volatile LAS unsigned*)&ldu[L_FLAG + idx] = 1u;
  };
  auto count_up = [&](int idx) {
    int r = lds_inc(lds0 + (L_FLAG + idx) * 4);
    asm volatile("s_waitcnt lgkmcnt(0)" : "+v"(r));
  };
  unsigned gm[4];
#pragma unroll
  for (int r = 0; r < 4; ++r) gm[r] = ((lane & 7) >> 1) == r ? ((lane & 1) ? 0xffff0000u : 0x0000ffffu) : 0u;
  unsigned tag;

  if (__builtin_expect(w == NW, 1)) {
    __builtin_amdgcn_s_setprio(3);
    unsigned xd[5], xq[3];
#pragma unroll
    for (int k = 0; k < 5; ++k) xd[k] = __builtin_amdgcn_raw_buffer_load_b32(rx, lane * 4 + k * 256, 0, 0);
#pragma unroll
    for (int k = 0; k < 3; ++k) xq[k] = __builtin_amdgcn_raw_buffer_load_b16(rx, (144 * (s >> 2) + lane + 64 * k) * 2, 0, 0);
    const unsigned epoch = __builtin_amdgcn_raw_buffer_load_b32(rws, lane == 0 ? WS_EPOCH : OOB, 0, AUX_SC1);
    v4u wqv[18];
    {
      const __amdgpu_buffer_rsrc_t rq = mk_rsrc(wq, DM * QD * 2);
      const int o = (144 * (s >> 2) + (lane >> 3)) * (QD * 2) + (g * 4 + (s & 3)) * 128 + (lane & 7) * 16;
#pragma unroll
      for (int i = 0; i < 18; ++i) wqv[i] = __builtin_amdgcn_raw_buffer_load_b128(rq, o, i * 8 * QD * 2, AUX_NT);
    }
    if (lane < 16) ldu[L_FLAG + lane] = 0u;
    asm volatile("s_waitcnt lgkmcnt(0)" ::: "memory");
    __builtin_amdgcn_sched_barrier(0);
    __builtin_amdgcn_s_barrier();
    __builtin_amdgcn_sched_barrier(0);
    float mb, den;
    {
      float xv[10];
#pragma unroll
      for (int k = 0; k < 5; ++k) { xv[2 * k] = lo_f(xd[k]); xv[2 * k + 1] = hi_f(xd[k]); }
      float sm = 0.f;
#pragma unroll
      for (int k = 0; k < 10; ++k) sm += xv[k];
      const float mean = wave_sum(sm) * (1.0f / DM);
      float sq = 0.f;
#pragma unroll
      for (int k = 0; k < 8; ++k) { const float d = xv[k] - mean; sq = fmaf(d, d, sq); }
      const float d8 = lane < 32 ? xv[8] - mean : 0.f, d9 = lane < 32 ? xv[9] - mean : 0.f;
      sq = fmaf(d8, d8, sq); sq = fmaf(d9, d9, sq);
      const float var = wave_sum(sq) * (1.0f / DM);
      mb = rnd(mean);
      den = rnd(sqrtf(rnd(rnd(var) + 1e-5f)));
    }
    if (lane == 0) *reinterpret_cast<f32x2*>(&lds[L_LN]) = f32x2{mb, den};
    set_flag(F_XRDY);
    unsigned xn[3];
#pragma unroll
    for (int k = 0; k < 3; ++k) { const float v = rnd(lo_f(xq[k]) - mb) / den; xn[k] = pk_bf16(v, v); }
    unsigned xr[18];
#pragma unroll
    for (int i = 0; i < 18; ++i) xr[i] = __shfl(xn[i >> 3], (8 * i + (lane >> 3)) & 63);
#pragma unroll
    for (int i = 0; i < 18; ++i) asm volatile("" : "+v"(xr[i]));
    __builtin_amdgcn_sched_barrier(0);
    f32x4 qa = {0.f, 0.f, 0.f, 0.f};
#pragma unroll
    for (int i = 0; i < 18; ++i) { qa = mfma16(and4(xr[i], gm), wqv[i], qa); __builtin_amdgcn_sched_barrier(0); }
    qa = gemv_fin(qa, lane);
    tag = __builtin_amdgcn_readfirstlane(epoch) + 1u;
    {
      const bool up2 = (lane & 8) != 0;
      const int col = (lane & 7) * 8 + 4 * ((lane >> 4) & 1) + (up2 ? 2 : 0);
      __builtin_amdgcn_raw_buffer_store_b128(v4u{__float_as_uint(up2 ? qa[2] : qa[0]), tag, __float_as_uint(up2 ? qa[3] : qa[1]), tag}, rws,
                                             lane < 32 ? WS_QG + ((g * 4 + (s >> 2)) * 256 + (s & 3) * 64 + col) * 8 : OOB, 0, AUX_SC1);
    }
    if (lane == 0) ldu[L_FLAG + F_TAG] = tag;
    for (int i = 0; i < QDELAY; ++i) __builtin_amdgcn_s_sleep(8);
    float q4[4];
    for (unsigned spins = 0;; ++spins) {
      asm volatile("" ::: "memory");
      v4u a[8];
#pragma unroll
      for (int r = 0; r < 4; ++r) {
        a[2 * r] = __builtin_amdgcn_raw_buffer_load_b128(rws, WS_QG + g * 8192 + lane * 32, r * 2048, AUX_SC1);
        a[2 * r + 1] = __builtin_amdgcn_raw_buffer_load_b128(rws, WS_QG + g * 8192 + lane * 32 + 16, r * 2048, AUX_SC1);
      }
      __builtin_amdgcn_sched_barrier(0);
      set_flag(F_POLL);
      __builtin_amdgcn_sched_barrier(0);
      unsigned bad = 0;
#pragma unroll
      for (int j = 0; j < 4; ++j) q4[j] = 0.f;
#pragma unroll
      for (int r = 0; r < 4; ++r) {
        bad |= (a[2 * r].y ^ tag) | (a[2 * r].w ^ tag) | (a[2 * r + 1].y ^ tag) | (a[2 * r + 1].w ^ tag);
        q4[0] += __uint_as_float(a[2 * r].x); q4[1] += __uint_as_float(a[2 * r].z);
        q4[2] += __uint_as_float(a[2 * r + 1].x); q4[3] += __uint_as_float(a[2 * r + 1].z);
      }
      if (__all(bad == 0) || spins > SPIN_CAP) break;
      __builtin_amdgcn_s_sleep(1);
    }
    *reinterpret_cast<v2u*>(&ldu[L_Q + 2 * lane]) = v2u{pk_bf16(q4[0], q4[1]), pk_bf16(q4[2], q4[3])};
    set_flag(F_QRDY);
    __builtin_amdgcn_s_setprio(0);
    float slope = (float)TAU_NOM;
    unsigned hist = 0, prev16 = 0, pstall = 0;
    auto checkpoint = [&](int k, int idx, int span) {
      wait_flag(F_PROG, idx);
      const unsigned t1 = (unsigned)__builtin_amdgcn_s_memrealtime();
      const unsigned stall = __builtin_amdgcn_readfirstlane(*(volatile LAS unsigned*)&ldu[L_FLAG + F_STALL]);
      hist = lane == k ? t1 : hist;
      const unsigned tg = lane <= k ? tag : ~tag;
      __builtin_amdgcn_raw_buffer_store_b128(v4u{hist, tg, hist, tg}, rws, lane < 8 ? WS_CP + b * 128 + lane * 16 : OOB, 0, AUX_SC1);
      wait_flag(F_PROG, idx + CP_D);
      const int src = (lane >> 4) < KGL ? c * 16 + ((s + 5 * c + 4 * (lane >> 4)) & 15) : g * 16 + c;
      const v2u cp = __builtin_amdgcn_raw_buffer_load_b64(rws, WS_CP + src * 128 + k * 16, 0, AUX_SC1);
      float dt = cp.y == tag ? (float)(int)(cp.x - t1) : (float)(CP_D * TAU_NOM / 16);
      const unsigned t16 = (t1 << 4) + (unsigned)(int)(wave_sum(dt) * 0.25f);
      if (k > 0) {
        const float meas = (float)(int)(t16 - prev16 - (stall - pstall)) / (float)span;
        slope = fminf(meas > slope * 1.006f ? meas * 1.01f : fmaxf((float)TAU_NOM, slope * 0.995f), 2.0f * TAU_NOM);
      }
      prev16 = t16; pstall = stall;
      if (lane == 0) *reinterpret_cast<v2u*>(&ldu[L_FLAG + F_TEND]) = v2u{(t16 + (unsigned)(int)(slope * (float)(2 * NT - idx)) - stall) | 1u, (unsigned)(int)slope};
    };
    checkpoint(0, 12, 0); checkpoint(1, 32, 20); checkpoint(2, 56, 24);
    wait_flag(F_P1CNT, NSW);
    {
      const float mw = lds[L_MW + (lane & 7)];
      float mx = fmaxf(mw, dpp<XOR2>(mw)); mx = fmaxf(mx, dpp<ROR4>(mx));
      float l = 0.f;
#pragma unroll 2
      for (int i = 0; i < NSW * NT / 8; ++i) {
        const f32x4 v = *reinterpret_cast<const f32x4*>(&lds[L_SC + i * 256 + lane * 4]);
        l += (__builtin_amdgcn_exp2f(v[0] - mx) + __builtin_amdgcn_exp2f(v[1] - mx)) + (__builtin_amdgcn_exp2f(v[2] - mx) + __builtin_amdgcn_exp2f(v[3] - mx));
      }
      l += dpp<XOR2>(l); l += dpp<ROR4>(l); l += dpp<XOR8>(l); l = sum_x32(sum_x16(l));
      __builtin_amdgcn_raw_buffer_store_b128(v4u{__float_as_uint(mx), tag, __float_as_uint(l), tag}, rws,
                                             lane < 8 ? WS_ML + b * 128 + lane * 16 : OOB, 0, AUX_SC1);
      wait_flag(F_PROG, ML_AT);
      v4u ml;
      for (unsigned spins = 0;; ++spins) {
        asm volatile("" ::: "memory");
        ml = __builtin_amdgcn_raw_buffer_load_b128(rws, WS_ML + (g * 16 + ((lane >> 1) & 15)) * 128 + (lane & 1) * 16, 0, AUX_SC1);
        if (__all(((ml.y ^ tag) | (ml.w ^ tag)) == 0) || spins > SPIN_CAP) break;
        __builtin_amdgcn_s_sleep(32);
      }
      const float mp = __uint_as_float(ml.x);
      float M = fmaxf(mp, dpp<XOR2>(mp)); M = fmaxf(M, dpp<ROR4>(M)); M = fmaxf(M, dpp<XOR8>(M)); M = max_x16(M);
      float L = __builtin_amdgcn_exp2f(mp - M) * __uint_as_float(ml.z);
      asm volatile("" : "+v"(L));
      L += dpp<XOR2>(L); L += dpp<ROR4>(L); L += dpp<XOR8>(L); L = sum_x16(L);
      if (lane < 8) lds[L_MW + lane] = __builtin_amdgcn_exp2f(mw - M) / L;
      set_flag(F_SRDY);
    }
    checkpoint(3, 84, 28);
    if (is2) {
      wait_flag(F_PROG, NEAR_AT);
      wait_flag(F_FDONE, 3);
#pragma unroll
      for (int k = 0; k < 5; ++k) {
        const int t = k * 64 + lane;
        const f32x2 yf = *reinterpret_cast<const f32x2*>(&lds[L_YF + 2 * (t < 288 ? t : 0)]);
        __builtin_amdgcn_raw_buffer_store_b128(v4u{__float_as_uint(yf.x), tag, __float_as_uint(yf.y), tag}, rws,
                                               t < 288 ? WS_PF + ((t >> 3) * NFW + f2) * 128 + (t & 7) * 16 : OOB, 0, AUX_SC1);
      }
    }
    __builtin_amdgcn_s_setprio(3);
    wait_flag(F_ENDCNT, NSW);
    for (int i = 0; i < 3; ++i) __builtin_amdgcn_s_sleep(4);
    {
      int o2[2] = {WS_P1 + (g * 16 + s) * 2048 + lane * 16, WS_P1 + (g * 16 + s) * 2048 + 1024 + lane * 16};
      float pv4[4];
      v4u pl[3][2];
      gather<2>(rws, o2, tag, pv4, pl);
      auto h0 = __builtin_amdgcn_permlane32_swap(__float_as_uint(pv4[0] + pv4[2]), __float_as_uint(pv4[1] + pv4[3]), false, false);
      float v = sum_x16(__uint_as_float(h0[0]) + __uint_as_float(h0[1]));
      v += dpp<XOR8>(v);
      if ((lane & 24) == 0) lds[L_O + 2 * (lane & 7) + (lane >> 5)] = rnd(v);
      set_flag(F_ORDY);
      gather_end<2>(pl);
    }
    return;
  }

  const int nrole = is1 ? 4 : is2 ? 3 : 0;
  const int voff = lane * 16;
  __builtin_amdgcn_sched_barrier(0);
  __builtin_amdgcn_s_barrier();
  __builtin_amdgcn_sched_barrier(0);

  if (w >= NSW && w < NSW + nrole) {
    const int fw = w - NSW;
    v4u fr[24];
    if (is1) {
      const __amdgpu_buffer_rsrc_t rfg = mk_rsrc(wgate, DM * FF * 2);
      const __amdgpu_buffer_rsrc_t rfu = mk_rsrc(wup, DM * FF * 2);
      const int row0 = 288 * (f1 & 1) + 72 * fw + (lane >> 3);
      const int foff = row0 * (FF * 2) + (f1 >> 1) * 128 + (lane & 7) * 16;
      const unsigned xa = __builtin_amdgcn_raw_buffer_load_b16(rx, (288 * (f1 & 1) + 72 * fw + lane) * 2, 0, 0);
      const unsigned xb = __builtin_amdgcn_raw_buffer_load_b16(rx, (row0 + 64) * 2, 0, 0);
#pragma unroll
      for (int i = 0; i < 9; ++i) {
        fr[2 * i] = __builtin_amdgcn_raw_buffer_load_b128(rfg, foff, i * 8 * FF * 2, AUX_NT);
        fr[2 * i + 1] = __builtin_amdgcn_raw_buffer_load_b128(rfu, foff, i * 8 * FF * 2, AUX_NT);
        __builtin_amdgcn_sched_barrier(0); asm volatile("s_waitcnt vmcnt(%0)" :: "n"(KF1)); __builtin_amdgcn_sched_barrier(0);
      }
      wait_flag(F_XRDY, 1);
      const f32x2 ln = *reinterpret_cast<const f32x2*>(&lds[L_LN]);
      const float va = rnd(lo_f(xa) - ln.x) / ln.y, vb = rnd(lo_f(xb) - ln.x) / ln.y;
      const unsigned na = pk_bf16(va, va), nb = pk_bf16(vb, vb);
      f32x4 ag = {0.f, 0.f, 0.f, 0.f}, au = {0.f, 0.f, 0.f, 0.f};
#pragma unroll
      for (int i = 0; i < 9; ++i) {
        const v4u av = and4(i < 8 ? __shfl(na, 8 * i + (lane >> 3)) : nb, gm);
        ag = mfma16(av, fr[2 * i], ag); au = mfma16(av, fr[2 * i + 1], au);
      }
      ag = gemv_fin(ag, lane); au = gemv_fin(au, lane);
      if ((lane & 8) == 0) *reinterpret_cast<f32x4*>(&lds[L_YF + fw * 128 + (lane >> 5) * 64 + (lane & 7) * 8 + 4 * ((lane >> 4) & 1)]) = lane < 32 ? ag : au;
      wait_flag(F_POLL, 1);
      tag = ldu[L_FLAG + F_TAG];
      int old = lds_inc(lds0 + (L_FLAG + F_S1CNT) * 4);
      float gs = 0.f, us = 0.f;
#pragma unroll
      for (int ww = 0; ww < 4; ++ww) { gs += lds[L_YF + ww * 128 + lane]; us += lds[L_YF + ww * 128 + 64 + lane]; }
      asm volatile("s_waitcnt lgkmcnt(0)" : "+v"(old), "+v"(gs), "+v"(us));
      if (__builtin_amdgcn_readfirstlane(old) == 3)
        __builtin_amdgcn_raw_buffer_store_b128(v4u{__float_as_uint(gs), tag, __float_as_uint(us), tag}, rws, WS_GU + (f1 * 64 + lane) * 16, 0, AUX_SC1);
    } else {
      const __amdgpu_buffer_rsrc_t rwd = mk_rsrc(wdown + (size_t)f2 * 64 * DM, 64 * DM * 2);
      const int doff = (lane >> 3) * (DM * 2) + fw * 384 + (lane & 7) * 16;
#pragma unroll
      for (int rg = 0; rg < 8; ++rg) {
#pragma unroll
        for (int k = 0; k < 3; ++k) fr[rg * 3 + k] = __builtin_amdgcn_raw_buffer_load_b128(rwd, doff + k * 128, rg * 8 * DM * 2, AUX_NT);
        __builtin_amdgcn_sched_barrier(0); asm volatile("s_waitcnt vmcnt(%0)" :: "n"(KF2)); __builtin_amdgcn_sched_barrier(0);
      }
      wait_flag(F_QRDY, 1);
      tag = ldu[L_FLAG + F_TAG];
      for (int i = 0; i < 8; ++i) __builtin_amdgcn_s_sleep(8);
      v4u p0, p1;
      for (unsigned spins = 0;; ++spins) {
        asm volatile("" ::: "memory");
        p0 = __builtin_amdgcn_raw_buffer_load_b128(rws, WS_GU + (f2 * 128 + lane) * 16, 0, AUX_SC1);
        p1 = __builtin_amdgcn_raw_buffer_load_b128(rws, WS_GU + (f2 * 128 + lane) * 16, 1024, AUX_SC1);
        if (__all(((p0.y ^ tag) | (p0.w ^ tag) | (p1.y ^ tag) | (p1.w ^ tag)) == 0) || spins > SPIN_CAP) break;
        __builtin_amdgcn_s_sleep(16);
      }
      const float gt = rnd(__uint_as_float(p0.x) + __uint_as_float(p1.x)), up = rnd(__uint_as_float(p0.z) + __uint_as_float(p1.z));
      const float inner = 0.7978845608028654f * (gt + 0.044715f * gt * gt * gt);
      const float hv = rnd(0.5f * gt * (1.0f + tanhf(inner))) * up;
      const unsigned hp = pk_bf16(hv, hv);
      f32x4 ya[3];
#pragma unroll
      for (int k = 0; k < 3; ++k) ya[k] = f32x4{0.f, 0.f, 0.f, 0.f};
#pragma unroll
      for (int rg = 0; rg < 8; ++rg) {
        const v4u av = and4(__shfl(hp, rg * 8 + (lane >> 3)), gm);
#pragma unroll
        for (int k = 0; k < 3; ++k) ya[k] = mfma16(av, fr[rg * 3 + k], ya[k]);
      }
#pragma unroll
      for (int k = 0; k < 3; ++k) {
        ya[k] = gemv_fin(ya[k], lane);
        if (lane < 32 && (lane & 8) == 0) *reinterpret_cast<f32x4*>(&lds[L_YF + fw * 192 + k * 64 + (lane & 7) * 8 + 4 * (lane >> 4)]) = ya[k];
      }
      count_up(F_FDONE);
    }
  } else if (w < NSW) {
    const __amdgpu_buffer_rsrc_t rk = mk_rsrc(kg + (size_t)g * SK * 128, SK * 256);
    const __amdgpu_buffer_rsrc_t rv = mk_rsrc(vg + (size_t)g * SK * 128, SK * 256);
    const int tfirst = ((s & 7) << 21) + ((s >> 3) << 15) + (w << 12);
#define TOFF(t) (tfirst + (((t) >> 1) << 16))
#define TSUB 16384
#define VOFF(t) ((t) < NT ? TOFF(t) + ((t) & 1) * TSUB : OOB)
#define tb(n, i) buf[(n) * TL + (i)]
    v4u buf[4 * TL];
    for (int i = 0; i < KKLATE; ++i) __builtin_amdgcn_s_sleep(8);
#pragma unroll
    for (int i = 0; i < 2 * TL; ++i) buf[i] = __builtin_amdgcn_raw_buffer_load_b128(rk, voff + (i & 3) * 1024, TOFF(0) + (i >> 2) * TSUB, AUX_NT);
    __builtin_amdgcn_sched_barrier(0);
    if (KGATE == 1 || (KGATE == 2 && nrole)) wait_flag(F_POLL, 1);
    if (KGATE == 3) wait_flag(F_XRDY, 1);
#pragma unroll
    for (int i = 0; i < 2 * TL; ++i) buf[2 * TL + i] = __builtin_amdgcn_raw_buffer_load_b128(rk, voff + (i & 3) * 1024, TOFF(2) + (i >> 2) * TSUB, AUX_NT);
    __builtin_amdgcn_sched_barrier(0);
    wait_flag(F_QRDY, 1);
    const int hf = (c >> 2) & 1;
    const v4u qf = *reinterpret_cast<const v4u*>(&ldu[L_Q + hf * 64 + 4 * c]);
    const v4u qs = *reinterpret_cast<const v4u*>(&ldu[L_Q + (hf ^ 1) * 64 + 4 * c]);
    constexpr float LOG2E = 1.4426950408889634f, SCALE = 0.08838834764831845f;
    const bool b0 = (c & 1) != 0, b1 = (c & 2) != 0;
    float* const scw = &lds[L_SC + w * NT * 32 + (lane >> 4) * 8 + (lane & 7)];
    float m = -INFINITY;
    float y[4];
    auto qk1 = [&](const v4u* kt) {
#pragma unroll
      for (int i = 0; i < TL; ++i) {
        float xf = 0.f, xs = 0.f;
#pragma unroll
        for (int j = 0; j < 4; ++j) { xf = dot2(kt[i][j], qf[j], xf); xs = dot2(kt[i][j], qs[j], xs); }
        y[i] = xf + dpp<XOR7>(xs);
      }
      asm volatile("" : "+v"(y[0]), "+v"(y[1]), "+v"(y[2]), "+v"(y[3]));
      __builtin_amdgcn_sched_barrier(0);
    };
    auto qk2 = [&](int t) {
      const float u0 = (b0 ? y[1] : y[0]) + dpp<XOR1>(b0 ? y[0] : y[1]);
      const float u1 = (b0 ? y[3] : y[2]) + dpp<XOR1>(b0 ? y[2] : y[3]);
      float sc = (b1 ? u1 : u0) + dpp<XOR2>(b1 ? u0 : u1);
      sc += dpp<XOR8>(sc);
      const float tl2 = rnd(rnd(sc) * SCALE) * LOG2E;
      m = fmaxf(m, tl2);
      scw[t * 32] = tl2;
    };
#pragma unroll
    for (int n = 0; n < 2; ++n) {
      qk1(&tb(n, 0));
#pragma unroll
      for (int i = 0; i < TL; ++i) tb(n, i) = __builtin_amdgcn_raw_buffer_load_b128(rk, voff + i * 1024, TOFF(4) + n * TSUB, AUX_NT);
      __builtin_amdgcn_sched_barrier(0);
      qk2(n);
    }
    qk1(&tb(2, 0)); qk2(2);
    qk1(&tb(3, 0)); qk2(3);
    unsigned next = (unsigned)__builtin_amdgcn_s_memrealtime() << 4, tau = TAU_NOM;
    unsigned stall = 0, tend = 0;
    int slope = TAU_NOM;
    auto aim = [&](int done) {
      if (w == 0 && lane == 0) *(volatile LAS v2u*)&ldu[L_FLAG + F_PROG] = v2u{(unsigned)done, stall};
      if (done & 2) return;
      const v2u te = *(volatile LAS v2u*)&ldu[L_FLAG + F_TEND];
      const unsigned t0 = __builtin_amdgcn_readfirstlane(te.x);
      if (t0 == 0) return;
      tend = t0; slope = __builtin_amdgcn_readfirstlane(te.y);
      const int v = __builtin_amdgcn_readfirstlane((int)((float)(int)(tend + stall - next) * __builtin_amdgcn_rcpf((float)(2 * NT - done))));
      tau = (unsigned)min(slope + TAU_UP, max(slope - TAU_DN, v));
    };
#pragma unroll 1
    for (int t = 4; t < NT; t += 2) {
      const bool lastk = t + 2 >= NT;
      const __amdgpu_buffer_rsrc_t rn = lastk ? rv : rk;
      const int nxt = TOFF(lastk ? 0 : t + 2);
      if (t <= 12) stall = 0;
      aim(t);
#pragma unroll
      for (int n = 0; n < 2; ++n) {
        qk1(&tb(n, 0));
        pace(next, tau, stall);
#pragma unroll
        for (int i = 0; i < TL; ++i) tb(n, i) = __builtin_amdgcn_raw_buffer_load_b128(rn, voff + i * 1024, nxt + n * TSUB, AUX_NT);
        __builtin_amdgcn_sched_barrier(0);
        qk2(t + n);
      }
    }
    m = fmaxf(m, dpp<XOR1>(m)); m = fmaxf(m, dpp<XOR2>(m));
    m = max_x32(max_x16(m));
    if ((lane & 59) == 0) lds[L_MW + 2 * w + (lane >> 2)] = m;
    count_up(F_P1CNT);
    f32x4 acc = {0.f, 0.f, 0.f, 0.f};
    unsigned amask[4];
    {
      const int jp = (c & 3) + 4 * (c >> 3);
#pragma unroll
      for (int r = 0; r < 4; ++r) amask[r] = (jp >> 1) == r ? ((jp & 1) ? 0xffff0000u : 0x0000ffffu) : 0u;
    }
    auto pv = [&](float sv, const v4u* vt) {
      const float p = __builtin_amdgcn_exp2f(sv - m);
      const unsigned pp = pk_bf16(p, p);
#pragma unroll
      for (int i = 0; i < TL; ++i) {
        const unsigned pb = i == 0 ? dppu<0x00>(pp) : i == 1 ? dppu<0x55>(pp) : i == 2 ? dppu<0xAA>(pp) : dppu<0xFF>(pp);
        acc = mfma16(and4(pb, amask), vt[i], acc);
      }
      asm volatile("" : "+v"(acc));
      __builtin_amdgcn_sched_barrier(0);
    };
    int t = 0;
    auto vrun = [&](int lim) {
#pragma unroll 1
      for (; t < lim; t += 2) {
        aim(NT + t);
        const int nxt = TOFF(t + 2);
        const float s0 = scw[t * 32], s1 = scw[t * 32 + 32];
#pragma unroll
        for (int n = 0; n < 2; ++n) {
          pv(n ? s1 : s0, &tb(n, 0));
          pace(next, tau, stall);
#pragma unroll
          for (int i = 0; i < TL; ++i) tb(n, i) = __builtin_amdgcn_raw_buffer_load_b128(rv, voff + i * 1024, nxt + n * TSUB, AUX_NT);
          __builtin_amdgcn_sched_barrier(0);
        }
      }
    };
    vrun(NT - X_MAX);
    {
      const int late = min(1 << 16, max(0, (int)(next + X_MAX * (slope - TAU_FAST) - tend - stall)));
      vrun(NT - min(X_MAX, X_MIN + 2 * ((late * X_C) >> 15)));
    }
#pragma unroll
    for (int k = 2; k < 4; ++k) {
#pragma unroll
      for (int i = 0; i < TL; ++i) tb(k, i) = __builtin_amdgcn_raw_buffer_load_b128(rv, voff + i * 1024, TOFF(t + 2) + (k & 1) * TSUB, AUX_NT);
      __builtin_amdgcn_sched_barrier(0);
    }
    __builtin_amdgcn_s_setprio(2);
    wait_flag(F_SRDY, 1);
    const float ed = lds[L_MW + 2 * w + ((lane >> 4) & 1)];
    tag = ldu[L_FLAG + F_TAG];
    const int po = WS_P1 + ((g * 16 + (lane >> 3)) * 16 + s) * 128 + (lane & 7) * 16;
#pragma unroll 1
    for (; t + 4 < NT; t += 4) {
      if (w == 0 && lane == 0) *(volatile LAS int*)&ldu[L_FLAG + F_PROG] = NT + t + 2;
#pragma unroll
      for (int k = 0; k < 4; ++k) {
        const int o = VOFF(t + k + 4);
        pv(scw[(t + k) * 32], &tb(k, 0));
#pragma unroll
        for (int i = 0; i < TL; ++i) tb(k, i) = __builtin_amdgcn_raw_buffer_load_b128(rv, voff + i * 1024, o, AUX_NT);
        __builtin_amdgcn_sched_barrier(0);
      }
    }
    {
      if (w == 0 && lane == 0) *(volatile LAS int*)&ldu[L_FLAG + F_PROG] = 126;
      v4u av[4][TL];
#pragma unroll
      for (int k = 0; k < 4; ++k) {
        const float p = __builtin_amdgcn_exp2f((t + k < NT ? scw[(t + k) * 32] : -INFINITY) - m);
        const unsigned pp = pk_bf16(p, p);
        av[k][0] = and4(dppu<0x00>(pp), amask); av[k][1] = and4(dppu<0x55>(pp), amask);
        av[k][2] = and4(dppu<0xAA>(pp), amask); av[k][3] = and4(dppu<0xFF>(pp), amask);
        asm volatile("" : "+v"(av[k][0]), "+v"(av[k][1]), "+v"(av[k][2]), "+v"(av[k][3]));
      }
      __builtin_amdgcn_sched_barrier(0);
#pragma unroll
      for (int k = 0; k < 2; ++k)
#pragma unroll
        for (int i = 0; i < TL; ++i) acc = mfma16(av[k][i], tb(k, i), acc);
      if (t + 2 < NT) {
#pragma unroll
        for (int k = 2; k < 4; ++k)
#pragma unroll
          for (int i = 0; i < TL; ++i) acc = mfma16(av[k][i], tb(k, i), acc);
      }
    }
    *reinterpret_cast<f32x4*>(&lds[L_ACC + w * 256 + ((lane >> 4) & 1) * 128 + 8 * c + 4 * (lane >> 5)]) = acc * ed;
    int old = lds_inc(lds0 + (L_FLAG + F_ENDCNT) * 4);
    f32x2 o0 = {0.f, 0.f}, o1 = {0.f, 0.f};
#pragma unroll
    for (int ww = 0; ww < NSW; ++ww) { o0 += *reinterpret_cast<const f32x2*>(&lds[L_ACC + ww * 256 + 2 * lane]); o1 += *reinterpret_cast<const f32x2*>(&lds[L_ACC + ww * 256 + 128 + 2 * lane]); }
    asm volatile("s_waitcnt lgkmcnt(0)" : "+v"(old), "+v"(o0), "+v"(o1));
    if (__builtin_amdgcn_readfirstlane(old) == NSW - 1) {
      __builtin_amdgcn_raw_buffer_store_b128(v4u{__float_as_uint(o0[0]), tag, __float_as_uint(o0[1]), tag}, rws, po, 0, AUX_SC1);
      __builtin_amdgcn_raw_buffer_store_b128(v4u{__float_as_uint(o1[0]), tag, __float_as_uint(o1[1]), tag}, rws, po, 8 * 2048, AUX_SC1);
    }
  }

  if (w >= NSW) {
    const int t = (w - NSW) * 64 + lane;
    const __amdgpu_buffer_rsrc_t rwo = mk_rsrc(wo + (size_t)b * 16 * DM, 16 * DM * 2);
    if (KTCH && w == NW - 1) {
      if (KTCH == 2) wait_flag(F_POLL, 1);
      if (KTCH == 3) wait_flag(F_PROG, 8 + 2 * (b & 15));
      const unsigned t0 = __builtin_amdgcn_raw_buffer_load_b32(mk_rsrc(vg + (size_t)g * SK * 128, SK * 256), lane < KTV ? ((s & 7) << 21) + lane * 0x1ffffc : OOB, 0, AUX_NT);
      const unsigned t1 = __builtin_amdgcn_raw_buffer_load_b32(rwo, lane == 0 ? 0 : OOB, 0, AUX_NT);
      const unsigned t2 = __builtin_amdgcn_raw_buffer_load_b32(mk_rsrc(out, DM * 2), lane == 0 && b < NRED ? b * 32 : OOB, 0, AUX_SC1);
      const unsigned t3 = __builtin_amdgcn_raw_buffer_load_b32(rws, lane == 0 ? WS_PAD : OOB, 0, AUX_NT);
      asm volatile("" :: "v"(t0), "v"(t1), "v"(t2), "v"(t3));
    }
#pragma unroll 1
    for (unsigned spins = 0; spins < (SPIN_CAP << 5); ++spins) {
      if (__builtin_amdgcn_readfirstlane(*(volatile LAS unsigned*)&ldu[L_FLAG + F_ENDCNT]) >= 1u) break;
      __builtin_amdgcn_s_sleep(2);
    }
    float wf[32];
    {
      unsigned wod[16];
#pragma unroll
      for (int j = 0; j < 16; ++j) wod[j] = __builtin_amdgcn_raw_buffer_load_b32(rwo, t < 288 ? t * 4 : OOB, j * DM * 2, AUX_NT);
#pragma unroll
      for (int j = 0; j < 16; ++j) { wf[2 * j] = lo_f(wod[j]); wf[2 * j + 1] = hi_f(wod[j]); }
#pragma unroll
      for (int j = 0; j < 32; ++j) asm volatile("" : "+v"(wf[j]));
    }
    tag = ldu[L_FLAG + F_TAG];
    const int po = t < 288 ? WS_P2 + ((t >> 3) * NWG + b) * 128 + (t & 7) * 16 : OOB;
    f32x4 o4[4];
#pragma unroll 1
    for (unsigned spins = 0; spins < (SPIN_CAP << 8); ++spins) {
      unsigned f = *(volatile LAS unsigned*)&ldu[L_FLAG + F_ORDY];
      asm volatile("" ::: "memory");
#pragma unroll
      for (int k = 0; k < 4; ++k) o4[k] = *reinterpret_cast<const f32x4*>(&lds[L_O + 4 * k]);
      asm volatile("" : "+v"(o4[0]), "+v"(o4[1]), "+v"(o4[2]), "+v"(o4[3]) :: "memory");
      if (__builtin_amdgcn_readfirstlane(f) != 0u) break;
    }
    f32x2 ya = {0.f, 0.f}, yb = {0.f, 0.f};
#pragma unroll
    for (int j = 0; j < 16; j += 2) {
      ya += o4[j >> 2][j & 3] * f32x2{wf[2 * j], wf[2 * j + 1]};
      yb += o4[j >> 2][(j & 3) + 1] * f32x2{wf[2 * j + 2], wf[2 * j + 3]};
    }
    __builtin_amdgcn_raw_buffer_store_b128(v4u{__float_as_uint(ya.x + yb.x), tag, __float_as_uint(ya.y + yb.y), tag}, rws, po, 0, AUX_SC1);
  }

  if (b < NRED && w < 8) {
    if (w == 0) {
      const unsigned xp = __builtin_amdgcn_raw_buffer_load_b32(rx, lane < 8 ? b * 32 + lane * 4 : OOB, 0, 0);
      float f0, f1v;
      for (unsigned spins = 0;; ++spins) {
        asm volatile("" ::: "memory");
        v4u pf[9];
#pragma unroll
        for (int k = 0; k < 9; ++k) pf[k] = __builtin_amdgcn_raw_buffer_load_b128(rws, WS_PF + b * NFW * 128 + lane * 16, k * 1024, AUX_SC1);
        unsigned bad = 0; f0 = 0.f; f1v = 0.f;
#pragma unroll
        for (int k = 0; k < 9; ++k) { bad |= (pf[k].y ^ tag) | (pf[k].w ^ tag); f0 += __uint_as_float(pf[k].x); f1v += __uint_as_float(pf[k].z); }
        if (__all(bad == 0) || spins > SPIN_CAP) break;
        __builtin_amdgcn_s_sleep(8);
      }
      f0 += dpp<XOR8>(f0); f0 = rnd(sum_x32(sum_x16(f0)));
      f1v += dpp<XOR8>(f1v); f1v = rnd(sum_x32(sum_x16(f1v)));
      if (lane < 8) {
        *reinterpret_cast<f32x2*>(&lds[L_G + lane * 2]) = f32x2{rnd(f0 / (1.0f + __expf(-f0))), rnd(f1v / (1.0f + __expf(-f1v)))};
        ldu[L_G + 16 + lane] = xp;
      }
    }
    if (w < NSW) wait_flag(F_ORDY, 1);
    for (int i = 0; i < 4; ++i) __builtin_amdgcn_s_sleep(4);
    int o4[4];
    float sv[8];
#pragma unroll
    for (int k = 0; k < 4; ++k) o4[k] = WS_P2 + b * NWG * 128 + (w * 64 + lane) * 16 + k * 8192;
    v4u pl[3][4];
    gather<4>(rws, o4, tag, sv, pl);
    float s0 = (sv[0] + sv[2]) + (sv[4] + sv[6]), s1 = (sv[1] + sv[3]) + (sv[5] + sv[7]);
    s0 += dpp<XOR8>(s0); s0 = sum_x32(sum_x16(s0));
    s1 += dpp<XOR8>(s1); s1 = sum_x32(sum_x16(s1));
    if (lane < 8) *reinterpret_cast<f32x2*>(&lds[L_FIN + w * 16 + lane * 2]) = f32x2{s0, s1};
    int old = lds_inc(lds0 + (L_FLAG + F_FINCNT) * 4);
    f32x2 t2 = *reinterpret_cast<const f32x2*>(&lds[L_FIN + lane * 2]);
    f32x2 gv = *reinterpret_cast<const f32x2*>(&lds[L_G + (lane & 7) * 2]);
    unsigned xp = ldu[L_G + 16 + (lane & 7)];
    asm volatile("s_waitcnt lgkmcnt(0)" : "+v"(old), "+v"(t2), "+v"(gv), "+v"(xp));
    if (__builtin_amdgcn_readfirstlane(old) == 7) {
      t2.x += dpp<XOR8>(t2.x); t2.x = sum_x32(sum_x16(t2.x));
      t2.y += dpp<XOR8>(t2.y); t2.y = sum_x32(sum_x16(t2.y));
      const unsigned r = pk_bf16(lo_f(xp) + rnd(rnd(t2.x) * gv.x), hi_f(xp) + rnd(rnd(t2.y) * gv.y));
      __builtin_amdgcn_raw_buffer_store_b32(r, mk_rsrc(out, DM * 2), lane < 8 ? b * 32 + lane * 4 : OOB, 0, 0);
      if (b == 0 && lane == 0) __builtin_amdgcn_raw_buffer_store_b32(tag, rws, WS_EPOCH, 0, AUX_SC1);
    }
    gather_end<4>(pl);
  }
}

}

extern "C" void kernel_launch(void* const* d_in, const int* in_sizes, int n_in, void* d_out, int out_size, void* d_ws, size_t ws_size,
                              hipStream_t stream) {
  (void)in_sizes; (void)n_in; (void)out_size; (void)ws_size;
  fused_decode<<<NWG, NTHR, 0, stream>>>((const unsigned short*)d_in[0], (unsigned char*)d_ws, (const unsigned short*)d_in[3],
                                         (const unsigned short*)d_in[1], (const unsigned short*)d_in[6], (const unsigned short*)d_in[7],
                                         (const unsigned short*)d_in[8], (const unsigned short*)d_in[2], (const unsigned short*)d_in[4],
                                         (unsigned short*)d_out);
}
```

```cpp
#include <hip/hip_runtime.h>
#include <hip/hip_bf16.h>
#include <stdint.h>

namespace {

typedef unsigned v4u __attribute__((ext_vector_type(4)));
typedef unsigned v2u __attribute__((ext_vector_type(2)));
typedef float f32x4 __attribute__((ext_vector_type(4)));
typedef float f32x2 __attribute__((ext_vector_type(2)));
typedef __bf16 bf16x8 __attribute__((ext_vector_type(8)));
typedef __bf16 bf16x2 __attribute__((ext_vector_type(2)));
#define LAS __attribute__((address_space(3)))

constexpr int DM = 576, SK = 65536, FF = 4608, QD = 4096;
constexpr int NWG = 256;
constexpr int NSW = 4;
constexpr int NW = 9;
constexpr int NTHR = (NW + 1) * 64;
constexpr int NRED = 36;
constexpr int NFW = 72;
constexpr int TL = 4;
constexpr int NT = 64;
constexpr int OOB = 0x70000000;

constexpr int TAU_NOM = 874;
constexpr int TAU_DN = 35, TAU_UP = 22, TAU_FAST = 20;
constexpr int BURST = 20, STALL = 100;
constexpr int CP_D = 6;
constexpr int ML_AT = 74;
constexpr int X_MIN = 4, X_MAX = 20, X_C = 68;
constexpr int QDELAY = 7;
constexpr int NEAR_AT = 124;

constexpr int WS_EPOCH = 0;
constexpr int WS_QG = 4096;
constexpr int WS_ML = WS_QG + 16 * 4 * 256 * 8;
constexpr int WS_CP = WS_ML + NWG * 128;
constexpr int WS_P1 = WS_CP + NWG * 128;
constexpr int WS_P2 = WS_P1 + 16 * 16 * 16 * 128;
constexpr int WS_PF = WS_P2 + NRED * NWG * 128;
constexpr int WS_GU = WS_PF + NRED * NFW * 128;
constexpr int WS_PAD = WS_GU + NFW * 2 * 64 * 16;
constexpr int WS_END = WS_PAD + 128;

constexpr int AUX_NT = 2, AUX_SC1 = 16;
constexpr int RSRC_W3 = 0x00020000;
constexpr unsigned SPIN_CAP = 1u << 16;

constexpr int L_FLAG = 0;
enum { F_TAG, F_QRDY, F_XRDY, F_FDONE, F_POLL, F_P1CNT, F_SRDY, F_ORDY, F_PROG, F_STALL, F_TEND, F_SLOPE, F_ENDCNT, F_FINCNT, F_S1CNT };
constexpr int L_MW = 16;
constexpr int L_O = L_MW + 16;
constexpr int L_G = L_O + 16;
constexpr int L_Q = L_G + 32;
constexpr int L_LN = L_Q + 128;
constexpr int L_YF = L_LN + 4;
constexpr int L_FIN = L_YF + 576;
constexpr int L_ACC = L_FIN + 128;
constexpr int L_SC = L_ACC + NSW * 256;
constexpr int L_FLOATS = 24 * 1024;
static_assert(L_SC % 4 == 0 && L_ACC % 4 == 0 && L_Q % 4 == 0 && L_O % 4 == 0 && L_SC + NSW * NT * 32 <= L_FLOATS, "LDS map");
static_assert(16 * NSW * NT * 16 == SK && X_MIN >= 4, "key split");

template <int CTRL> __device__ __forceinline__ float dpp(float x) {
  return __builtin_bit_cast(float, __builtin_amdgcn_mov_dpp(__builtin_bit_cast(int, x), CTRL, 0xf, 0xf, true));
}
template <int CTRL> __device__ __forceinline__ unsigned dppu(unsigned x) {
  return (unsigned)__builtin_amdgcn_mov_dpp((int)x, CTRL, 0xf, 0xf, true);
}
constexpr int XOR1 = 0xB1, XOR2 = 0x4E, XOR7 = 0x141, XOR8 = 0x128, ROR4 = 0x124;
__device__ __forceinline__ float sum_x16(float x) {
  auto s = __builtin_amdgcn_permlane16_swap(__float_as_uint(x), __float_as_uint(x), false, false);
  return __uint_as_float(s[0]) + __uint_as_float(s[1]);
}
__device__ __forceinline__ float sum_x32(float x) {
  auto s = __builtin_amdgcn_permlane32_swap(__float_as_uint(x), __float_as_uint(x), false, false);
  return __uint_as_float(s[0]) + __uint_as_float(s[1]);
}
__device__ __forceinline__ float max_x16(float x) {
  auto s = __builtin_amdgcn_permlane16_swap(__float_as_uint(x), __float_as_uint(x), false, false);
  return fmaxf(__uint_as_float(s[0]), __uint_as_float(s[1]));
}
__device__ __forceinline__ float max_x32(float x) {
  auto s = __builtin_amdgcn_permlane32_swap(__float_as_uint(x), __float_as_uint(x), false, false);
  return fmaxf(__uint_as_float(s[0]), __uint_as_float(s[1]));
}
__device__ __forceinline__ float wave_sum(float x) {
  x += dpp<XOR1>(x); x += dpp<XOR2>(x); x += dpp<ROR4>(x); x += dpp<XOR8>(x);
  return __uint_as_float(__builtin_amdgcn_readfirstlane(__float_as_uint(sum_x32(sum_x16(x)))));
}
__device__ __forceinline__ unsigned pk_bf16(float lo, float hi) {
  bf16x2 v = {(__bf16)lo, (__bf16)hi};
  return __builtin_bit_cast(unsigned, v);
}
__device__ __forceinline__ float rnd(float x) { return (float)(__bf16)x; }
__device__ __forceinline__ float lo_f(unsigned u) { return __uint_as_float(u << 16); }
__device__ __forceinline__ float hi_f(unsigned u) { return __uint_as_float(u & 0xffff0000u); }
__device__ __forceinline__ float dot2(unsigned a, unsigned b, float c) {
  return __builtin_amdgcn_fdot2_f32_bf16(__builtin_bit_cast(bf16x2, a), __builtin_bit_cast(bf16x2, b), c, false);
}
__device__ __forceinline__ __amdgpu_buffer_rsrc_t mk_rsrc(const void* p, int bytes) {
  return __builtin_amdgcn_make_buffer_rsrc((void*)p, 0, bytes, RSRC_W3);
}
__device__ __forceinline__ v4u and4(unsigned x, const unsigned (&m)[4]) { return v4u{x & m[0], x & m[1], x & m[2], x & m[3]}; }
__device__ __forceinline__ f32x4 mfma16(v4u a, v4u bm, f32x4 cacc) {
  return __builtin_amdgcn_mfma_f32_16x16x32_bf16(__builtin_bit_cast(bf16x8, a), __builtin_bit_cast(bf16x8, bm), cacc, 0, 0, 0);
}
__device__ __forceinline__ f32x4 gemv_fin(f32x4 d, int lane) {
  const bool valid = (lane >> 5) == ((lane >> 3) & 1);
#pragma unroll
  for (int r = 0; r < 4; ++r) { float t = valid ? d[r] : 0.f; t += dpp<XOR8>(t); d[r] = sum_x32(t); }
  return d;
}
__device__ __forceinline__ int lds_inc(unsigned addr) {
  int r; unsigned long long save;
  asm volatile("s_mov_b64 %1, exec\n\ts_mov_b64 exec, 1\n\tds_add_rtn_u32 %0, %2, %3\n\ts_mov_b64 exec, %1"
               : "=&v"(r), "=&s"(save) : "v"(addr), "v"(1) : "memory");
  return r;
}
template <int N>
__device__ __forceinline__ void gather(__amdgpu_buffer_rsrc_t rs, int (&off)[N], unsigned tag, float (&val)[2 * N], v4u (&p)[3][N]) {
  auto issue = [&](int j) {
    asm volatile("" ::: "memory");
#pragma unroll
    for (int k = 0; k < N; ++k) p[j][k] = __builtin_amdgcn_raw_buffer_load_b128(rs, off[k], 0, AUX_SC1);
    __builtin_amdgcn_sched_barrier(0);
  };
  auto check = [&](int j) {
    bool done = true;
#pragma unroll
    for (int k = 0; k < N; ++k) {
      const bool ok = off[k] != OOB && ((p[j][k].y ^ tag) | (p[j][k].w ^ tag)) == 0;
      val[2 * k] = ok ? __uint_as_float(p[j][k].x) : val[2 * k]; val[2 * k + 1] = ok ? __uint_as_float(p[j][k].z) : val[2 * k + 1];
      off[k] = ok ? OOB : off[k];
      done = done && off[k] == OOB;
    }
    return __all(done) != 0;
  };
#pragma unroll
  for (int k = 0; k < 2 * N; ++k) val[k] = 0.f;
  issue(0); __builtin_amdgcn_s_sleep(5); issue(1); __builtin_amdgcn_s_sleep(5);
#pragma unroll 1
  for (unsigned spins = 0; spins < SPIN_CAP; ++spins) {
    issue(2); if (check(0)) break;
    issue(0); if (check(1)) break;
    issue(1); if (check(2)) break;
  }
}
template <int N>
__device__ __forceinline__ void gather_end(v4u (&p)[3][N]) {
#pragma unroll
  for (int j = 0; j < 3; ++j)
#pragma unroll
    for (int k = 0; k < N; ++k) asm volatile("" :: "v"(p[j][k]));
}
__device__ __forceinline__ void pace(unsigned& next, unsigned tau, unsigned& stall) {
  int d;
  asm volatile("1:\n\ts_memrealtime vcc\n\ts_waitcnt lgkmcnt(0)\n\ts_lshl_b32 %0, vcc_lo, 4\n\ts_sub_i32 %0, %0, %1\n\ts_cmp_ge_i32 %0, 0\n\ts_cbranch_scc1 2f\n\ts_sleep 1\n\ts_branch 1b\n2:"
               : "=&s"(d) : "s"(next) : "scc", "vcc");
  next += tau + (unsigned)max(0, d - BURST * 16);
  stall += d > STALL * 16 ? d : 0;
}

__global__ void __launch_bounds__(NTHR, 3)
fused_decode(const unsigned short* __restrict__ xg, unsigned char* __restrict__ ws, const unsigned short* __restrict__ wq,
             const unsigned short* __restrict__ kg, const unsigned short* __restrict__ wgate, const unsigned short* __restrict__ wup,
             const unsigned short* __restrict__ wdown, const unsigned short* __restrict__ vg, const unsigned short* __restrict__ wo,
             unsigned short* __restrict__ out) {
  __shared__ __attribute__((aligned(16))) float lds[L_FLOATS];
  asm volatile("" :: "s"(xg), "s"(ws), "s"(wq), "s"(kg), "s"(wgate), "s"(wup), "s"(wdown));
  const int lane = threadIdx.x & 63, pw = __builtin_amdgcn_readfirstlane(threadIdx.x >> 6);
  const int w = pw == 0 ? NW : pw - 1;
  const int b = blockIdx.x, g = b >> 4, s = b & 15;
  const int c = lane & 15;
  const int sr = (s + 3 * g) & 15, ns2 = 5 - (g & 1);
  const bool is1 = sr < 9, is2 = !is1 && sr < 9 + ns2;
  const int f1 = g * 9 + sr, f2 = (g >> 1) * 9 + (g & 1) * 5 + sr - 9;
  unsigned* const ldu = reinterpret_cast<unsigned*>(lds);
  const unsigned lds0 = (unsigned)(uintptr_t)(LAS float*)lds;

  const __amdgpu_buffer_rsrc_t rws = mk_rsrc(ws, WS_END);
  const __amdgpu_buffer_rsrc_t rx = mk_rsrc(xg, DM * 2);

  auto wait_flag = [&](int idx, unsigned want) {
#pragma unroll 1
    for (unsigned spins = 0; spins < (SPIN_CAP << 6); ++spins) {
      if (__builtin_amdgcn_readfirstlane(*(volatile LAS unsigned*)&ldu[L_FLAG + idx]) >= want) break;
      __builtin_amdgcn_s_sleep(1);
    }
    asm volatile("" ::: "memory");
  };
  auto set_flag = [&](int idx) {
    asm volatile("" ::: "memory");
    if (lane == 0) *(volatile LAS unsigned*)&ldu[L_FLAG + idx] = 1u;
  };
  auto count_up = [&](int idx) {
    int r = lds_inc(lds0 + (L_FLAG + idx) * 4);
    asm volatile("s_waitcnt lgkmcnt(0)" : "+v"(r));
  };
  unsigned gm[4];
#pragma unroll
  for (int r = 0; r < 4; ++r) gm[r] = ((lane & 7) >> 1) == r ? ((lane & 1) ? 0xffff0000u : 0x0000ffffu) : 0u;
  unsigned tag;

  if (__builtin_expect(w == NW, 1)) {
    __builtin_amdgcn_s_setprio(3);
    unsigned xd[5], xq[3];
#pragma unroll
    for (int k = 0; k < 5; ++k) xd[k] = __builtin_amdgcn_raw_buffer_load_b32(rx, lane * 4 + k * 256, 0, 0);
#pragma unroll
    for (int k = 0; k < 3; ++k) xq[k] = __builtin_amdgcn_raw_buffer_load_b16(rx, (144 * (s >> 2) + lane + 64 * k) * 2, 0, 0);
    const unsigned epoch = __builtin_amdgcn_raw_buffer_load_b32(rws, lane == 0 ? WS_EPOCH : OOB, 0, AUX_SC1);
    v4u wqv[18];
    {
      const __amdgpu_buffer_rsrc_t rq = mk_rsrc(wq, DM * QD * 2);
      const int o = (144 * (s >> 2) + (lane >> 3)) * (QD * 2) + (g * 4 + (s & 3)) * 128 + (lane & 7) * 16;
#pragma unroll
      for (int i = 0; i < 18; ++i) wqv[i] = __builtin_amdgcn_raw_buffer_load_b128(rq, o, i * 8 * QD * 2, AUX_NT);
    }
    if (lane < 16) ldu[L_FLAG + lane] = 0u;
    asm volatile("s_waitcnt lgkmcnt(0)" ::: "memory");
    __builtin_amdgcn_sched_barrier(0);
    __builtin_amdgcn_s_barrier();
    __builtin_amdgcn_sched_barrier(0);
    float mb, den;
    {
      float xv[10];
#pragma unroll
      for (int k = 0; k < 5; ++k) { xv[2 * k] = lo_f(xd[k]); xv[2 * k + 1] = hi_f(xd[k]); }
      float sm = 0.f;
#pragma unroll
      for (int k = 0; k < 10; ++k) sm += xv[k];
      const float mean = wave_sum(sm) * (1.0f / DM);
      float sq = 0.f;
#pragma unroll
      for (int k = 0; k < 8; ++k) { const float d = xv[k] - mean; sq = fmaf(d, d, sq); }
      const float d8 = lane < 32 ? xv[8] - mean : 0.f, d9 = lane < 32 ? xv[9] - mean : 0.f;
      sq = fmaf(d8, d8, sq); sq = fmaf(d9, d9, sq);
      const float var = wave_sum(sq) * (1.0f / DM);
      mb = rnd(mean);
      den = rnd(sqrtf(rnd(rnd(var) + 1e-5f)));
    }
    if (lane == 0) *reinterpret_cast<f32x2*>(&lds[L_LN]) = f32x2{mb, den};
    set_flag(F_XRDY);
    unsigned xn[3];
#pragma unroll
    for (int k = 0; k < 3; ++k) { const float v = rnd(lo_f(xq[k]) - mb) / den; xn[k] = pk_bf16(v, v); }
    unsigned xr[18];
#pragma unroll
    for (int i = 0; i < 18; ++i) xr[i] = __shfl(xn[i >> 3], (8 * i + (lane >> 3)) & 63);
#pragma unroll
    for (int i = 0; i < 18; ++i) asm volatile("" : "+v"(xr[i]));
    __builtin_amdgcn_sched_barrier(0);
    f32x4 qa = {0.f, 0.f, 0.f, 0.f};
#pragma unroll
    for (int i = 0; i < 18; ++i) { qa = mfma16(and4(xr[i], gm), wqv[i], qa); __builtin_amdgcn_sched_barrier(0); }
    qa = gemv_fin(qa, lane);
    tag = __builtin_amdgcn_readfirstlane(epoch) + 1u;
    {
      const bool up2 = (lane & 8) != 0;
      const int col = (lane & 7) * 8 + 4 * ((lane >> 4) & 1) + (up2 ? 2 : 0);
      __builtin_amdgcn_raw_buffer_store_b128(v4u{__float_as_uint(up2 ? qa[2] : qa[0]), tag, __float_as_uint(up2 ? qa[3] : qa[1]), tag}, rws,
                                             lane < 32 ? WS_QG + ((g * 4 + (s >> 2)) * 256 + (s & 3) * 64 + col) * 8 : OOB, 0, AUX_SC1);
    }
    if (lane == 0) ldu[L_FLAG + F_TAG] = tag;
    for (int i = 0; i < QDELAY; ++i) __builtin_amdgcn_s_sleep(8);
    float q4[4];
    for (unsigned spins = 0;; ++spins) {
      asm volatile("" ::: "memory");
      v4u a[8];
#pragma unroll
      for (int r = 0; r < 4; ++r) {
        a[2 * r] = __builtin_amdgcn_raw_buffer_load_b128(rws, WS_QG + g * 8192 + lane * 32, r * 2048, AUX_SC1);
        a[2 * r + 1] = __builtin_amdgcn_raw_buffer_load_b128(rws, WS_QG + g * 8192 + lane * 32 + 16, r * 2048, AUX_SC1);
      }
      __builtin_amdgcn_sched_barrier(0);
      set_flag(F_POLL);
      __builtin_amdgcn_sched_barrier(0);
      unsigned bad = 0;
#pragma unroll
      for (int j = 0; j < 4; ++j) q4[j] = 0.f;
#pragma unroll
      for (int r = 0; r < 4; ++r) {
        bad |= (a[2 * r].y ^ tag) | (a[2 * r].w ^ tag) | (a[2 * r + 1].y ^ tag) | (a[2 * r + 1].w ^ tag);
        q4[0] += __uint_as_float(a[2 * r].x); q4[1] += __uint_as_float(a[2 * r].z);
        q4[2] += __uint_as_float(a[2 * r + 1].x); q4[3] += __uint_as_float(a[2 * r + 1].z);
      }
      if (__all(bad == 0) || spins > SPIN_CAP) break;
      __builtin_amdgcn_s_sleep(1);
    }
    *reinterpret_cast<v2u*>(&ldu[L_Q + 2 * lane]) = v2u{pk_bf16(q4[0], q4[1]), pk_bf16(q4[2], q4[3])};
    set_flag(F_QRDY);
    __builtin_amdgcn_s_setprio(0);
    float slope = (float)TAU_NOM;
    unsigned hist = 0, prev16 = 0, pstall = 0;
    auto checkpoint = [&](int k, int idx, int span) {
      wait_flag(F_PROG, idx);
      const unsigned t1 = (unsigned)__builtin_amdgcn_s_memrealtime();
      const unsigned stall = __builtin_amdgcn_readfirstlane(*(volatile LAS unsigned*)&ldu[L_FLAG + F_STALL]);
      hist = lane == k ? t1 : hist;
      const unsigned tg = lane <= k ? tag : ~tag;
      __builtin_amdgcn_raw_buffer_store_b128(v4u{hist, tg, hist, tg}, rws, lane < 8 ? WS_CP + b * 128 + lane * 16 : OOB, 0, AUX_SC1);
      wait_flag(F_PROG, idx + CP_D);
      const int src = lane < 32 ? c * 16 + ((s + 5 * c + 4 * (lane >> 4)) & 15) : g * 16 + c;
      const v2u cp = __builtin_amdgcn_raw_buffer_load_b64(rws, WS_CP + src * 128 + k * 16, 0, AUX_SC1);
      float dt = cp.y == tag ? (float)(int)(cp.x - t1) : (float)(CP_D * TAU_NOM / 16);
      const unsigned t16 = (t1 << 4) + (unsigned)(int)(wave_sum(dt) * 0.25f);
      if (k > 0) {
        const float meas = (float)(int)(t16 - prev16 - (stall - pstall)) / (float)span;
        slope = fminf(meas > slope * 1.006f ? meas * 1.01f : fmaxf((float)TAU_NOM, slope * 0.995f), 2.0f * TAU_NOM);
      }
      prev16 = t16; pstall = stall;
      if (lane == 0) *reinterpret_cast<v2u*>(&ldu[L_FLAG + F_TEND]) = v2u{(t16 + (unsigned)(int)(slope * (float)(2 * NT - idx)) - stall) | 1u, (unsigned)(int)slope};
    };
    checkpoint(0, 12, 0); checkpoint(1, 32, 20); checkpoint(2, 56, 24);
    wait_flag(F_P1CNT, NSW);
    {
      const float mw = lds[L_MW + (lane & 7)];
      float mx = fmaxf(mw, dpp<XOR2>(mw)); mx = fmaxf(mx, dpp<ROR4>(mx));
      float l = 0.f;
#pragma unroll 2
      for (int i = 0; i < NSW * NT / 8; ++i) {
        const f32x4 v = *reinterpret_cast<const f32x4*>(&lds[L_SC + i * 256 + lane * 4]);
        l += (__builtin_amdgcn_exp2f(v[0] - mx) + __builtin_amdgcn_exp2f(v[1] - mx)) + (__builtin_amdgcn_exp2f(v[2] - mx) + __builtin_amdgcn_exp2f(v[3] - mx));
      }
      l += dpp<XOR2>(l); l += dpp<ROR4>(l); l += dpp<XOR8>(l); l = sum_x32(sum_x16(l));
      __builtin_amdgcn_raw_buffer_store_b128(v4u{__float_as_uint(mx), tag, __float_as_uint(l), tag}, rws,
                                             lane < 8 ? WS_ML + b * 128 + lane * 16 : OOB, 0, AUX_SC1);
      wait_flag(F_PROG, ML_AT);
      v4u ml;
      for (unsigned spins = 0;; ++spins) {
        asm volatile("" ::: "memory");
        ml = __builtin_amdgcn_raw_buffer_load_b128(rws, WS_ML + (g * 16 + ((lane >> 1) & 15)) * 128 + (lane & 1) * 16, 0, AUX_SC1);
        if (__all(((ml.y ^ tag) | (ml.w ^ tag)) == 0) || spins > SPIN_CAP) break;
        __builtin_amdgcn_s_sleep(32);
      }
      const float mp = __uint_as_float(ml.x);
      float M = fmaxf(mp, dpp<XOR2>(mp)); M = fmaxf(M, dpp<ROR4>(M)); M = fmaxf(M, dpp<XOR8>(M)); M = max_x16(M);
      float L = __builtin_amdgcn_exp2f(mp - M) * __uint_as_float(ml.z);
      asm volatile("" : "+v"(L));
      L += dpp<XOR2>(L); L += dpp<ROR4>(L); L += dpp<XOR8>(L); L = sum_x16(L);
      if (lane < 8) lds[L_MW + lane] = __builtin_amdgcn_exp2f(mw - M) / L;
      set_flag(F_SRDY);
    }
    checkpoint(3, 84, 28);
    if (is2) {
      wait_flag(F_PROG, NEAR_AT);
      wait_flag(F_FDONE, 3);
#pragma unroll
      for (int k = 0; k < 5; ++k) {
        const int t = k * 64 + lane;
        const f32x2 yf = *reinterpret_cast<const f32x2*>(&lds[L_YF + 2 * (t < 288 ? t : 0)]);
        __builtin_amdgcn_raw_buffer_store_b128(v4u{__float_as_uint(yf.x), tag, __float_as_uint(yf.y), tag}, rws,
                                               t < 288 ? WS_PF + ((t >> 3) * NFW + f2) * 128 + (t & 7) * 16 : OOB, 0, AUX_SC1);
      }
    }
    __builtin_amdgcn_s_setprio(3);
    wait_flag(F_ENDCNT, NSW);
    for (int i = 0; i < 3; ++i) __builtin_amdgcn_s_sleep(4);
    {
      int o2[2] = {WS_P1 + (g * 16 + s) * 2048 + lane * 16, WS_P1 + (g * 16 + s) * 2048 + 1024 + lane * 16};
      float pv4[4];
      v4u pl[3][2];
      gather<2>(rws, o2, tag, pv4, pl);
      auto h0 = __builtin_amdgcn_permlane32_swap(__float_as_uint(pv4[0] + pv4[2]), __float_as_uint(pv4[1] + pv4[3]), false, false);
      float v = sum_x16(__uint_as_float(h0[0]) + __uint_as_float(h0[1]));
      v += dpp<XOR8>(v);
      if ((lane & 24) == 0) lds[L_O + 2 * (lane & 7) + (lane >> 5)] = rnd(v);
      set_flag(F_ORDY);
      gather_end<2>(pl);
    }
    return;
  }

  const int nrole = is1 ? 4 : is2 ? 3 : 0;
  const int voff = lane * 16;
  __builtin_amdgcn_sched_barrier(0);
  __builtin_amdgcn_s_barrier();
  __builtin_amdgcn_sched_barrier(0);

  if (w >= NSW && w < NSW + nrole) {
    const int fw = w - NSW;
    v4u fr[24];
    if (is1) {
      const __amdgpu_buffer_rsrc_t rfg = mk_rsrc(wgate, DM * FF * 2);
      const __amdgpu_buffer_rsrc_t rfu = mk_rsrc(wup, DM * FF * 2);
      const int row0 = 288 * (f1 & 1) + 72 * fw + (lane >> 3);
      const int foff = row0 * (FF * 2) + (f1 >> 1) * 128 + (lane & 7) * 16;
      const unsigned xa = __builtin_amdgcn_raw_buffer_load_b16(rx, (288 * (f1 & 1) + 72 * fw + lane) * 2, 0, 0);
      const unsigned xb = __builtin_amdgcn_raw_buffer_load_b16(rx, (row0 + 64) * 2, 0, 0);
#pragma unroll
      for (int i = 0; i < 9; ++i) {
        fr[2 * i] = __builtin_amdgcn_raw_buffer_load_b128(rfg, foff, i * 8 * FF * 2, AUX_NT);
        fr[2 * i + 1] = __builtin_amdgcn_raw_buffer_load_b128(rfu, foff, i * 8 * FF * 2, AUX_NT);
        __builtin_amdgcn_sched_barrier(0); asm volatile("s_waitcnt vmcnt(6)"); __builtin_amdgcn_sched_barrier(0);
      }
      wait_flag(F_XRDY, 1);
      const f32x2 ln = *reinterpret_cast<const f32x2*>(&lds[L_LN]);
      const float va = rnd(lo_f(xa) - ln.x) / ln.y, vb = rnd(lo_f(xb) - ln.x) / ln.y;
      const unsigned na = pk_bf16(va, va), nb = pk_bf16(vb, vb);
      f32x4 ag = {0.f, 0.f, 0.f, 0.f}, au = {0.f, 0.f, 0.f, 0.f};
#pragma unroll
      for (int i = 0; i < 9; ++i) {
        const v4u av = and4(i < 8 ? __shfl(na, 8 * i + (lane >> 3)) : nb, gm);
        ag = mfma16(av, fr[2 * i], ag); au = mfma16(av, fr[2 * i + 1], au);
      }
      ag = gemv_fin(ag, lane); au = gemv_fin(au, lane);
      if ((lane & 8) == 0) *reinterpret_cast<f32x4*>(&lds[L_YF + fw * 128 + (lane >> 5) * 64 + (lane & 7) * 8 + 4 * ((lane >> 4) & 1)]) = lane < 32 ? ag : au;
      wait_flag(F_POLL, 1);
      tag = ldu[L_FLAG + F_TAG];
      int old = lds_inc(lds0 + (L_FLAG + F_S1CNT) * 4);
      float gs = 0.f, us = 0.f;
#pragma unroll
      for (int ww = 0; ww < 4; ++ww) { gs += lds[L_YF + ww * 128 + lane]; us += lds[L_YF + ww * 128 + 64 + lane]; }
      asm volatile("s_waitcnt lgkmcnt(0)" : "+v"(old), "+v"(gs), "+v"(us));
      if (__builtin_amdgcn_readfirstlane(old) == 3)
        __builtin_amdgcn_raw_buffer_store_b128(v4u{__float_as_uint(gs), tag, __float_as_uint(us), tag}, rws, WS_GU + (f1 * 64 + lane) * 16, 0, AUX_SC1);
    } else {
      const __amdgpu_buffer_rsrc_t rwd = mk_rsrc(wdown + (size_t)f2 * 64 * DM, 64 * DM * 2);
      const int doff = (lane >> 3) * (DM * 2) + fw * 384 + (lane & 7) * 16;
#pragma unroll
      for (int rg = 0; rg < 8; ++rg) {
#pragma unroll
        for (int k = 0; k < 3; ++k) fr[rg * 3 + k] = __builtin_amdgcn_raw_buffer_load_b128(rwd, doff + k * 128, rg * 8 * DM * 2, AUX_NT);
        __builtin_amdgcn_sched_barrier(0); asm volatile("s_waitcnt vmcnt(7)"); __builtin_amdgcn_sched_barrier(0);
      }
      wait_flag(F_QRDY, 1);
      tag = ldu[L_FLAG + F_TAG];
      for (int i = 0; i < 8; ++i) __builtin_amdgcn_s_sleep(8);
      v4u p0, p1;
      for (unsigned spins = 0;; ++spins) {
        asm volatile("" ::: "memory");
        p0 = __builtin_amdgcn_raw_buffer_load_b128(rws, WS_GU + (f2 * 128 + lane) * 16, 0, AUX_SC1);
        p1 = __builtin_amdgcn_raw_buffer_load_b128(rws, WS_GU + (f2 * 128 + lane) * 16, 1024, AUX_SC1);
        if (__all(((p0.y ^ tag) | (p0.w ^ tag) | (p1.y ^ tag) | (p1.w ^ tag)) == 0) || spins > SPIN_CAP) break;
        __builtin_amdgcn_s_sleep(16);
      }
      const float gt = rnd(__uint_as_float(p0.x) + __uint_as_float(p1.x)), up = rnd(__uint_as_float(p0.z) + __uint_as_float(p1.z));
      const float inner = 0.7978845608028654f * (gt + 0.044715f * gt * gt * gt);
      const float hv = rnd(0.5f * gt * (1.0f + tanhf(inner))) * up;
      const unsigned hp = pk_bf16(hv, hv);
      f32x4 ya[3];
#pragma unroll
      for (int k = 0; k < 3; ++k) ya[k] = f32x4{0.f, 0.f, 0.f, 0.f};
#pragma unroll
      for (int rg = 0; rg < 8; ++rg) {
        const v4u av = and4(__shfl(hp, rg * 8 + (lane >> 3)), gm);
#pragma unroll
        for (int k = 0; k < 3; ++k) ya[k] = mfma16(av, fr[rg * 3 + k], ya[k]);
      }
#pragma unroll
      for (int k = 0; k < 3; ++k) {
        ya[k] = gemv_fin(ya[k], lane);
        if (lane < 32 && (lane & 8) == 0) *reinterpret_cast<f32x4*>(&lds[L_YF + fw * 192 + k * 64 + (lane & 7) * 8 + 4 * (lane >> 4)]) = ya[k];
      }
      count_up(F_FDONE);
    }
  } else if (w < NSW) {
    const __amdgpu_buffer_rsrc_t rk = mk_rsrc(kg + (size_t)g * SK * 128, SK * 256);
    const __amdgpu_buffer_rsrc_t rv = mk_rsrc(vg + (size_t)g * SK * 128, SK * 256);
    const int tfirst = ((s & 7) << 21) + ((s >> 3) << 15) + (w << 12);
#define TOFF(t) (tfirst + (((t) >> 1) << 16))
#define TSUB 16384
#define VOFF(t) ((t) < NT ? TOFF(t) + ((t) & 1) * TSUB : OOB)
#define tb(n, i) buf[(n) * TL + (i)]
    v4u buf[4 * TL];
#pragma unroll
    for (int i = 0; i < 2 * TL; ++i) buf[i] = __builtin_amdgcn_raw_buffer_load_b128(rk, voff + (i & 3) * 1024, TOFF(0) + (i >> 2) * TSUB, AUX_NT);
    __builtin_amdgcn_sched_barrier(0);
    wait_flag(F_POLL, 1);
#pragma unroll
    for (int i = 0; i < 2 * TL; ++i) buf[2 * TL + i] = __builtin_amdgcn_raw_buffer_load_b128(rk, voff + (i & 3) * 1024, TOFF(2) + (i >> 2) * TSUB, AUX_NT);
    __builtin_amdgcn_sched_barrier(0);
    wait_flag(F_QRDY, 1);
    const int hf = (c >> 2) & 1;
    const v4u qf = *reinterpret_cast<const v4u*>(&ldu[L_Q + hf * 64 + 4 * c]);
    const v4u qs = *reinterpret_cast<const v4u*>(&ldu[L_Q + (hf ^ 1) * 64 + 4 * c]);
    constexpr float LOG2E = 1.4426950408889634f, SCALE = 0.08838834764831845f;
    const bool b0 = (c & 1) != 0, b1 = (c & 2) != 0;
    float* const scw = &lds[L_SC + w * NT * 32 + (lane >> 4) * 8 + (lane & 7)];
    float m = -INFINITY;
    float y[4];
    auto qk1 = [&](const v4u* kt) {
#pragma unroll
      for (int i = 0; i < TL; ++i) {
        float xf = 0.f, xs = 0.f;
#pragma unroll
        for (int j = 0; j < 4; ++j) { xf = dot2(kt[i][j], qf[j], xf); xs = dot2(kt[i][j], qs[j], xs); }
        y[i] = xf + dpp<XOR7>(xs);
      }
      asm volatile("" : "+v"(y[0]), "+v"(y[1]), "+v"(y[2]), "+v"(y[3]));
      __builtin_amdgcn_sched_barrier(0);
    };
    auto qk2 = [&](int t) {
      const float u0 = (b0 ? y[1] : y[0]) + dpp<XOR1>(b0 ? y[0] : y[1]);
      const float u1 = (b0 ? y[3] : y[2]) + dpp<XOR1>(b0 ? y[2] : y[3]);
      float sc = (b1 ? u1 : u0) + dpp<XOR2>(b1 ? u0 : u1);
      sc += dpp<XOR8>(sc);
      const float tl2 = rnd(rnd(sc) * SCALE) * LOG2E;
      m = fmaxf(m, tl2);
      scw[t * 32] = tl2;
    };
#pragma unroll
    for (int n = 0; n < 2; ++n) {
      qk1(&tb(n, 0));
#pragma unroll
      for (int i = 0; i < TL; ++i) tb(n, i) = __builtin_amdgcn_raw_buffer_load_b128(rk, voff + i * 1024, TOFF(4) + n * TSUB, AUX_NT);
      __builtin_amdgcn_sched_barrier(0);
      qk2(n);
    }
    qk1(&tb(2, 0)); qk2(2);
    qk1(&tb(3, 0)); qk2(3);
    unsigned next = (unsigned)__builtin_amdgcn_s_memrealtime() << 4, tau = TAU_NOM;
    unsigned stall = 0, tend = 0;
    int slope = TAU_NOM;
    auto aim = [&](int done) {
      if (w == 0 && lane == 0) *(volatile LAS v2u*)&ldu[L_FLAG + F_PROG] = v2u{(unsigned)done, stall};
      if (done & 2) return;
      const v2u te = *(volatile LAS v2u*)&ldu[L_FLAG + F_TEND];
      const unsigned t0 = __builtin_amdgcn_readfirstlane(te.x);
      if (t0 == 0) return;
      tend = t0; slope = __builtin_amdgcn_readfirstlane(te.y);
      const int v = __builtin_amdgcn_readfirstlane((int)((float)(int)(tend + stall - next) * __builtin_amdgcn_rcpf((float)(2 * NT - done))));
      tau = (unsigned)min(slope + TAU_UP, max(slope - TAU_DN, v));
    };
#pragma unroll 1
    for (int t = 4; t < NT; t += 2) {
      const bool lastk = t + 2 >= NT;
      const __amdgpu_buffer_rsrc_t rn = lastk ? rv : rk;
      const int nxt = TOFF(lastk ? 0 : t + 2);
      if (t <= 12) stall = 0;
      aim(t);
#pragma unroll
      for (int n = 0; n < 2; ++n) {
        qk1(&tb(n, 0));
        pace(next, tau, stall);
#pragma unroll
        for (int i = 0; i < TL; ++i) tb(n, i) = __builtin_amdgcn_raw_buffer_load_b128(rn, voff + i * 1024, nxt + n * TSUB, AUX_NT);
        __builtin_amdgcn_sched_barrier(0);
        qk2(t + n);
      }
    }
    m = fmaxf(m, dpp<XOR1>(m)); m = fmaxf(m, dpp<XOR2>(m));
    m = max_x32(max_x16(m));
    if ((lane & 59) == 0) lds[L_MW + 2 * w + (lane >> 2)] = m;
    count_up(F_P1CNT);
    f32x4 acc = {0.f, 0.f, 0.f, 0.f};
    unsigned amask[4];
    {
      const int jp = (c & 3) + 4 * (c >> 3);
#pragma unroll
      for (int r = 0; r < 4; ++r) amask[r] = (jp >> 1) == r ? ((jp & 1) ? 0xffff0000u : 0x0000ffffu) : 0u;
    }
    auto pv = [&](float sv, const v4u* vt) {
      const float p = __builtin_amdgcn_exp2f(sv - m);
      const unsigned pp = pk_bf16(p, p);
#pragma unroll
      for (int i = 0; i < TL; ++i) {
        const unsigned pb = i == 0 ? dppu<0x00>(pp) : i == 1 ? dppu<0x55>(pp) : i == 2 ? dppu<0xAA>(pp) : dppu<0xFF>(pp);
        acc = mfma16(and4(pb, amask), vt[i], acc);
      }
      asm volatile("" : "+v"(acc));
      __builtin_amdgcn_sched_barrier(0);
    };
    int t = 0;
    auto vrun = [&](int lim) {
#pragma unroll 1
      for (; t < lim; t += 2) {
        aim(NT + t);
        const int nxt = TOFF(t + 2);
        const float s0 = scw[t * 32], s1 = scw[t * 32 + 32];
#pragma unroll
        for (int n = 0; n < 2; ++n) {
          pv(n ? s1 : s0, &tb(n, 0));
          pace(next, tau, stall);
#pragma unroll
          for (int i = 0; i < TL; ++i) tb(n, i) = __builtin_amdgcn_raw_buffer_load_b128(rv, voff + i * 1024, nxt + n * TSUB, AUX_NT);
          __builtin_amdgcn_sched_barrier(0);
        }
      }
    };
    vrun(NT - X_MAX);
    {
      const int late = min(1 << 16, max(0, (int)(next + X_MAX * (slope - TAU_FAST) - tend - stall)));
      vrun(NT - min(X_MAX, X_MIN + 2 * ((late * X_C) >> 15)));
    }
#pragma unroll
    for (int k = 2; k < 4; ++k) {
#pragma unroll
      for (int i = 0; i < TL; ++i) tb(k, i) = __builtin_amdgcn_raw_buffer_load_b128(rv, voff + i * 1024, TOFF(t + 2) + (k & 1) * TSUB, AUX_NT);
      __builtin_amdgcn_sched_barrier(0);
    }
    __builtin_amdgcn_s_setprio(2);
    wait_flag(F_SRDY, 1);
    const float ed = lds[L_MW + 2 * w + ((lane >> 4) & 1)];
    tag = ldu[L_FLAG + F_TAG];
    const int po = WS_P1 + ((g * 16 + (lane >> 3)) * 16 + s) * 128 + (lane & 7) * 16;
#pragma unroll 1
    for (; t + 4 < NT; t += 4) {
      if (w == 0 && lane == 0) *(volatile LAS int*)&ldu[L_FLAG + F_PROG] = NT + t + 2;
#pragma unroll
      for (int k = 0; k < 4; ++k) {
        const int o = VOFF(t + k + 4);
        pv(scw[(t + k) * 32], &tb(k, 0));
#pragma unroll
        for (int i = 0; i < TL; ++i) tb(k, i) = __builtin_amdgcn_raw_buffer_load_b128(rv, voff + i * 1024, o, AUX_NT);
        __builtin_amdgcn_sched_barrier(0);
      }
    }
    {
      if (w == 0 && lane == 0) *(volatile LAS int*)&ldu[L_FLAG + F_PROG] = 126;
      v4u av[4][TL];
#pragma unroll
      for (int k = 0; k < 4; ++k) {
        const float p = __builtin_amdgcn_exp2f((t + k < NT ? scw[(t + k) * 32] : -INFINITY) - m);
        const unsigned pp = pk_bf16(p, p);
        av[k][0] = and4(dppu<0x00>(pp), amask); av[k][1] = and4(dppu<0x55>(pp), amask);
        av[k][2] = and4(dppu<0xAA>(pp), amask); av[k][3] = and4(dppu<0xFF>(pp), amask);
        asm volatile("" : "+v"(av[k][0]), "+v"(av[k][1]), "+v"(av[k][2]), "+v"(av[k][3]));
      }
      __builtin_amdgcn_sched_barrier(0);
#pragma unroll
      for (int k = 0; k < 2; ++k)
#pragma unroll
        for (int i = 0; i < TL; ++i) acc = mfma16(av[k][i], tb(k, i), acc);
      if (t + 2 < NT) {
#pragma unroll
        for (int k = 2; k < 4; ++k)
#pragma unroll
          for (int i = 0; i < TL; ++i) acc = mfma16(av[k][i], tb(k, i), acc);
      }
    }
    *reinterpret_cast<f32x4*>(&lds[L_ACC + w * 256 + ((lane >> 4) & 1) * 128 + 8 * c + 4 * (lane >> 5)]) = acc * ed;
    int old = lds_inc(lds0 + (L_FLAG + F_ENDCNT) * 4);
    f32x2 o0 = {0.f, 0.f}, o1 = {0.f, 0.f};
#pragma unroll
    for (int ww = 0; ww < NSW; ++ww) { o0 += *reinterpret_cast<const f32x2*>(&lds[L_ACC + ww * 256 + 2 * lane]); o1 += *reinterpret_cast<const f32x2*>(&lds[L_ACC + ww * 256 + 128 + 2 * lane]); }
    asm volatile("s_waitcnt lgkmcnt(0)" : "+v"(old), "+v"(o0), "+v"(o1));
    if (__builtin_amdgcn_readfirstlane(old) == NSW - 1) {
      __builtin_amdgcn_raw_buffer_store_b128(v4u{__float_as_uint(o0[0]), tag, __float_as_uint(o0[1]), tag}, rws, po, 0, AUX_SC1);
      __builtin_amdgcn_raw_buffer_store_b128(v4u{__float_as_uint(o1[0]), tag, __float_as_uint(o1[1]), tag}, rws, po, 8 * 2048, AUX_SC1);
    }
  }

  if (w >= NSW) {
    const int t = (w - NSW) * 64 + lane;
    const __amdgpu_buffer_rsrc_t rwo = mk_rsrc(wo + (size_t)b * 16 * DM, 16 * DM * 2);
    if (w == NW - 1) {
      const unsigned t0 = __builtin_amdgcn_raw_buffer_load_b32(mk_rsrc(vg + (size_t)g * SK * 128, SK * 256), lane < 2 ? ((s & 7) << 21) + lane * 0x1ffffc : OOB, 0, AUX_NT);
      const unsigned t1 = __builtin_amdgcn_raw_buffer_load_b32(rwo, lane == 0 ? 0 : OOB, 0, AUX_NT);
      const unsigned t2 = __builtin_amdgcn_raw_buffer_load_b32(mk_rsrc(out, DM * 2), lane == 0 && b < NRED ? b * 32 : OOB, 0, AUX_SC1);
      const unsigned t3 = __builtin_amdgcn_raw_buffer_load_b32(rws, lane == 0 ? WS_PAD : OOB, 0, AUX_NT);
      asm volatile("" :: "v"(t0), "v"(t1), "v"(t2), "v"(t3));
    }
#pragma unroll 1
    for (unsigned spins = 0; spins < (SPIN_CAP << 5); ++spins) {
      if (__builtin_amdgcn_readfirstlane(*(volatile LAS unsigned*)&ldu[L_FLAG + F_ENDCNT]) >= 1u) break;
      __builtin_amdgcn_s_sleep(2);
    }
    float wf[32];
    {
      unsigned wod[16];
#pragma unroll
      for (int j = 0; j < 16; ++j) wod[j] = __builtin_amdgcn_raw_buffer_load_b32(rwo, t < 288 ? t * 4 : OOB, j * DM * 2, AUX_NT);
#pragma unroll
      for (int j = 0; j < 16; ++j) { wf[2 * j] = lo_f(wod[j]); wf[2 * j + 1] = hi_f(wod[j]); }
#pragma unroll
      for (int j = 0; j < 32; ++j) asm volatile("" : "+v"(wf[j]));
    }
    tag = ldu[L_FLAG + F_TAG];
    const int po = t < 288 ? WS_P2 + ((t >> 3) * NWG + b) * 128 + (t & 7) * 16 : OOB;
    f32x4 o4[4];
#pragma unroll 1
    for (unsigned spins = 0; spins < (SPIN_CAP << 8); ++spins) {
      unsigned f = *(volatile LAS unsigned*)&ldu[L_FLAG + F_ORDY];
      asm volatile("" ::: "memory");
#pragma unroll
      for (int k = 0; k < 4; ++k) o4[k] = *reinterpret_cast<const f32x4*>(&lds[L_O + 4 * k]);
      asm volatile("" : "+v"(o4[0]), "+v"(o4[1]), "+v"(o4[2]), "+v"(o4[3]) :: "memory");
      if (__builtin_amdgcn_readfirstlane(f) != 0u) break;
    }
    f32x2 ya = {0.f, 0.f}, yb = {0.f, 0.f};
#pragma unroll
    for (int j = 0; j < 16; j += 2) {
      ya += o4[j >> 2][j & 3] * f32x2{wf[2 * j], wf[2 * j + 1]};
      yb += o4[j >> 2][(j & 3) + 1] * f32x2{wf[2 * j + 2], wf[2 * j + 3]};
    }
    __builtin_amdgcn_raw_buffer_store_b128(v4u{__float_as_uint(ya.x + yb.x), tag, __float_as_uint(ya.y + yb.y), tag}, rws, po, 0, AUX_SC1);
  }

  if (b < NRED && w < 8) {
    if (w == 0) {
      const unsigned xp = __builtin_amdgcn_raw_buffer_load_b32(rx, lane < 8 ? b * 32 + lane * 4 : OOB, 0, 0);
      float f0, f1v;
      for (unsigned spins = 0;; ++spins) {
        asm volatile("" ::: "memory");
        v4u pf[9];
#pragma unroll
        for (int k = 0; k < 9; ++k) pf[k] = __builtin_amdgcn_raw_buffer_load_b128(rws, WS_PF + b * NFW * 128 + lane * 16, k * 1024, AUX_SC1);
        unsigned bad = 0; f0 = 0.f; f1v = 0.f;
#pragma unroll
        for (int k = 0; k < 9; ++k) { bad |= (pf[k].y ^ tag) | (pf[k].w ^ tag); f0 += __uint_as_float(pf[k].x); f1v += __uint_as_float(pf[k].z); }
        if (__all(bad == 0) || spins > SPIN_CAP) break;
        __builtin_amdgcn_s_sleep(8);
      }
      f0 += dpp<XOR8>(f0); f0 = rnd(sum_x32(sum_x16(f0)));
      f1v += dpp<XOR8>(f1v); f1v = rnd(sum_x32(sum_x16(f1v)));
      if (lane < 8) {
        *reinterpret_cast<f32x2*>(&lds[L_G + lane * 2]) = f32x2{rnd(f0 / (1.0f + __expf(-f0))), rnd(f1v / (1.0f + __expf(-f1v)))};
        ldu[L_G + 16 + lane] = xp;
      }
    }
    if (w < NSW) wait_flag(F_ORDY, 1);
    for (int i = 0; i < 4; ++i) __builtin_amdgcn_s_sleep(4);
    int o4[4];
    float sv[8];
#pragma unroll
    for (int k = 0; k < 4; ++k) o4[k] = WS_P2 + b * NWG * 128 + (w * 64 + lane) * 16 + k * 8192;
    v4u pl[3][4];
    gather<4>(rws, o4, tag, sv, pl);
    float s0 = (sv[0] + sv[2]) + (sv[4] + sv[6]), s1 = (sv[1] + sv[3]) + (sv[5] + sv[7]);
    s0 += dpp<XOR8>(s0); s0 = sum_x32(sum_x16(s0));
    s1 += dpp<XOR8>(s1); s1 = sum_x32(sum_x16(s1));
    if (lane < 8) *reinterpret_cast<f32x2*>(&lds[L_FIN + w * 16 + lane * 2]) = f32x2{s0, s1};
    int old = lds_inc(lds0 + (L_FLAG + F_FINCNT) * 4);
    f32x2 t2 = *reinterpret_cast<const f32x2*>(&lds[L_FIN + lane * 2]);
    f32x2 gv = *reinterpret_cast<const f32x2*>(&lds[L_G + (lane & 7) * 2]);
    unsigned xp = ldu[L_G + 16 + (lane & 7)];
    asm volatile("s_waitcnt lgkmcnt(0)" : "+v"(old), "+v"(t2), "+v"(gv), "+v"(xp));
    if (__builtin_amdgcn_readfirstlane(old) == 7) {
      t2.x += dpp<XOR8>(t2.x); t2.x = sum_x32(sum_x16(t2.x));
      t2.y += dpp<XOR8>(t2.y); t2.y = sum_x32(sum_x16(t2.y));
      const unsigned r = pk_bf16(lo_f(xp) + rnd(rnd(t2.x) * gv.x), hi_f(xp) + rnd(rnd(t2.y) * gv.y));
      __builtin_amdgcn_raw_buffer_store_b32(r, mk_rsrc(out, DM * 2), lane < 8 ? b * 32 + lane * 4 : OOB, 0, 0);
      if (b == 0 && lane == 0) __builtin_amdgcn_raw_buffer_store_b32(tag, rws, WS_EPOCH, 0, AUX_SC1);
    }
    gather_end<4>(pl);
  }
}

}

extern "C" void kernel_launch(void* const* d_in, const int* in_sizes, int n_in, void* d_out, int out_size, void* d_ws, size_t ws_size,
                              hipStream_t stream) {
  (void)in_sizes; (void)n_in; (void)out_size; (void)ws_size;
  fused_decode<<<NWG, NTHR, 0, stream>>>((const unsigned short*)d_in[0], (unsigned char*)d_ws, (const unsigned short*)d_in[3],
                                         (const unsigned short*)d_in[1], (const unsigned short*)d_in[6], (const unsigned short*)d_in[7],
                                         (const unsigned short*)d_in[8], (const unsigned short*)d_in[2], (const unsigned short*)d_in[4],
                                         (unsigned short*)d_out);
}
```

```cpp
#include <hip/hip_runtime.h>
#include <hip/hip_bf16.h>
#include <stdint.h>

namespace {

typedef unsigned v4u __attribute__((ext_vector_type(4)));
typedef unsigned v2u __attribute__((ext_vector_type(2)));
typedef float f32x4 __attribute__((ext_vector_type(4)));
typedef float f32x2 __attribute__((ext_vector_type(2)));
typedef __bf16 bf16x8 __attribute__((ext_vector_type(8)));
typedef __bf16 bf16x2 __attribute__((ext_vector_type(2)));
#define LAS __attribute__((address_space(3)))

constexpr int DM = 576, SK = 65536, FF = 4608, QD = 4096;
constexpr int NWG = 256;
constexpr int NSW = 4;
constexpr int NW = 9;
constexpr int NTHR = (NW + 1) * 64;
constexpr int NRED = 36;
constexpr int NFW = 72;
constexpr int TL = 4;
constexpr int NT = 64;
constexpr int OOB = 0x70000000;

constexpr int TAU_NOM = 874;
constexpr int TAU_DN = 35, TAU_UP = 22, TAU_FAST = 20;
constexpr int BURST = 20, STALL = 100;
constexpr int CP_D = 6;
constexpr int ML_AT = 74;
constexpr int X_MIN = 4, X_MAX = 20, X_C = 68;
constexpr int Q_AT = 325, Q_MIN = 60, Q_MAX = 150;
constexpr int NEAR_AT = 124;

constexpr int WS_EPOCH = 0;
constexpr int WS_QG = 4096;
constexpr int WS_ML = WS_QG + 16 * 4 * 256 * 8;
constexpr int WS_CP = WS_ML + NWG * 128;
constexpr int WS_P1 = WS_CP + NWG * 128;
constexpr int WS_P2 = WS_P1 + 16 * 16 * 16 * 128;
constexpr int WS_PF = WS_P2 + NRED * NWG * 128;
constexpr int WS_GU = WS_PF + NRED * NFW * 128;
constexpr int WS_PAD = WS_GU + NFW * 2 * 64 * 16;
constexpr int WS_END = WS_PAD + 128;

constexpr int AUX_NT = 2, AUX_SC1 = 16;
constexpr int RSRC_W3 = 0x00020000;
constexpr unsigned SPIN_CAP = 1u << 16;

constexpr int L_FLAG = 0;
enum { F_TAG, F_QRDY, F_XRDY, F_FDONE, F_POLL, F_P1CNT, F_SRDY, F_ORDY, F_PROG, F_STALL, F_TEND, F_SLOPE, F_ENDCNT, F_FINCNT, F_S1CNT };
constexpr int L_MW = 16;
constexpr int L_O = L_MW + 16;
constexpr int L_G = L_O + 16;
constexpr int L_Q = L_G + 32;
constexpr int L_LN = L_Q + 128;
constexpr int L_YF = L_LN + 4;
constexpr int L_FIN = L_YF + 576;
constexpr int L_ACC = L_FIN + 128;
constexpr int L_SC = L_ACC + NSW * 256;
constexpr int L_FLOATS = 24 * 1024;
static_assert(L_SC % 4 == 0 && L_ACC % 4 == 0 && L_Q % 4 == 0 && L_O % 4 == 0 && L_SC + NSW * NT * 32 <= L_FLOATS, "LDS map");
static_assert(16 * NSW * NT * 16 == SK && X_MIN >= 4, "key split");

template <int CTRL> __device__ __forceinline__ float dpp(float x) {
  return __builtin_bit_cast(float, __builtin_amdgcn_mov_dpp(__builtin_bit_cast(int, x), CTRL, 0xf, 0xf, true));
}
template <int CTRL> __device__ __forceinline__ unsigned dppu(unsigned x) {
  return (unsigned)__builtin_amdgcn_mov_dpp((int)x, CTRL, 0xf, 0xf, true);
}
constexpr int XOR1 = 0xB1, XOR2 = 0x4E, XOR7 = 0x141, XOR8 = 0x128, ROR4 = 0x124;
__device__ __forceinline__ float sum_x16(float x) {
  auto s = __builtin_amdgcn_permlane16_swap(__float_as_uint(x), __float_as_uint(x), false, false);
  return __uint_as_float(s[0]) + __uint_as_float(s[1]);
}
__device__ __forceinline__ float sum_x32(float x) {
  auto s = __builtin_amdgcn_permlane32_swap(__float_as_uint(x), __float_as_uint(x), false, false);
  return __uint_as_float(s[0]) + __uint_as_float(s[1]);
}
__device__ __forceinline__ float max_x16(float x) {
  auto s = __builtin_amdgcn_permlane16_swap(__float_as_uint(x), __float_as_uint(x), false, false);
  return fmaxf(__uint_as_float(s[0]), __uint_as_float(s[1]));
}
__device__ __forceinline__ float max_x32(float x) {
  auto s = __builtin_amdgcn_permlane32_swap(__float_as_uint(x), __float_as_uint(x), false, false);
  return fmaxf(__uint_as_float(s[0]), __uint_as_float(s[1]));
}
__device__ __forceinline__ float wave_sum(float x) {
  x += dpp<XOR1>(x); x += dpp<XOR2>(x); x += dpp<ROR4>(x); x += dpp<XOR8>(x);
  return __uint_as_float(__builtin_amdgcn_readfirstlane(__float_as_uint(sum_x32(sum_x16(x)))));
}
__device__ __forceinline__ unsigned pk_bf16(float lo, float hi) {
  bf16x2 v = {(__bf16)lo, (__bf16)hi};
  return __builtin_bit_cast(unsigned, v);
}
__device__ __forceinline__ float rnd(float x) { return (float)(__bf16)x; }
__device__ __forceinline__ float lo_f(unsigned u) { return __uint_as_float(u << 16); }
__device__ __forceinline__ float hi_f(unsigned u) { return __uint_as_float(u & 0xffff0000u); }
__device__ __forceinline__ float dot2(unsigned a, unsigned b, float c) {
  return __builtin_amdgcn_fdot2_f32_bf16(__builtin_bit_cast(bf16x2, a), __builtin_bit_cast(bf16x2, b), c, false);
}
__device__ __forceinline__ __amdgpu_buffer_rsrc_t mk_rsrc(const void* p, int bytes) {
  return __builtin_amdgcn_make_buffer_rsrc((void*)p, 0, bytes, RSRC_W3);
}
__device__ __forceinline__ v4u and4(unsigned x, const unsigned (&m)[4]) { return v4u{x & m[0], x & m[1], x & m[2], x & m[3]}; }
__device__ __forceinline__ f32x4 mfma16(v4u a, v4u bm, f32x4 cacc) {
  return __builtin_amdgcn_mfma_f32_16x16x32_bf16(__builtin_bit_cast(bf16x8, a), __builtin_bit_cast(bf16x8, bm), cacc, 0, 0, 0);
}
__device__ __forceinline__ f32x4 gemv_fin(f32x4 d, int lane) {
  const bool valid = (lane >> 5) == ((lane >> 3) & 1);
#pragma unroll
  for (int r = 0; r < 4; ++r) { float t = valid ? d[r] : 0.f; t += dpp<XOR8>(t); d[r] = sum_x32(t); }
  return d;
}
__device__ __forceinline__ int lds_inc(unsigned addr) {
  int r; unsigned long long save;
  asm volatile("s_mov_b64 %1, exec\n\ts_mov_b64 exec, 1\n\tds_add_rtn_u32 %0, %2, %3\n\ts_mov_b64 exec, %1"
               : "=&v"(r), "=&s"(save) : "v"(addr), "v"(1) : "memory");
  return r;
}
template <int N>
__device__ __forceinline__ void gather(__amdgpu_buffer_rsrc_t rs, int (&off)[N], unsigned tag, float (&val)[2 * N], v4u (&p)[3][N]) {
  auto issue = [&](int j) {
    asm volatile("" ::: "memory");
#pragma unroll
    for (int k = 0; k < N; ++k) p[j][k] = __builtin_amdgcn_raw_buffer_load_b128(rs, off[k], 0, AUX_SC1);
    __builtin_amdgcn_sched_barrier(0);
  };
  auto check = [&](int j) {
    bool done = true;
#pragma unroll
    for (int k = 0; k < N; ++k) {
      const bool ok = off[k] != OOB && ((p[j][k].y ^ tag) | (p[j][k].w ^ tag)) == 0;
      val[2 * k] = ok ? __uint_as_float(p[j][k].x) : val[2 * k]; val[2 * k + 1] = ok ? __uint_as_float(p[j][k].z) : val[2 * k + 1];
      off[k] = ok ? OOB : off[k];
      done = done && off[k] == OOB;
    }
    return __all(done) != 0;
  };
#pragma unroll
  for (int k = 0; k < 2 * N; ++k) val[k] = 0.f;
  issue(0); __builtin_amdgcn_s_sleep(5); issue(1); __builtin_amdgcn_s_sleep(5);
#pragma unroll 1
  for (unsigned spins = 0; spins < SPIN_CAP; ++spins) {
    issue(2); if (check(0)) break;
    issue(0); if (check(1)) break;
    issue(1); if (check(2)) break;
  }
}
template <int N>
__device__ __forceinline__ void gather_end(v4u (&p)[3][N]) {
#pragma unroll
  for (int j = 0; j < 3; ++j)
#pragma unroll
    for (int k = 0; k < N; ++k) asm volatile("" :: "v"(p[j][k]));
}
__device__ __forceinline__ void pace(unsigned& next, unsigned tau, unsigned& stall) {
  int d;
  asm volatile("1:\n\ts_memrealtime vcc\n\ts_waitcnt lgkmcnt(0)\n\ts_lshl_b32 %0, vcc_lo, 4\n\ts_sub_i32 %0, %0, %1\n\ts_cmp_ge_i32 %0, 0\n\ts_cbranch_scc1 2f\n\ts_sleep 1\n\ts_branch 1b\n2:"
               : "=&s"(d) : "s"(next) : "scc", "vcc");
  next += tau + (unsigned)max(0, d - BURST * 16);
  stall += d > STALL * 16 ? d : 0;
}

__global__ void __launch_bounds__(NTHR, 3)
fused_decode(const unsigned short* __restrict__ xg, unsigned char* __restrict__ ws, const unsigned short* __restrict__ wq,
             const unsigned short* __restrict__ kg, const unsigned short* __restrict__ wgate, const unsigned short* __restrict__ wup,
             const unsigned short* __restrict__ wdown, const unsigned short* __restrict__ vg, const unsigned short* __restrict__ wo,
             unsigned short* __restrict__ out) {
  __shared__ __attribute__((aligned(16))) float lds[L_FLOATS];
  asm volatile("" :: "s"(xg), "s"(ws), "s"(wq), "s"(kg), "s"(wgate), "s"(wup), "s"(wdown));
  const int lane = threadIdx.x & 63, pw = __builtin_amdgcn_readfirstlane(threadIdx.x >> 6);
  const int w = pw == 0 ? NW : pw - 1;
  const int b = blockIdx.x, g = b >> 4, s = b & 15;
  const int c = lane & 15;
  const int sr = (s + 3 * g) & 15, ns2 = 5 - (g & 1);
  const bool is1 = sr < 9, is2 = !is1 && sr < 9 + ns2;
  const int f1 = g * 9 + sr, f2 = (g >> 1) * 9 + (g & 1) * 5 + sr - 9;
  unsigned* const ldu = reinterpret_cast<unsigned*>(lds);
  const unsigned lds0 = (unsigned)(uintptr_t)(LAS float*)lds;

  const __amdgpu_buffer_rsrc_t rws = mk_rsrc(ws, WS_END);
  const __amdgpu_buffer_rsrc_t rx = mk_rsrc(xg, DM * 2);

  auto wait_flag = [&](int idx, unsigned want) {
#pragma unroll 1
    for (unsigned spins = 0; spins < (SPIN_CAP << 6); ++spins) {
      if (__builtin_amdgcn_readfirstlane(*(volatile LAS unsigned*)&ldu[L_FLAG + idx]) >= want) break;
      __builtin_amdgcn_s_sleep(1);
    }
    asm volatile("" ::: "memory");
  };
  auto set_flag = [&](int idx) {
    asm volatile("" ::: "memory");
    if (lane == 0) *(volatile LAS unsigned*)&ldu[L_FLAG + idx] = 1u;
  };
  auto count_up = [&](int idx) {
    int r = lds_inc(lds0 + (L_FLAG + idx) * 4);
    asm volatile("s_waitcnt lgkmcnt(0)" : "+v"(r));
  };
  unsigned gm[4];
#pragma unroll
  for (int r = 0; r < 4; ++r) gm[r] = ((lane & 7) >> 1) == r ? ((lane & 1) ? 0xffff0000u : 0x0000ffffu) : 0u;
  unsigned tag;

  if (__builtin_expect(w == NW, 1)) {
    __builtin_amdgcn_s_setprio(3);
    const unsigned tstart = (unsigned)__builtin_amdgcn_s_memrealtime();
    unsigned xd[5], xq[3];
#pragma unroll
    for (int k = 0; k < 5; ++k) xd[k] = __builtin_amdgcn_raw_buffer_load_b32(rx, lane * 4 + k * 256, 0, 0);
#pragma unroll
    for (int k = 0; k < 3; ++k) xq[k] = __builtin_amdgcn_raw_buffer_load_b16(rx, (144 * (s >> 2) + lane + 64 * k) * 2, 0, 0);
    const unsigned epoch = __builtin_amdgcn_raw_buffer_load_b32(rws, lane == 0 ? WS_EPOCH : OOB, 0, AUX_SC1);
    v4u wqv[18];
    {
      const __amdgpu_buffer_rsrc_t rq = mk_rsrc(wq, DM * QD * 2);
      const int o = (144 * (s >> 2) + (lane >> 3)) * (QD * 2) + (g * 4 + (s & 3)) * 128 + (lane & 7) * 16;
#pragma unroll
      for (int i = 0; i < 18; ++i) wqv[i] = __builtin_amdgcn_raw_buffer_load_b128(rq, o, i * 8 * QD * 2, AUX_NT);
    }
    if (lane < 16) ldu[L_FLAG + lane] = 0u;
    asm volatile("s_waitcnt lgkmcnt(0)" ::: "memory");
    __builtin_amdgcn_sched_barrier(0);
    __builtin_amdgcn_s_barrier();
    __builtin_amdgcn_sched_barrier(0);
    float mb, den;
    {
      float xv[10];
#pragma unroll
      for (int k = 0; k < 5; ++k) { xv[2 * k] = lo_f(xd[k]); xv[2 * k + 1] = hi_f(xd[k]); }
      float sm = 0.f;
#pragma unroll
      for (int k = 0; k < 10; ++k) sm += xv[k];
      const float mean = wave_sum(sm) * (1.0f / DM);
      float sq = 0.f;
#pragma unroll
      for (int k = 0; k < 8; ++k) { const float d = xv[k] - mean; sq = fmaf(d, d, sq); }
      const float d8 = lane < 32 ? xv[8] - mean : 0.f, d9 = lane < 32 ? xv[9] - mean : 0.f;
      sq = fmaf(d8, d8, sq); sq = fmaf(d9, d9, sq);
      const float var = wave_sum(sq) * (1.0f / DM);
      mb = rnd(mean);
      den = rnd(sqrtf(rnd(rnd(var) + 1e-5f)));
    }
    if (lane == 0) *reinterpret_cast<f32x2*>(&lds[L_LN]) = f32x2{mb, den};
    set_flag(F_XRDY);
    unsigned xn[3];
#pragma unroll
    for (int k = 0; k < 3; ++k) { const float v = rnd(lo_f(xq[k]) - mb) / den; xn[k] = pk_bf16(v, v); }
    unsigned xr[18];
#pragma unroll
    for (int i = 0; i < 18; ++i) xr[i] = __shfl(xn[i >> 3], (8 * i + (lane >> 3)) & 63);
#pragma unroll
    for (int i = 0; i < 18; ++i) asm volatile("" : "+v"(xr[i]));
    __builtin_amdgcn_sched_barrier(0);
    f32x4 qa = {0.f, 0.f, 0.f, 0.f};
#pragma unroll
    for (int i = 0; i < 18; ++i) { qa = mfma16(and4(xr[i], gm), wqv[i], qa); __builtin_amdgcn_sched_barrier(0); }
    qa = gemv_fin(qa, lane);
    tag = __builtin_amdgcn_readfirstlane(epoch) + 1u;
    {
      const bool up2 = (lane & 8) != 0;
      const int col = (lane & 7) * 8 + 4 * ((lane >> 4) & 1) + (up2 ? 2 : 0);
      __builtin_amdgcn_raw_buffer_store_b128(v4u{__float_as_uint(up2 ? qa[2] : qa[0]), tag, __float_as_uint(up2 ? qa[3] : qa[1]), tag}, rws,
                                             lane < 32 ? WS_QG + ((g * 4 + (s >> 2)) * 256 + (s & 3) * 64 + col) * 8 : OOB, 0, AUX_SC1);
    }
    if (lane == 0) ldu[L_FLAG + F_TAG] = tag;
    {
      const unsigned tp = (unsigned)__builtin_amdgcn_s_memrealtime();
      const int dl = min(Q_MAX, max(Q_MIN, Q_AT - (int)(tp - tstart)));
#pragma unroll 1
      for (int i = 0; i < 4096 && (int)((unsigned)__builtin_amdgcn_s_memrealtime() - tp) < dl; ++i) __builtin_amdgcn_s_sleep(1);
    }
    float q4[4];
    for (unsigned spins = 0;; ++spins) {
      asm volatile("" ::: "memory");
      v4u a[8];
#pragma unroll
      for (int r = 0; r < 4; ++r) {
        a[2 * r] = __builtin_amdgcn_raw_buffer_load_b128(rws, WS_QG + g * 8192 + lane * 32, r * 2048, AUX_SC1);
        a[2 * r + 1] = __builtin_amdgcn_raw_buffer_load_b128(rws, WS_QG + g * 8192 + lane * 32 + 16, r * 2048, AUX_SC1);
      }
      __builtin_amdgcn_sched_barrier(0);
      set_flag(F_POLL);
      __builtin_amdgcn_sched_barrier(0);
      unsigned bad = 0;
#pragma unroll
      for (int j = 0; j < 4; ++j) q4[j] = 0.f;
#pragma unroll
      for (int r = 0; r < 4; ++r) {
        bad |= (a[2 * r].y ^ tag) | (a[2 * r].w ^ tag) | (a[2 * r + 1].y ^ tag) | (a[2 * r + 1].w ^ tag);
        q4[0] += __uint_as_float(a[2 * r].x); q4[1] += __uint_as_float(a[2 * r].z);
        q4[2] += __uint_as_float(a[2 * r + 1].x); q4[3] += __uint_as_float(a[2 * r + 1].z);
      }
      if (__all(bad == 0) || spins > SPIN_CAP) break;
      __builtin_amdgcn_s_sleep(1);
    }
    *reinterpret_cast<v2u*>(&ldu[L_Q + 2 * lane]) = v2u{pk_bf16(q4[0], q4[1]), pk_bf16(q4[2], q4[3])};
    set_flag(F_QRDY);
    __builtin_amdgcn_s_setprio(0);
    float slope = (float)TAU_NOM;
    unsigned hist = 0, prev16 = 0, pstall = 0;
    auto checkpoint = [&](int k, int idx, int span) {
      wait_flag(F_PROG, idx);
      const unsigned t1 = (unsigned)__builtin_amdgcn_s_memrealtime();
      const unsigned stall = __builtin_amdgcn_readfirstlane(*(volatile LAS unsigned*)&ldu[L_FLAG + F_STALL]);
      hist = lane == k ? t1 : hist;
      const unsigned tg = lane <= k ? tag : ~tag;
      __builtin_amdgcn_raw_buffer_store_b128(v4u{hist, tg, hist, tg}, rws, lane < 8 ? WS_CP + b * 128 + lane * 16 : OOB, 0, AUX_SC1);
      wait_flag(F_PROG, idx + CP_D);
      const int src = lane < 32 ? c * 16 + ((s + 5 * c + 4 * (lane >> 4)) & 15) : g * 16 + c;
      const v2u cp = __builtin_amdgcn_raw_buffer_load_b64(rws, WS_CP + src * 128 + k * 16, 0, AUX_SC1);
      float dt = cp.y == tag ? (float)(int)(cp.x - t1) : (float)(CP_D * TAU_NOM / 16);
      const unsigned t16 = (t1 << 4) + (unsigned)(int)(wave_sum(dt) * 0.25f);
      if (k > 0) {
        const float meas = (float)(int)(t16 - prev16 - (stall - pstall)) / (float)span;
        slope = fminf(meas > slope * 1.006f ? meas * 1.01f : fmaxf((float)TAU_NOM, slope * 0.995f), 2.0f * TAU_NOM);
      }
      prev16 = t16; pstall = stall;
      if (lane == 0) *reinterpret_cast<v2u*>(&ldu[L_FLAG + F_TEND]) = v2u{(t16 + (unsigned)(int)(slope * (float)(2 * NT - idx)) - stall) | 1u, (unsigned)(int)slope};
    };
    checkpoint(0, 12, 0); checkpoint(1, 32, 20); checkpoint(2, 56, 24);
    wait_flag(F_P1CNT, NSW);
    {
      const float mw = lds[L_MW + (lane & 7)];
      float mx = fmaxf(mw, dpp<XOR2>(mw)); mx = fmaxf(mx, dpp<ROR4>(mx));
      float l = 0.f;
#pragma unroll 2
      for (int i = 0; i < NSW * NT / 8; ++i) {
        const f32x4 v = *reinterpret_cast<const f32x4*>(&lds[L_SC + i * 256 + lane * 4]);
        l += (__builtin_amdgcn_exp2f(v[0] - mx) + __builtin_amdgcn_exp2f(v[1] - mx)) + (__builtin_amdgcn_exp2f(v[2] - mx) + __builtin_amdgcn_exp2f(v[3] - mx));
      }
      l += dpp<XOR2>(l); l += dpp<ROR4>(l); l += dpp<XOR8>(l); l = sum_x32(sum_x16(l));
      __builtin_amdgcn_raw_buffer_store_b128(v4u{__float_as_uint(mx), tag, __float_as_uint(l), tag}, rws,
                                             lane < 8 ? WS_ML + b * 128 + lane * 16 : OOB, 0, AUX_SC1);
      wait_flag(F_PROG, ML_AT);
      v4u ml;
      for (unsigned spins = 0;; ++spins) {
        asm volatile("" ::: "memory");
        ml = __builtin_amdgcn_raw_buffer_load_b128(rws, WS_ML + (g * 16 + ((lane >> 1) & 15)) * 128 + (lane & 1) * 16, 0, AUX_SC1);
        if (__all(((ml.y ^ tag) | (ml.w ^ tag)) == 0) || spins > SPIN_CAP) break;
        __builtin_amdgcn_s_sleep(32);
      }
      const float mp = __uint_as_float(ml.x);
      float M = fmaxf(mp, dpp<XOR2>(mp)); M = fmaxf(M, dpp<ROR4>(M)); M = fmaxf(M, dpp<XOR8>(M)); M = max_x16(M);
      float L = __builtin_amdgcn_exp2f(mp - M) * __uint_as_float(ml.z);
      asm volatile("" : "+v"(L));
      L += dpp<XOR2>(L); L += dpp<ROR4>(L); L += dpp<XOR8>(L); L = sum_x16(L);
      if (lane < 8) lds[L_MW + lane] = __builtin_amdgcn_exp2f(mw - M) / L;
      set_flag(F_SRDY);
    }
    checkpoint(3, 84, 28);
    if (is2) {
      wait_flag(F_PROG, NEAR_AT);
      wait_flag(F_FDONE, 3);
#pragma unroll
      for (int k = 0; k < 5; ++k) {
        const int t = k * 64 + lane;
        const f32x2 yf = *reinterpret_cast<const f32x2*>(&lds[L_YF + 2 * (t < 288 ? t : 0)]);
        __builtin_amdgcn_raw_buffer_store_b128(v4u{__float_as_uint(yf.x), tag, __float_as_uint(yf.y), tag}, rws,
                                               t < 288 ? WS_PF + ((t >> 3) * NFW + f2) * 128 + (t & 7) * 16 : OOB, 0, AUX_SC1);
      }
    }
    __builtin_amdgcn_s_setprio(3);
    wait_flag(F_ENDCNT, NSW);
    for (int i = 0; i < 6; ++i) __builtin_amdgcn_s_sleep(4);
    {
      int o2[2] = {WS_P1 + (g * 16 + s) * 2048 + lane * 16, WS_P1 + (g * 16 + s) * 2048 + 1024 + lane * 16};
      float pv4[4];
      v4u pl[3][2];
      gather<2>(rws, o2, tag, pv4, pl);
      auto h0 = __builtin_amdgcn_permlane32_swap(__float_as_uint(pv4[0] + pv4[2]), __float_as_uint(pv4[1] + pv4[3]), false, false);
      float v = sum_x16(__uint_as_float(h0[0]) + __uint_as_float(h0[1]));
      v += dpp<XOR8>(v);
      if ((lane & 24) == 0) lds[L_O + 2 * (lane & 7) + (lane >> 5)] = rnd(v);
      set_flag(F_ORDY);
      gather_end<2>(pl);
    }
    return;
  }

  const int nrole = is1 ? 4 : is2 ? 3 : 0;
  const int voff = lane * 16;
  __builtin_amdgcn_sched_barrier(0);
  __builtin_amdgcn_s_barrier();
  __builtin_amdgcn_sched_barrier(0);

  if (w >= NSW && w < NSW + nrole) {
    const int fw = w - NSW;
    v4u fr[24];
    if (is1) {
      const __amdgpu_buffer_rsrc_t rfg = mk_rsrc(wgate, DM * FF * 2);
      const __amdgpu_buffer_rsrc_t rfu = mk_rsrc(wup, DM * FF * 2);
      const int row0 = 288 * (f1 & 1) + 72 * fw + (lane >> 3);
      const int foff = row0 * (FF * 2) + (f1 >> 1) * 128 + (lane & 7) * 16;
      const unsigned xa = __builtin_amdgcn_raw_buffer_load_b16(rx, (288 * (f1 & 1) + 72 * fw + lane) * 2, 0, 0);
      const unsigned xb = __builtin_amdgcn_raw_buffer_load_b16(rx, (row0 + 64) * 2, 0, 0);
#pragma unroll
      for (int i = 0; i < 9; ++i) {
        fr[2 * i] = __builtin_amdgcn_raw_buffer_load_b128(rfg, foff, i * 8 * FF * 2, AUX_NT);
        fr[2 * i + 1] = __builtin_amdgcn_raw_buffer_load_b128(rfu, foff, i * 8 * FF * 2, AUX_NT);
        __builtin_amdgcn_sched_barrier(0); asm volatile("s_waitcnt vmcnt(6)"); __builtin_amdgcn_sched_barrier(0);
      }
      wait_flag(F_XRDY, 1);
      const f32x2 ln = *reinterpret_cast<const f32x2*>(&lds[L_LN]);
      const float va = rnd(lo_f(xa) - ln.x) / ln.y, vb = rnd(lo_f(xb) - ln.x) / ln.y;
      const unsigned na = pk_bf16(va, va), nb = pk_bf16(vb, vb);
      f32x4 ag = {0.f, 0.f, 0.f, 0.f}, au = {0.f, 0.f, 0.f, 0.f};
#pragma unroll
      for (int i = 0; i < 9; ++i) {
        const v4u av = and4(i < 8 ? __shfl(na, 8 * i + (lane >> 3)) : nb, gm);
        ag = mfma16(av, fr[2 * i], ag); au = mfma16(av, fr[2 * i + 1], au);
      }
      ag = gemv_fin(ag, lane); au = gemv_fin(au, lane);
      if ((lane & 8) == 0) *reinterpret_cast<f32x4*>(&lds[L_YF + fw * 128 + (lane >> 5) * 64 + (lane & 7) * 8 + 4 * ((lane >> 4) & 1)]) = lane < 32 ? ag : au;
      wait_flag(F_POLL, 1);
      tag = ldu[L_FLAG + F_TAG];
      int old = lds_inc(lds0 + (L_FLAG + F_S1CNT) * 4);
      float gs = 0.f, us = 0.f;
#pragma unroll
      for (int ww = 0; ww < 4; ++ww) { gs += lds[L_YF + ww * 128 + lane]; us += lds[L_YF + ww * 128 + 64 + lane]; }
      asm volatile("s_waitcnt lgkmcnt(0)" : "+v"(old), "+v"(gs), "+v"(us));
      if (__builtin_amdgcn_readfirstlane(old) == 3)
        __builtin_amdgcn_raw_buffer_store_b128(v4u{__float_as_uint(gs), tag, __float_as_uint(us), tag}, rws, WS_GU + (f1 * 64 + lane) * 16, 0, AUX_SC1);
    } else {
      const __amdgpu_buffer_rsrc_t rwd = mk_rsrc(wdown + (size_t)f2 * 64 * DM, 64 * DM * 2);
      const int doff = (lane >> 3) * (DM * 2) + fw * 384 + (lane & 7) * 16;
#pragma unroll
      for (int rg = 0; rg < 8; ++rg) {
#pragma unroll
        for (int k = 0; k < 3; ++k) fr[rg * 3 + k] = __builtin_amdgcn_raw_buffer_load_b128(rwd, doff + k * 128, rg * 8 * DM * 2, AUX_NT);
        __builtin_amdgcn_sched_barrier(0); asm volatile("s_waitcnt vmcnt(7)"); __builtin_amdgcn_sched_barrier(0);
      }
      wait_flag(F_QRDY, 1);
      tag = ldu[L_FLAG + F_TAG];
      for (int i = 0; i < 8; ++i) __builtin_amdgcn_s_sleep(8);
      v4u p0, p1;
      for (unsigned spins = 0;; ++spins) {
        asm volatile("" ::: "memory");
        p0 = __builtin_amdgcn_raw_buffer_load_b128(rws, WS_GU + (f2 * 128 + lane) * 16, 0, AUX_SC1);
        p1 = __builtin_amdgcn_raw_buffer_load_b128(rws, WS_GU + (f2 * 128 + lane) * 16, 1024, AUX_SC1);
        if (__all(((p0.y ^ tag) | (p0.w ^ tag) | (p1.y ^ tag) | (p1.w ^ tag)) == 0) || spins > SPIN_CAP) break;
        __builtin_amdgcn_s_sleep(16);
      }
      const float gt = rnd(__uint_as_float(p0.x) + __uint_as_float(p1.x)), up = rnd(__uint_as_float(p0.z) + __uint_as_float(p1.z));
      const float inner = 0.7978845608028654f * (gt + 0.044715f * gt * gt * gt);
      const float hv = rnd(0.5f * gt * (1.0f + tanhf(inner))) * up;
      const unsigned hp = pk_bf16(hv, hv);
      f32x4 ya[3];
#pragma unroll
      for (int k = 0; k < 3; ++k) ya[k] = f32x4{0.f, 0.f, 0.f, 0.f};
#pragma unroll
      for (int rg = 0; rg < 8; ++rg) {
        const v4u av = and4(__shfl(hp, rg * 8 + (lane >> 3)), gm);
#pragma unroll
        for (int k = 0; k < 3; ++k) ya[k] = mfma16(av, fr[rg * 3 + k], ya[k]);
      }
#pragma unroll
      for (int k = 0; k < 3; ++k) {
        ya[k] = gemv_fin(ya[k], lane);
        if (lane < 32 && (lane & 8) == 0) *reinterpret_cast<f32x4*>(&lds[L_YF + fw * 192 + k * 64 + (lane & 7) * 8 + 4 * (lane >> 4)]) = ya[k];
      }
      count_up(F_FDONE);
    }
  } else if (w < NSW) {
    const __amdgpu_buffer_rsrc_t rk = mk_rsrc(kg + (size_t)g * SK * 128, SK * 256);
    const __amdgpu_buffer_rsrc_t rv = mk_rsrc(vg + (size_t)g * SK * 128, SK * 256);
    const int tfirst = ((s & 7) << 21) + ((s >> 3) << 15) + (w << 12);
#define TOFF(t) (tfirst + (((t) >> 1) << 16))
#define TSUB 16384
#define VOFF(t) ((t) < NT ? TOFF(t) + ((t) & 1) * TSUB : OOB)
#define tb(n, i) buf[(n) * TL + (i)]
    v4u buf[4 * TL];
#pragma unroll
    for (int i = 0; i < 2 * TL; ++i) buf[i] = __builtin_amdgcn_raw_buffer_load_b128(rk, voff + (i & 3) * 1024, TOFF(0) + (i >> 2) * TSUB, AUX_NT);
    __builtin_amdgcn_sched_barrier(0);
    wait_flag(F_POLL, 1);
#pragma unroll
    for (int i = 0; i < 2 * TL; ++i) buf[2 * TL + i] = __builtin_amdgcn_raw_buffer_load_b128(rk, voff + (i & 3) * 1024, TOFF(2) + (i >> 2) * TSUB, AUX_NT);
    __builtin_amdgcn_sched_barrier(0);
    wait_flag(F_QRDY, 1);
    const int hf = (c >> 2) & 1;
    const v4u qf = *reinterpret_cast<const v4u*>(&ldu[L_Q + hf * 64 + 4 * c]);
    const v4u qs = *reinterpret_cast<const v4u*>(&ldu[L_Q + (hf ^ 1) * 64 + 4 * c]);
    constexpr float LOG2E = 1.4426950408889634f, SCALE = 0.08838834764831845f;
    const bool b0 = (c & 1) != 0, b1 = (c & 2) != 0;
    float* const scw = &lds[L_SC + w * NT * 32 + (lane >> 4) * 8 + (lane & 7)];
    float m = -INFINITY;
    float y[4];
    auto qk1 = [&](const v4u* kt) {
#pragma unroll
      for (int i = 0; i < TL; ++i) {
        float xf = 0.f, xs = 0.f;
#pragma unroll
        for (int j = 0; j < 4; ++j) { xf = dot2(kt[i][j], qf[j], xf); xs = dot2(kt[i][j], qs[j], xs); }
        y[i] = xf + dpp<XOR7>(xs);
      }
      asm volatile("" : "+v"(y[0]), "+v"(y[1]), "+v"(y[2]), "+v"(y[3]));
      __builtin_amdgcn_sched_barrier(0);
    };
    auto qk2 = [&](int t) {
      const float u0 = (b0 ? y[1] : y[0]) + dpp<XOR1>(b0 ? y[0] : y[1]);
      const float u1 = (b0 ? y[3] : y[2]) + dpp<XOR1>(b0 ? y[2] : y[3]);
      float sc = (b1 ? u1 : u0) + dpp<XOR2>(b1 ? u0 : u1);
      sc += dpp<XOR8>(sc);
      const float tl2 = rnd(rnd(sc) * SCALE) * LOG2E;
      m = fmaxf(m, tl2);
      scw[t * 32] = tl2;
    };
#pragma unroll
    for (int n = 0; n < 2; ++n) {
      qk1(&tb(n, 0));
#pragma unroll
      for (int i = 0; i < TL; ++i) tb(n, i) = __builtin_amdgcn_raw_buffer_load_b128(rk, voff + i * 1024, TOFF(4) + n * TSUB, AUX_NT);
      __builtin_amdgcn_sched_barrier(0);
      qk2(n);
    }
    qk1(&tb(2, 0)); qk2(2);
    qk1(&tb(3, 0)); qk2(3);
    unsigned next = (unsigned)__builtin_amdgcn_s_memrealtime() << 4, tau = TAU_NOM;
    unsigned stall = 0, tend = 0;
    int slope = TAU_NOM;
    auto aim = [&](int done) {
      if (w == 0 && lane == 0) *(volatile LAS v2u*)&ldu[L_FLAG + F_PROG] = v2u{(unsigned)done, stall};
      if (done & 2) return;
      const v2u te = *(volatile LAS v2u*)&ldu[L_FLAG + F_TEND];
      const unsigned t0 = __builtin_amdgcn_readfirstlane(te.x);
      if (t0 == 0) return;
      tend = t0; slope = __builtin_amdgcn_readfirstlane(te.y);
      const int v = __builtin_amdgcn_readfirstlane((int)((float)(int)(tend + stall - next) * __builtin_amdgcn_rcpf((float)(2 * NT - done))));
      tau = (unsigned)min(slope + TAU_UP, max(slope - TAU_DN, v));
    };
#pragma unroll 1
    for (int t = 4; t < NT; t += 2) {
      const bool lastk = t + 2 >= NT;
      const __amdgpu_buffer_rsrc_t rn = lastk ? rv : rk;
      const int nxt = TOFF(lastk ? 0 : t + 2);
      if (t <= 12) stall = 0;
      aim(t);
#pragma unroll
      for (int n = 0; n < 2; ++n) {
        qk1(&tb(n, 0));
        pace(next, tau, stall);
#pragma unroll
        for (int i = 0; i < TL; ++i) tb(n, i) = __builtin_amdgcn_raw_buffer_load_b128(rn, voff + i * 1024, nxt + n * TSUB, AUX_NT);
        __builtin_amdgcn_sched_barrier(0);
        qk2(t + n);
      }
    }
    m = fmaxf(m, dpp<XOR1>(m)); m = fmaxf(m, dpp<XOR2>(m));
    m = max_x32(max_x16(m));
    if ((lane & 59) == 0) lds[L_MW + 2 * w + (lane >> 2)] = m;
    count_up(F_P1CNT);
    f32x4 acc = {0.f, 0.f, 0.f, 0.f};
    unsigned amask[4];
    {
      const int jp = (c & 3) + 4 * (c >> 3);
#pragma unroll
      for (int r = 0; r < 4; ++r) amask[r] = (jp >> 1) == r ? ((jp & 1) ? 0xffff0000u : 0x0000ffffu) : 0u;
    }
    auto pv = [&](float sv, const v4u* vt) {
      const float p = __builtin_amdgcn_exp2f(sv - m);
      const unsigned pp = pk_bf16(p, p);
#pragma unroll
      for (int i = 0; i < TL; ++i) {
        const unsigned pb = i == 0 ? dppu<0x00>(pp) : i == 1 ? dppu<0x55>(pp) : i == 2 ? dppu<0xAA>(pp) : dppu<0xFF>(pp);
        acc = mfma16(and4(pb, amask), vt[i], acc);
      }
      asm volatile("" : "+v"(acc));
      __builtin_amdgcn_sched_barrier(0);
    };
    int t = 0;
    auto vrun = [&](int lim) {
#pragma unroll 1
      for (; t < lim; t += 2) {
        aim(NT + t);
        const int nxt = TOFF(t + 2);
        const float s0 = scw[t * 32], s1 = scw[t * 32 + 32];
#pragma unroll
        for (int n = 0; n < 2; ++n) {
          pv(n ? s1 : s0, &tb(n, 0));
          pace(next, tau, stall);
#pragma unroll
          for (int i = 0; i < TL; ++i) tb(n, i) = __builtin_amdgcn_raw_buffer_load_b128(rv, voff + i * 1024, nxt + n * TSUB, AUX_NT);
          __builtin_amdgcn_sched_barrier(0);
        }
      }
    };
    vrun(NT - X_MAX);
    {
      const int late = min(1 << 16, max(0, (int)(next + X_MAX * (slope - TAU_FAST) - tend - stall)));
      vrun(NT - min(X_MAX, X_MIN + 2 * ((late * X_C) >> 15)));
    }
#pragma unroll
    for (int k = 2; k < 4; ++k) {
#pragma unroll
      for (int i = 0; i < TL; ++i) tb(k, i) = __builtin_amdgcn_raw_buffer_load_b128(rv, voff + i * 1024, TOFF(t + 2) + (k & 1) * TSUB, AUX_NT);
      __builtin_amdgcn_sched_barrier(0);
    }
    __builtin_amdgcn_s_setprio(2);
    wait_flag(F_SRDY, 1);
    const float ed = lds[L_MW + 2 * w + ((lane >> 4) & 1)];
    tag = ldu[L_FLAG + F_TAG];
    const int po = WS_P1 + ((g * 16 + (lane >> 3)) * 16 + s) * 128 + (lane & 7) * 16;
#pragma unroll 1
    for (; t + 4 < NT; t += 4) {
      if (w == 0 && lane == 0) *(volatile LAS int*)&ldu[L_FLAG + F_PROG] = NT + t + 2;
#pragma unroll
      for (int k = 0; k < 4; ++k) {
        const int o = VOFF(t + k + 4);
        pv(scw[(t + k) * 32], &tb(k, 0));
#pragma unroll
        for (int i = 0; i < TL; ++i) tb(k, i) = __builtin_amdgcn_raw_buffer_load_b128(rv, voff + i * 1024, o, AUX_NT);
        __builtin_amdgcn_sched_barrier(0);
      }
    }
    {
      if (w == 0 && lane == 0) *(volatile LAS int*)&ldu[L_FLAG + F_PROG] = 126;
      v4u av[4][TL];
#pragma unroll
      for (int k = 0; k < 4; ++k) {
        const float p = __builtin_amdgcn_exp2f((t + k < NT ? scw[(t + k) * 32] : -INFINITY) - m);
        const unsigned pp = pk_bf16(p, p);
        av[k][0] = and4(dppu<0x00>(pp), amask); av[k][1] = and4(dppu<0x55>(pp), amask);
        av[k][2] = and4(dppu<0xAA>(pp), amask); av[k][3] = and4(dppu<0xFF>(pp), amask);
        asm volatile("" : "+v"(av[k][0]), "+v"(av[k][1]), "+v"(av[k][2]), "+v"(av[k][3]));
      }
      __builtin_amdgcn_sched_barrier(0);
#pragma unroll
      for (int k = 0; k < 2; ++k)
#pragma unroll
        for (int i = 0; i < TL; ++i) acc = mfma16(av[k][i], tb(k, i), acc);
      if (t + 2 < NT) {
#pragma unroll
        for (int k = 2; k < 4; ++k)
#pragma unroll
          for (int i = 0; i < TL; ++i) acc = mfma16(av[k][i], tb(k, i), acc);
      }
    }
    *reinterpret_cast<f32x4*>(&lds[L_ACC + w * 256 + ((lane >> 4) & 1) * 128 + 8 * c + 4 * (lane >> 5)]) = acc * ed;
    int old = lds_inc(lds0 + (L_FLAG + F_ENDCNT) * 4);
    f32x2 o0 = {0.f, 0.f}, o1 = {0.f, 0.f};
#pragma unroll
    for (int ww = 0; ww < NSW; ++ww) { o0 += *reinterpret_cast<const f32x2*>(&lds[L_ACC + ww * 256 + 2 * lane]); o1 += *reinterpret_cast<const f32x2*>(&lds[L_ACC + ww * 256 + 128 + 2 * lane]); }
    asm volatile("s_waitcnt lgkmcnt(0)" : "+v"(old), "+v"(o0), "+v"(o1));
    if (__builtin_amdgcn_readfirstlane(old) == NSW - 1) {
      __builtin_amdgcn_raw_buffer_store_b128(v4u{__float_as_uint(o0[0]), tag, __float_as_uint(o0[1]), tag}, rws, po, 0, AUX_SC1);
      __builtin_amdgcn_raw_buffer_store_b128(v4u{__float_as_uint(o1[0]), tag, __float_as_uint(o1[1]), tag}, rws, po, 8 * 2048, AUX_SC1);
    }
  }

  if (w >= NSW) {
    const int t = (w - NSW) * 64 + lane;
    const __amdgpu_buffer_rsrc_t rwo = mk_rsrc(wo + (size_t)b * 16 * DM, 16 * DM * 2);
    if (w == NW - 1) {
      const unsigned t0 = __builtin_amdgcn_raw_buffer_load_b32(mk_rsrc(vg + (size_t)g * SK * 128, SK * 256), lane < 2 ? ((s & 7) << 21) + lane * 0x1ffffc : OOB, 0, AUX_NT);
      const unsigned t1 = __builtin_amdgcn_raw_buffer_load_b32(rwo, lane == 0 ? 0 : OOB, 0, AUX_NT);
      const unsigned t2 = __builtin_amdgcn_raw_buffer_load_b32(mk_rsrc(out, DM * 2), lane == 0 && b < NRED ? b * 32 : OOB, 0, AUX_SC1);
      const unsigned t3 = __builtin_amdgcn_raw_buffer_load_b32(rws, lane == 0 ? WS_PAD : OOB, 0, AUX_NT);
      asm volatile("" :: "v"(t0), "v"(t1), "v"(t2), "v"(t3));
    }
#pragma unroll 1
    for (unsigned spins = 0; spins < (SPIN_CAP << 5); ++spins) {
      if (__builtin_amdgcn_readfirstlane(*(volatile LAS unsigned*)&ldu[L_FLAG + F_ENDCNT]) >= 1u) break;
      __builtin_amdgcn_s_sleep(2);
    }
    float wf[32];
    {
      unsigned wod[16];
#pragma unroll
      for (int j = 0; j < 16; ++j) wod[j] = __builtin_amdgcn_raw_buffer_load_b32(rwo, t < 288 ? t * 4 : OOB, j * DM * 2, AUX_NT);
#pragma unroll
      for (int j = 0; j < 16; ++j) { wf[2 * j] = lo_f(wod[j]); wf[2 * j + 1] = hi_f(wod[j]); }
#pragma unroll
      for (int j = 0; j < 32; ++j) asm volatile("" : "+v"(wf[j]));
    }
    tag = ldu[L_FLAG + F_TAG];
    const int po = t < 288 ? WS_P2 + ((t >> 3) * NWG + b) * 128 + (t & 7) * 16 : OOB;
    f32x4 o4[4];
#pragma unroll 1
    for (unsigned spins = 0; spins < (SPIN_CAP << 8); ++spins) {
      unsigned f = *(volatile LAS unsigned*)&ldu[L_FLAG + F_ORDY];
      asm volatile("" ::: "memory");
#pragma unroll
      for (int k = 0; k < 4; ++k) o4[k] = *reinterpret_cast<const f32x4*>(&lds[L_O + 4 * k]);
      asm volatile("" : "+v"(o4[0]), "+v"(o4[1]), "+v"(o4[2]), "+v"(o4[3]) :: "memory");
      if (__builtin_amdgcn_readfirstlane(f) != 0u) break;
    }
    f32x2 ya = {0.f, 0.f}, yb = {0.f, 0.f};
#pragma unroll
    for (int j = 0; j < 16; j += 2) {
      ya += o4[j >> 2][j & 3] * f32x2{wf[2 * j], wf[2 * j + 1]};
      yb += o4[j >> 2][(j & 3) + 1] * f32x2{wf[2 * j + 2], wf[2 * j + 3]};
    }
    __builtin_amdgcn_raw_buffer_store_b128(v4u{__float_as_uint(ya.x + yb.x), tag, __float_as_uint(ya.y + yb.y), tag}, rws, po, 0, AUX_SC1);
  }

  if (b < NRED && w < 8) {
    if (w == 0) {
      const unsigned xp = __builtin_amdgcn_raw_buffer_load_b32(rx, lane < 8 ? b * 32 + lane * 4 : OOB, 0, 0);
      float f0, f1v;
      for (unsigned spins = 0;; ++spins) {
        asm volatile("" ::: "memory");
        v4u pf[9];
#pragma unroll
        for (int k = 0; k < 9; ++k) pf[k] = __builtin_amdgcn_raw_buffer_load_b128(rws, WS_PF + b * NFW * 128 + lane * 16, k * 1024, AUX_SC1);
        unsigned bad = 0; f0 = 0.f; f1v = 0.f;
#pragma unroll
        for (int k = 0; k < 9; ++k) { bad |= (pf[k].y ^ tag) | (pf[k].w ^ tag); f0 += __uint_as_float(pf[k].x); f1v += __uint_as_float(pf[k].z); }
        if (__all(bad == 0) || spins > SPIN_CAP) break;
        __builtin_amdgcn_s_sleep(8);
      }
      f0 += dpp<XOR8>(f0); f0 = rnd(sum_x32(sum_x16(f0)));
      f1v += dpp<XOR8>(f1v); f1v = rnd(sum_x32(sum_x16(f1v)));
      if (lane < 8) {
        *reinterpret_cast<f32x2*>(&lds[L_G + lane * 2]) = f32x2{rnd(f0 / (1.0f + __expf(-f0))), rnd(f1v / (1.0f + __expf(-f1v)))};
        ldu[L_G + 16 + lane] = xp;
      }
    }
    if (w < NSW) wait_flag(F_ORDY, 1);
    for (int i = 0; i < 4; ++i) __builtin_amdgcn_s_sleep(4);
    int o4[4];
    float sv[8];
#pragma unroll
    for (int k = 0; k < 4; ++k) o4[k] = WS_P2 + b * NWG * 128 + (w * 64 + lane) * 16 + k * 8192;
    v4u pl[3][4];
    gather<4>(rws, o4, tag, sv, pl);
    float s0 = (sv[0] + sv[2]) + (sv[4] + sv[6]), s1 = (sv[1] + sv[3]) + (sv[5] + sv[7]);
    s0 += dpp<XOR8>(s0); s0 = sum_x32(sum_x16(s0));
    s1 += dpp<XOR8>(s1); s1 = sum_x32(sum_x16(s1));
    if (lane < 8) *reinterpret_cast<f32x2*>(&lds[L_FIN + w * 16 + lane * 2]) = f32x2{s0, s1};
    int old = lds_inc(lds0 + (L_FLAG + F_FINCNT) * 4);
    f32x2 t2 = *reinterpret_cast<const f32x2*>(&lds[L_FIN + lane * 2]);
    f32x2 gv = *reinterpret_cast<const f32x2*>(&lds[L_G + (lane & 7) * 2]);
    unsigned xp = ldu[L_G + 16 + (lane & 7)];
    asm volatile("s_waitcnt lgkmcnt(0)" : "+v"(old), "+v"(t2), "+v"(gv), "+v"(xp));
    if (__builtin_amdgcn_readfirstlane(old) == 7) {
      t2.x += dpp<XOR8>(t2.x); t2.x = sum_x32(sum_x16(t2.x));
      t2.y += dpp<XOR8>(t2.y); t2.y = sum_x32(sum_x16(t2.y));
      const unsigned r = pk_bf16(lo_f(xp) + rnd(rnd(t2.x) * gv.x), hi_f(xp) + rnd(rnd(t2.y) * gv.y));
      __builtin_amdgcn_raw_buffer_store_b32(r, mk_rsrc(out, DM * 2), lane < 8 ? b * 32 + lane * 4 : OOB, 0, 0);
      if (b == 0 && lane == 0) __builtin_amdgcn_raw_buffer_store_b32(tag, rws, WS_EPOCH, 0, AUX_SC1);
    }
    gather_end<4>(pl);
  }
}

}

extern "C" void kernel_launch(void* const* d_in, const int* in_sizes, int n_in, void* d_out, int out_size, void* d_ws, size_t ws_size,
                              hipStream_t stream) {
  (void)in_sizes; (void)n_in; (void)out_size; (void)ws_size;
  fused_decode<<<NWG, NTHR, 0, stream>>>((const unsigned short*)d_in[0], (unsigned char*)d_ws, (const unsigned short*)d_in[3],
                                         (const unsigned short*)d_in[1], (const unsigned short*)d_in[6], (const unsigned short*)d_in[7],
                                         (const unsigned short*)d_in[8], (const unsigned short*)d_in[2], (const unsigned short*)d_in[4],
                                         (unsigned short*)d_out);
}
```
